```python
import torch

def _softmax(x, axis=-1):
    return torch.softmax(x, dim=axis)
def _gelu(x):
    return torch.nn.functional.gelu(x, approximate="tanh")
def _silu(x):
    return torch.nn.functional.silu(x)
def _sigmoid(x):
    return torch.sigmoid(x)
def _layernorm(x, axis=-1, eps=1e-5):
    m = x.mean(dim=axis, keepdim=True)
    v = x.var(dim=axis, keepdim=True, unbiased=False)
    return (x - m) / torch.sqrt(v + eps)
def _scatter_add(tgt, idx, src, axis=0):
    return tgt.clone().index_add_(axis, idx, src)
def _segment_sum(src, seg, n):
    out = torch.zeros((n,) + src.shape[1:], dtype=src.dtype)
    return out.index_add_(0, seg, src)
def _pad(x, pw):
    flat = tuple(v for pair in reversed(pw) for v in pair)
    return torch.nn.functional.pad(x, flat)

kernel_name = 'concat_heads__mha_block__linear_kernel_hedgehog__prompt'

def setup_inputs():

    _w = (lambda a: (lambda t: t.to(torch.bfloat16) if t.is_floating_point() else t)(torch.as_tensor(a)))
    def _A_setup():
        B, S, D, H = 1, 2048, 448, 16
        return {
            "x": _w((lambda *s: torch.randn(*s, dtype=torch.bfloat16) * 0.4)(B, S, D)),
            "wqkv": _w((lambda *s: torch.randn(*s, dtype=torch.bfloat16) * 0.4)(D, 3 * D)),
            "wo": _w((lambda *s: torch.randn(*s, dtype=torch.bfloat16) * 0.05)(D, D)),
        }
    _A_in = _A_setup()

    def _B_setup():
        B, S, D, H = 1, 2048, 448, 16
        Dh = D // H
        return {
            "x": _w((lambda *s: torch.randn(*s, dtype=torch.bfloat16) * 0.8)(B, S, D)),
            "wqkv": _w((lambda *s: torch.randn(*s, dtype=torch.bfloat16) * 1.6)(D, 3 * D)),
            "w_phi": _w((lambda *s: torch.randn(*s, dtype=torch.bfloat16) * 1.6)(H, Dh, Dh) * (Dh ** -0.5)),
            "wo": _w((lambda *s: torch.randn(*s, dtype=torch.bfloat16) * 0.1)(D, D)),
        }
    _B_in = _B_setup()

    return {
        "A_x": _A_in["x"],
        "A_wqkv": _A_in["wqkv"],
        "A_wo": _A_in["wo"],
        "B_x": _B_in["x"],
        "B_wqkv": _B_in["wqkv"],
        "B_w_phi": _B_in["w_phi"],
        "B_wo": _B_in["wo"],
    }

def reference(A_x, A_wqkv, A_wo, B_x, B_wqkv, B_w_phi, B_wo):
    def _A_ref(x, wqkv, wo):
        B, S, D = x.shape
        H = 16
        Dh = D // H
        qkv = (lambda x, *d: x.permute(*d))((x @ wqkv).reshape(B, S, 3, H, Dh), 2, 0, 3, 1, 4)
        q, k, v = qkv[0], qkv[1], qkv[2]
        scores = torch.einsum("bhqd,bhkd->bhqk", q, k) * (Dh ** -0.5)
        p = _softmax(scores, axis=-1)
        o = torch.einsum("bhqk,bhkd->bhqd", p, v)
        return (lambda x, *d: x.permute(*d))(o, 0, 2, 1, 3).reshape(B, S, D) @ wo

    def _B_ref(x, wqkv, w_phi, wo):
        B, S, D = x.shape
        H = 16; Dh = D // H
        qkv = (lambda x, *d: x.permute(*d))((x @ wqkv).reshape(B, S, 3, H, Dh), 2, 0, 3, 1, 4)
        q, k, v = qkv[0], qkv[1], qkv[2]
        pq = torch.einsum("bhsd,hdf->bhsf", q, w_phi)
        pk = torch.einsum("bhsd,hdf->bhsf", k, w_phi)
        phi_q = torch.concatenate([_softmax(pq, axis=-1), _softmax(-pq, axis=-1)], axis=-1)
        phi_k = torch.concatenate([_softmax(pk, axis=-1), _softmax(-pk, axis=-1)], axis=-1)
        kv = torch.einsum("bhsf,bhsd->bhfd", phi_k, v)
        z  = torch.einsum("bhsf->bhf", phi_k)
        num = torch.einsum("bhqf,bhfd->bhqd", phi_q, kv)
        den = torch.einsum("bhqf,bhf->bhq", phi_q, z)[..., None] + 1e-6
        o = num / den
        return (lambda x, *d: x.permute(*d))(o, 0, 2, 1, 3).reshape(B, S, D) @ wo

    a = _A_ref(A_x, A_wqkv, A_wo)
    b = _B_ref(B_x, B_wqkv, B_w_phi, B_wo)
    return torch.concatenate([a, b], axis=-1)
```

```cpp
#include <hip/hip_runtime.h>
#include <hip/hip_bf16.h>
#include <math.h>
typedef __hip_bfloat16 bf16;
constexpr int S = 2048, D = 448, H = 16, DH = 28, N3 = 1344;
__device__ __forceinline__ float b2f(bf16 x) { return __bfloat162float(x); }
__device__ __forceinline__ float rb(float x) { return __bfloat162float(__float2bfloat16(x)); }

__global__ void k_gemm(const bf16* x, const bf16* w, float* out, int M, int N, int K) {
    int n = blockIdx.x * blockDim.x + threadIdx.x, m = blockIdx.y;
    if (n >= N) return;
    float acc = 0.f;
    for (int k = 0; k < K; ++k) acc += b2f(x[m * K + k]) * b2f(w[k * N + n]);
    out[m * N + n] = rb(acc);
}
__global__ void k_attn(const float* qkv, float* o) {
    int q = blockIdx.x * blockDim.x + threadIdx.x, h = blockIdx.y;
    float qv[DH];
    for (int d = 0; d < DH; ++d) qv[d] = qkv[q * N3 + h * DH + d];
    float m = -INFINITY, l = 0.f, acc[DH];
    for (int d = 0; d < DH; ++d) acc[d] = 0.f;
    const float sc = 0.1889822365046136f;
    for (int k = 0; k < S; ++k) {
        float s = 0.f;
        for (int d = 0; d < DH; ++d) s += qv[d] * qkv[k * N3 + D + h * DH + d];
        s *= sc;
        float mn = fmaxf(m, s), a = __expf(m - mn), p = __expf(s - mn);
        l = l * a + p;
        for (int d = 0; d < DH; ++d) acc[d] = acc[d] * a + p * qkv[k * N3 + 2 * D + h * DH + d];
        m = mn;
    }
    for (int d = 0; d < DH; ++d) o[q * D + h * DH + d] = rb(acc[d] / l);
}
__global__ void k_phi(const float* qkv, const bf16* wphi, float* phi) {
    int s = blockIdx.x * blockDim.x + threadIdx.x, h = blockIdx.y, which = blockIdx.z;
    float p[DH];
    for (int f = 0; f < DH; ++f) {
        float a = 0.f;
        for (int d = 0; d < DH; ++d) a += qkv[s * N3 + which * D + h * DH + d] * b2f(wphi[(h * DH + d) * DH + f]);
        p[f] = rb(a);
    }
    float mx = -INFINITY, mn = INFINITY;
    for (int f = 0; f < DH; ++f) { mx = fmaxf(mx, p[f]); mn = fminf(mn, p[f]); }
    float s1 = 0.f, s2 = 0.f;
    for (int f = 0; f < DH; ++f) { s1 += __expf(p[f] - mx); s2 += __expf(mn - p[f]); }
    float* dst = phi + (((size_t)which * H + h) * S + s) * 56;
    for (int f = 0; f < DH; ++f) { dst[f] = rb(__expf(p[f] - mx) / s1); dst[28 + f] = rb(__expf(mn - p[f]) / s2); }
}
__global__ void k_kv(const float* qkv, const float* phi, float* kv) {
    int idx = threadIdx.x + blockIdx.x * blockDim.x, h = blockIdx.y;
    if (idx >= 56 * 29) return;
    int f = idx / 29, d = idx % 29;
    const float* pk = phi + ((size_t)H + h) * S * 56;
    float a = 0.f;
    for (int s = 0; s < S; ++s) a += pk[s * 56 + f] * (d < 28 ? qkv[s * N3 + 2 * D + h * DH + d] : 1.f);
    kv[(h * 56 + f) * 29 + d] = rb(a);
}
__global__ void k_lin(const float* phi, const float* kv, float* o) {
    int s = blockIdx.x * blockDim.x + threadIdx.x, h = blockIdx.y;
    const float* pq = phi + ((size_t)h * S + s) * 56;
    float den = 0.f;
    for (int f = 0; f < 56; ++f) den += pq[f] * kv[(h * 56 + f) * 29 + 28];
    den = rb(rb(den) + 1e-6f);
    for (int d = 0; d < DH; ++d) {
        float a = 0.f;
        for (int f = 0; f < 56; ++f) a += pq[f] * kv[(h * 56 + f) * 29 + d];
        o[s * D + h * DH + d] = rb(rb(a) / den);
    }
}
__global__ void k_out(const float* o, const bf16* wo, bf16* out, int coloff) {
    int n = blockIdx.x * blockDim.x + threadIdx.x, m = blockIdx.y;
    if (n >= D) return;
    float acc = 0.f;
    for (int k = 0; k < D; ++k) acc += o[m * D + k] * b2f(wo[k * D + n]);
    out[m * (2 * D) + coloff + n] = __float2bfloat16(acc);
}
extern "C" void kernel_launch(void* const* d_in, const int* in_sizes, int n_in, void* d_out, int out_size, void* d_ws, size_t ws_size, hipStream_t stream) {
    const bf16 *ax = (const bf16*)d_in[0], *aw = (const bf16*)d_in[1], *awo = (const bf16*)d_in[2];
    const bf16 *bx = (const bf16*)d_in[3], *bw = (const bf16*)d_in[4], *bphi = (const bf16*)d_in[5], *bwo = (const bf16*)d_in[6];
    float* ws = (float*)d_ws;
    float* qa = ws; float* qb = qa + S * N3; float* oa = qb + S * N3; float* ob = oa + S * D; float* phi = ob + S * D; float* kv = phi + 2 * H * S * 56;
    k_gemm<<<dim3(6, S), 256, 0, stream>>>(ax, aw, qa, S, N3, D);
    k_gemm<<<dim3(6, S), 256, 0, stream>>>(bx, bw, qb, S, N3, D);
    k_attn<<<dim3(S / 64, H), 64, 0, stream>>>(qa, oa);
    k_phi<<<dim3(S / 64, H, 2), 64, 0, stream>>>(qb, bphi, phi);
    k_kv<<<dim3(7, H), 256, 0, stream>>>(qb, phi, kv);
    k_lin<<<dim3(S / 64, H), 64, 0, stream>>>(phi, kv, ob);
    k_out<<<dim3(2, S), 256, 0, stream>>>(oa, awo, (bf16*)d_out, 0);
    k_out<<<dim3(2, S), 256, 0, stream>>>(ob, bwo, (bf16*)d_out, D);
}
```

```cpp
#include <hip/hip_runtime.h>
#include <hip/hip_bf16.h>
#include <stdint.h>

typedef __attribute__((ext_vector_type(8))) __bf16 bf16x8;
typedef __attribute__((ext_vector_type(2))) __bf16 bf16x2;
typedef __attribute__((ext_vector_type(2))) float f32x2;
typedef __attribute__((ext_vector_type(4))) float f32x4;
typedef __attribute__((ext_vector_type(16))) float f32x16;
typedef __attribute__((ext_vector_type(4))) unsigned u32x4;
typedef __attribute__((ext_vector_type(2))) unsigned u32x2;
typedef __attribute__((ext_vector_type(4))) short s16x4;
typedef __amdgpu_buffer_rsrc_t rsrc_t;
#define LAS __attribute__((address_space(3)))
#define DI __device__ __forceinline__

constexpr unsigned WS_EPOCH = 0;
constexpr unsigned WS_F1 = 4096;
constexpr unsigned WS_F2 = WS_F1 + 1024 * 128;
constexpr unsigned WS_F3 = WS_F2 + 512 * 128;
constexpr unsigned WS_FO = WS_F3 + 256 * 128;
constexpr unsigned WS_KV = 1u << 20;
constexpr unsigned WS_KVP = WS_KV + (4u << 20);
constexpr unsigned WS_KVR = WS_KVP + (2u << 20);
constexpr unsigned WS_O = WS_KVR + (1u << 20);
constexpr unsigned WS_DBG = WS_O + (4u << 20);
constexpr unsigned WS_BYTES = WS_DBG + (1u << 20);

constexpr unsigned L_R0 = 0;
constexpr unsigned L_R1 = 57856;
constexpr unsigned L_R2 = L_R1 + 32768;
constexpr unsigned L_QX = L_R2 + 57344;
constexpr unsigned L_PHI = L_QX + 8192;
constexpr unsigned L_CNT = L_PHI + 4096;
constexpr unsigned L_TAG = L_CNT + 256;
constexpr unsigned LDS_BYTES = L_TAG + 16;
enum { C_X0 = 0, C_G0 = 4, C_FX0 = 8, C_WQ = 12, C_WFREE = 13, C_QX = 14, C_XB0 = 15, C_FXB0 = 19, C_WB0 = 23, C_KDONE = 25,
       C_PV = 26, C_KVDONE = 27, C_WO0 = 28, C_WOFREE = 30, C_EX0 = 31, C_CMB = 35, C_VDONE = 36, C_QDONE = 37, C_PHI = 38 };

constexpr unsigned SPIN_CAP = 1u << 18;
constexpr float QSCALE = 0.18898223650461362f * 1.4426950408889634f;
constexpr float LOG2E = 1.4426950408889634f;
constexpr float REF_SHIFT = 64.f;

#define MFMA(a, b, c) __builtin_amdgcn_mfma_f32_32x32x16_bf16((a), (b), (c), 0, 0, 0)
#define CBAR() asm volatile("" ::: "memory")
#define WAIT_VM(n) asm volatile("s_waitcnt vmcnt(" #n ")" ::: "memory")
#define WAIT_LGKM0() asm volatile("s_waitcnt lgkmcnt(0)" ::: "memory")
#define SBAR() __builtin_amdgcn_sched_barrier(0)

#define STAMP(i) do {} while (0)

DI unsigned pk2(float a, float b) { f32x2 v = {a, b}; return __builtin_bit_cast(unsigned, __builtin_convertvector(v, bf16x2)); }
DI u32x4 pk8(const f32x16& a, int s) {
    return (u32x4){pk2(a[8 * s], a[8 * s + 1]), pk2(a[8 * s + 2], a[8 * s + 3]), pk2(a[8 * s + 4], a[8 * s + 5]), pk2(a[8 * s + 6], a[8 * s + 7])}; }
DI bf16x8 asbf(u32x4 v) { return __builtin_bit_cast(bf16x8, v); }
DI float swap_max(float m) { auto r = __builtin_amdgcn_permlane32_swap(__float_as_uint(m), __float_as_uint(m), false, false);
    return __builtin_fmaxf(__uint_as_float(r[0]), __uint_as_float(r[1])); }
DI float swap_min(float m) { auto r = __builtin_amdgcn_permlane32_swap(__float_as_uint(m), __float_as_uint(m), false, false);
    return __builtin_fminf(__uint_as_float(r[0]), __uint_as_float(r[1])); }
DI float swap_sum(float m) { auto r = __builtin_amdgcn_permlane32_swap(__float_as_uint(m), __float_as_uint(m), false, false);
    return __uint_as_float(r[0]) + __uint_as_float(r[1]); }
DI float from_hi(float m) { auto r = __builtin_amdgcn_permlane32_swap(__float_as_uint(m), __float_as_uint(m), false, false);
    return __uint_as_float(r[1]); }
DI float max16(const f32x16& s) {
    float a = __builtin_fmaxf(__builtin_fmaxf(s[0], s[1]), s[2]), b = __builtin_fmaxf(__builtin_fmaxf(s[3], s[4]), s[5]);
    a = __builtin_fmaxf(__builtin_fmaxf(a, s[6]), s[7]); b = __builtin_fmaxf(__builtin_fmaxf(b, s[8]), s[9]);
    a = __builtin_fmaxf(__builtin_fmaxf(a, s[10]), s[11]); b = __builtin_fmaxf(__builtin_fmaxf(b, s[12]), s[13]);
    a = __builtin_fmaxf(__builtin_fmaxf(a, s[14]), s[15]); return __builtin_fmaxf(a, b); }
DI float min16(const f32x16& s) {
    float a = __builtin_fminf(__builtin_fminf(s[0], s[1]), s[2]), b = __builtin_fminf(__builtin_fminf(s[3], s[4]), s[5]);
    a = __builtin_fminf(__builtin_fminf(a, s[6]), s[7]); b = __builtin_fminf(__builtin_fminf(b, s[8]), s[9]);
    a = __builtin_fminf(__builtin_fminf(a, s[10]), s[11]); b = __builtin_fminf(__builtin_fminf(b, s[12]), s[13]);
    a = __builtin_fminf(__builtin_fminf(a, s[14]), s[15]); return __builtin_fminf(a, b); }

DI void dma(rsrc_t rs, unsigned voff, unsigned soff, unsigned lds) {
    asm volatile("s_mov_b32 m0, %0\n\ts_nop 3\n\tbuffer_load_dwordx4 %1, %2, %3 offen lds" :: "s"(lds), "v"(voff), "s"(rs), "s"(soff) : "memory"); }
DI u32x4 lds_rd128(unsigned a) { return *(LAS const u32x4*)(uintptr_t)a; }
DI f32x4 lds_rdf4(unsigned a) { return *(LAS const f32x4*)(uintptr_t)a; }
DI void lds_wr128(unsigned a, u32x4 v) { *(LAS u32x4*)(uintptr_t)a = v; }
DI void lds_wrf4(unsigned a, f32x4 v) { *(LAS f32x4*)(uintptr_t)a = v; }
DI void lds_wr64(unsigned a, u32x2 v) { *(LAS u32x2*)(uintptr_t)a = v; }
DI bf16x8 lds_tr2(unsigned a0, unsigned a1) {
    s16x4 lo = __builtin_amdgcn_ds_read_tr16_b64_v4i16((LAS s16x4*)(uintptr_t)a0), hi = __builtin_amdgcn_ds_read_tr16_b64_v4i16((LAS s16x4*)(uintptr_t)a1);
    return __builtin_bit_cast(bf16x8, __builtin_shufflevector(lo, hi, 0, 1, 2, 3, 4, 5, 6, 7)); }
DI void sig(int c, int) {
    asm volatile("s_mov_b64 exec, 1\n\tds_add_u32 %0, %1\n\ts_mov_b64 exec, -1" :: "v"(L_CNT + 4 * c), "v"(1u) : "memory"); }
DI void waitc(int c, unsigned n) {
    unsigned v, sv, it;
    asm volatile("s_mov_b32 %2, %5\n"
                 "1:\n\tds_read_b32 %0, %3\n\ts_waitcnt lgkmcnt(0)\n\tv_readfirstlane_b32 %1, %0\n\ts_cmp_ge_u32 %1, %4\n\ts_cbranch_scc1 2f\n\t"
                 "s_sleep 1\n\ts_sub_u32 %2, %2, 1\n\ts_cbranch_scc0 1b\n2:"
                 : "=&v"(v), "=&s"(sv), "=&s"(it) : "v"(L_CNT + 4 * c), "i"(n), "i"(SPIN_CAP) : "memory", "scc"); }
DI u32x2 rd_tag() { const u32x2 t = *(LAS const u32x2*)(uintptr_t)L_TAG;
    return (u32x2){(unsigned)__builtin_amdgcn_readfirstlane(t[0]), (unsigned)__builtin_amdgcn_readfirstlane(t[1])}; }
DI u32x4 ld16(rsrc_t rs, unsigned voff, unsigned soff) { return __builtin_amdgcn_raw_buffer_load_b128(rs, voff, soff, 16); }
DI void st16(rsrc_t rs, u32x4 v, unsigned voff, unsigned soff) { __builtin_amdgcn_raw_buffer_store_b128(v, rs, voff, soff, 16); }
DI u32x2 ldflag(rsrc_t rs, unsigned voff) { return __builtin_amdgcn_raw_buffer_load_b64(rs, voff, 0, 16); }
DI bool tagok(u32x2 f, u32x2 tag) { return __all(f[0] == tag[0] && f[1] == tag[1]); }
DI void poll(rsrc_t rs, unsigned voff, u32x2 tag) {
    CBAR();
#pragma clang loop unroll(disable)
    for (unsigned it = 0; it < SPIN_CAP; ++it) { u32x2 f = ldflag(rs, voff); if (tagok(f, tag)) break; __builtin_amdgcn_s_sleep(2); CBAR(); }
    CBAR(); }
DI void post(rsrc_t rs, unsigned off, u32x2 tag, int lane) {
    WAIT_VM(0);
    if (lane == 0) __builtin_amdgcn_raw_buffer_store_b64(tag, rs, off, 0, 16);
    CBAR(); }

template <bool T, bool PROG>
DI void gemm2(const bf16x8 (&x)[28], unsigned w0, unsigned w1, f32x16& a0, f32x16& a1) {
    a0 = f32x16{}; a1 = f32x16{};
#pragma unroll
    for (int ks = 0; ks < 28; ++ks) {
        if (PROG && (ks & 7) == 0) waitc(C_G0 + (ks >> 3), 4);
        bf16x8 f0 = lds_tr2(w0 + ks * 2048, w0 + ks * 2048 + 512), f1 = lds_tr2(w1 + ks * 2048, w1 + ks * 2048 + 512);
        if (T) { a0 = MFMA(f0, x[ks], a0); a1 = MFMA(f1, x[ks], a1); }
        else   { a0 = MFMA(x[ks], f0, a0); a1 = MFMA(x[ks], f1, a1); }
    }
}
DI void read_x(bf16x8 (&x)[28], unsigned slot, int lane) {
    const int r = lane & 31, h = lane >> 5, f = (r >> 1) & 7;
    const unsigned rb = slot + r * 896;
#pragma unroll
    for (int i = 0; i < 4; ++i) {
        const unsigned a = rb + (((2 * i + h) ^ f) << 4);
#pragma unroll
        for (int g = 0; g < 7; ++g) x[4 * g + i] = asbf(lds_rd128(a + g * 128));
    }
}
DI unsigned tr_addr128(unsigned base, int cb, int rowgrp, int lane) {
    const int q = (lane & 15) >> 2, byte = cb + 32 * ((lane >> 4) & 1) + 8 * (lane & 3);
    return base + (rowgrp + q) * 128 + ((((byte >> 4) ^ ((q >> 1) << 2)) & 7) << 4) + (byte & 8);
}
DI void feat(f32x16& p, f32x16& e, f32x16& g, bool hi) {
#pragma unroll
    for (int i = 12; i < 16; ++i) p[i] = hi ? p[0] : p[i];
    const float mx = swap_max(max16(p)) * LOG2E, mn = swap_min(min16(p)) * LOG2E;
#pragma unroll
    for (int i = 0; i < 16; ++i) { e[i] = __builtin_amdgcn_exp2f(__builtin_fmaf(p[i], LOG2E, -mx)); g[i] = __builtin_amdgcn_exp2f(__builtin_fmaf(p[i], -LOG2E, mn)); }
}

__global__ __launch_bounds__(512, 2) void fused_fwd(const void* __restrict__ xa, const void* __restrict__ wa, const void* __restrict__ woa,
                                                    const void* __restrict__ xb, const void* __restrict__ wb, const void* __restrict__ wphi,
                                                    const void* __restrict__ wob, void* __restrict__ out, void* __restrict__ ws) {
    extern __shared__ __attribute__((aligned(16))) char smem[];
    const int tid = threadIdx.x, lane = tid & 63, wid = __builtin_amdgcn_readfirstlane(tid >> 6);
    const int b = blockIdx.x, xcd = b & 7, r = b >> 3, ml = r & 3, jl = r >> 2;
    const int mt = (xcd & 3) * 4 + ml, up = xcd >> 2;
    const bool hi = lane >= 32;
    const int tt0 = jl & 3;
    int g0 = 2 * ml + (jl & 1); g0 -= (g0 >= 7) ? 7 : 0;
    const int head = (int)((0xDB9642FECA875310ull >> (4 * (up * 8 + jl))) & 15);

    unsigned pfd = 0;
    if (wid == 0) {
        unsigned long long pc; unsigned len;
        asm volatile("s_getpc_b64 %0\n.Lpf_begin%=:\n\ts_mov_b32 %1, .Lpf_end-.Lpf_begin%=" : "=s"(pc), "=s"(len));
        const rsrc_t rc = __builtin_amdgcn_make_buffer_rsrc((void*)pc, 0, len, 0x00020000);
        asm volatile("s_nop 4\n\tbuffer_load_dword %0, %1, %2, 0 offen" : "+v"(pfd) : "v"(lane < 12 ? (r * 12 + lane) * 128 : 0x7FFFFFF0), "s"(rc) : "memory");
    }
    asm volatile("" :: "s"(xa), "s"(wa), "s"(woa), "s"(xb), "s"(wb), "s"(wphi), "s"(wob), "s"(out), "s"(ws));
    if (tid < 64) *(LAS unsigned*)(uintptr_t)(L_CNT + 4 * tid) = 0u;
    if (tid >= 64 && tid < 192) *(LAS unsigned*)(uintptr_t)(L_R0 + 57344 + 4 * (tid - 64)) = 0u;
    const rsrc_t rws = __builtin_amdgcn_make_buffer_rsrc(ws, 0, WS_BYTES, 0x00020000);
    STAMP(0);

    if (wid >= 4) {
        const int w = wid - 4;
        const rsrc_t rxa = __builtin_amdgcn_make_buffer_rsrc((void*)xa, 0, 2048 * 896, 0x00020000);
        const rsrc_t rwa = __builtin_amdgcn_make_buffer_rsrc((void*)wa, 0, 448 * 2688, 0x00020000);
        unsigned xo[7];
#pragma unroll
        for (int i = 0; i < 7; ++i) {
            const int L = 8 * (w + 4 * i) + (lane >> 3), row = (L * 9363) >> 16; int lam = L - 7 * row + g0; lam -= (lam >= 7) ? 7 : 0;
            xo[i] = row * 896 + lam * 128 + ((((lane & 7) ^ (row >> 1)) & 7) << 4);
        }
        int wc = (lane & 7) ^ (((lane >> 4) & 1) << 2);
        const unsigned wvo = (lane >> 3) * 2688 + (wc > 6 ? 6 : wc) * 16;
        const unsigned wqvo = (lane >> 2) * 2688 + (lane & 3) * 16;
#define DMA_X(rs, tile, slot) _Pragma("unroll") for (int i_ = 0; i_ < 7; ++i_) dma(rs, xo[i_], (tile) * 28672, (slot) + (w + 4 * i_) * 1024)
#define DMA_G(rs, cbase, dst, gi) do { int g_ = (gi) + g0; g_ -= (g_ >= 7) ? 7 : 0; \
            dma(rs, wvo, (cbase) + (g_ * 64 + w * 8) * 2688, (dst) + ((gi) * 8 + w) * 1024); \
            dma(rs, wvo, (cbase) + (g_ * 64 + w * 8 + 32) * 2688, (dst) + ((gi) * 8 + w + 4) * 1024); } while (0)
        const unsigned wcol = 896 * (1 + up) + 112 * jl;
        const int t4 = mt * 4;
        DMA_X(rxa, t4 + tt0, L_R2);
        DMA_G(rwa, wcol, L_R0, 0); DMA_G(rwa, wcol, L_R0, 1); DMA_G(rwa, wcol, L_R0, 2); DMA_G(rwa, wcol, L_R0, 3);
        asm volatile("s_waitcnt lgkmcnt(0)\n\ts_barrier" ::: "memory");
        WAIT_VM(8); sig(C_X0, lane);
        DMA_G(rwa, wcol, L_R0, 4); DMA_G(rwa, wcol, L_R0, 5);
        WAIT_VM(8); sig(C_G0, lane);
        DMA_G(rwa, wcol, L_R0, 6); DMA_X(rxa, t4 + ((tt0 + 1) & 3), L_R2 + 28672);
        WAIT_VM(13); sig(C_G0 + 1, lane);
        WAIT_VM(9); sig(C_G0 + 2, lane);
        waitc(C_FX0, 1);
        DMA_X(rxa, t4 + ((tt0 + 2) & 3), L_R2);
        WAIT_VM(14); sig(C_G0 + 3, lane);
        WAIT_VM(7); sig(C_X0 + 1, lane);
        waitc(C_FX0 + 1, 1);
        DMA_X(rxa, t4 + ((tt0 + 3) & 3), L_R2 + 28672);
        WAIT_VM(7); sig(C_X0 + 2, lane);
        {
            const unsigned qcol = (56 * head) & ~15;
#pragma unroll
            for (int i = 0; i < 7; ++i) { int g_ = i + g0; g_ -= (g_ >= 7) ? 7 : 0; dma(rwa, wqvo, qcol + (g_ * 64 + w * 16) * 2688, L_R1 + (i * 4 + w) * 1024); }
        }
        WAIT_VM(7); sig(C_X0 + 3, lane);
        STAMP(1);
        const rsrc_t rxb = __builtin_amdgcn_make_buffer_rsrc((void*)xb, 0, 2048 * 896, 0x00020000);
        const rsrc_t rwb = __builtin_amdgcn_make_buffer_rsrc((void*)wb, 0, 448 * 2688, 0x00020000);
        const rsrc_t rph = __builtin_amdgcn_make_buffer_rsrc((void*)wphi, 0, 16 * 1568, 0x00020000);
        waitc(C_FX0 + 2, 1);
        DMA_X(rxb, t4 + tt0, L_R2);
        dma(rph, lane * 16, jl * 3136 + w * 1024, L_PHI + w * 1024);
        WAIT_VM(8); sig(C_WQ, lane);
        waitc(C_FX0 + 3, 1);
        DMA_X(rxb, t4 + ((tt0 + 1) & 3), L_R2 + 28672);
        bf16x8 x[28];
        WAIT_VM(7); sig(C_XB0, lane); sig(C_PHI, lane);
        if (w == 0) { waitc(C_XB0, 4); read_x(x, L_R2, lane); WAIT_LGKM0(); sig(C_FXB0, lane); }
        waitc(C_FXB0, 1);
        DMA_X(rxb, t4 + ((tt0 + 2) & 3), L_R2);
        WAIT_VM(7); sig(C_XB0 + 1, lane);
        if (w == 1) { waitc(C_XB0 + 1, 4); read_x(x, L_R2 + 28672, lane); WAIT_LGKM0(); sig(C_FXB0 + 1, lane); }
        waitc(C_FXB0 + 1, 1);
        DMA_X(rxb, t4 + ((tt0 + 3) & 3), L_R2 + 28672);
        WAIT_VM(7); sig(C_XB0 + 2, lane);
        if (w == 2) { waitc(C_XB0 + 2, 4); read_x(x, L_R2, lane); WAIT_LGKM0(); sig(C_FXB0 + 2, lane); }
        waitc(C_WFREE, 4);
        const unsigned bcol = (up ? 0 : 896) + 112 * jl;
#pragma unroll
        for (int gi = 0; gi < 7; ++gi) DMA_G(rwb, bcol, L_R0, gi);
        WAIT_VM(14); sig(C_XB0 + 3, lane);
        if (w == 3) { waitc(C_XB0 + 3, 4); read_x(x, L_R2 + 28672, lane); WAIT_LGKM0(); sig(C_FXB0 + 3, lane); }
        if (!up) {
            waitc(C_FXB0 + 2, 1); waitc(C_FXB0 + 3, 1);
#pragma unroll
            for (int gi = 0; gi < 7; ++gi) DMA_G(rwb, bcol + 896, L_R2, gi);
            WAIT_VM(14);
        } else WAIT_VM(0);
        sig(C_WB0, lane);
        waitc(C_WB0, 4);
        STAMP(2);
        const unsigned tw0 = tr_addr128(0, 0, 8 * (lane >> 5), lane), tw1 = tr_addr128(0, 56, 8 * (lane >> 5), lane);
        f32x16 a0, a1;
        gemm2<true, false>(x, L_R0 + tw0, L_R0 + tw1, a0, a1);
        WAIT_LGKM0(); sig(C_KDONE, lane);
        const u32x2 tag = rd_tag();
        STAMP(3);
        waitc(C_PHI, 4);
        const int ttg = t4 + ((tt0 + w) & 3);
        u32x4 pe[2][4];
#pragma unroll
        for (int hh = 0; hh < 2; ++hh) {
            const f32x16& a = hh ? a1 : a0;
            u32x4 k0 = pk8(a, 0), k1 = pk8(a, 1);
            if (hi) { k1[2] = 0; k1[3] = 0; }
            const unsigned pb = L_PHI + hh * 1568 + (4 * (lane >> 5) + ((lane & 15) >> 2)) * 56 + 32 * ((lane >> 4) & 1) + 8 * (lane & 3);
            bf16x8 f0 = lds_tr2(pb, pb + 8 * 56);
            u32x4 f1 = __builtin_bit_cast(u32x4, lds_tr2(pb + 16 * 56, pb + 24 * 56));
            if (hi) { f1[2] = 0; f1[3] = 0; }
            f32x16 p = MFMA(f0, asbf(k0), f32x16{}); p = MFMA(asbf(f1), asbf(k1), p);
            f32x16 e, g; feat(p, e, g, hi);
            if (!up) {
#pragma unroll
                for (int i = 12; i < 16; ++i) { e[i] = hi ? 0.f : e[i]; g[i] = hi ? 0.f : g[i]; }
                float se = 0.f, sg = 0.f;
#pragma unroll
                for (int i = 0; i < 16; ++i) { se += e[i]; sg += g[i]; }
                se = __builtin_amdgcn_rcpf(swap_sum(se)); sg = __builtin_amdgcn_rcpf(swap_sum(sg));
#pragma unroll
                for (int i = 0; i < 16; ++i) { e[i] *= se; g[i] *= sg; }
            }
            pe[hh][0] = pk8(e, 0); pe[hh][1] = pk8(e, 1); pe[hh][2] = pk8(g, 0); pe[hh][3] = pk8(g, 1);
        }
        STAMP(4);
        const rsrc_t rwob = __builtin_amdgcn_make_buffer_rsrc((void*)wob, 0, 448 * 896, 0x00020000);
        const rsrc_t rwoa = __builtin_amdgcn_make_buffer_rsrc((void*)woa, 0, 448 * 896, 0x00020000);
        const int ct = r >> 2, rt = xcd * 4 + ml;
        const unsigned wovo = (lane >> 3) * 896 + wc * 16;
        waitc(C_QDONE, 4);
        if (!up) {
            const int tk = lane & 31, pi = (((tk >> 1) & 1) << 3) | (tk & 1) | (((tk >> 2) & 3) << 1);
#pragma unroll
            for (int hh = 0; hh < 2; ++hh)
#pragma unroll
                for (int u = 0; u < 8; ++u) {
                    const int piece = 8 * (u >> 2) + 2 * (u & 3) + (lane >> 5);
                    const u32x4 v = pe[hh][u >> 1];
                    lds_wr64(L_R1 + hh * 16384 + (w * 32 + tk) * 128 + ((piece ^ pi) << 3), (u & 1) ? (u32x2){v[2], v[3]} : (u32x2){v[0], v[1]});
                }
            WAIT_VM(0); sig(C_WB0 + 1, lane); waitc(C_WB0 + 1, 4);
            gemm2<false, false>(x, L_R2 + tw0, L_R2 + tw1, a0, a1);
            WAIT_LGKM0(); sig(C_VDONE, lane);
            waitc(C_KDONE, 4);
#pragma unroll
            for (int hh = 0; hh < 2; ++hh)
#pragma unroll
                for (int s = 0; s < 2; ++s) {
                    u32x4 v = pk8(hh ? a1 : a0, s);
                    if ((lane & 31) >= 28) { const unsigned c = (lane & 31) == 28 ? 0x3F803F80u : 0u; v = (u32x4){c, c, c, c}; }
                    lds_wr128(L_R0 + ((hh * 4 + w) * 2 + s) * 1024 + lane * 16, v);
                }
            WAIT_LGKM0(); sig(C_PV, lane); waitc(C_PV, 4);
            const int hh = w >> 1, ft = w & 1, q = (lane & 15) >> 2, P = 8 * ft + 4 * ((lane >> 4) & 1) + (lane & 3);
            f32x16 kv = {};
#pragma unroll
            for (int st = 0; st < 8; ++st) {
                const int t0 = 16 * (st & 1) + 4 * (lane >> 5) + q, t1 = t0 + 8;
                const int p0 = (((t0 >> 1) & 1) << 3) | (t0 & 1) | (((t0 >> 2) & 3) << 1), p1 = (((t1 >> 1) & 1) << 3) | (t1 & 1) | (((t1 >> 2) & 3) << 1);
                const unsigned ib = L_R1 + hh * 16384 + (st >> 1) * 4096;
                bf16x8 fa = lds_tr2(ib + t0 * 128 + ((P ^ p0) << 3), ib + t1 * 128 + ((P ^ p1) << 3));
                bf16x8 fb = asbf(lds_rd128(L_R0 + ((hh * 4 + (st >> 1)) * 2 + (st & 1)) * 1024 + lane * 16));
                kv = MFMA(fa, fb, kv);
            }
            WAIT_LGKM0(); sig(C_KVDONE, lane);
            const int hk = 2 * jl + hh;
#pragma unroll
            for (int u = 0; u < 4; ++u)
                st16(rws, __builtin_bit_cast(u32x4, (f32x4){kv[4 * u], kv[4 * u + 1], kv[4 * u + 2], kv[4 * u + 3]}), lane * 16, WS_KVP + ((hk * 16 + mt) * 8 + ft * 4 + u) * 1024);
            post(rws, WS_F2 + ((hk * 16 + mt) * 2 + ft) * 128, tag, lane);
            waitc(C_KVDONE, 4);
        } else {
            sig(C_VDONE, lane);
            waitc(C_KDONE, 4);
        }
        STAMP(5);
        if (r < 28) {
#pragma unroll
            for (int i = 0; i < 14; ++i) dma(rwob, wovo, ct * 128 + (w + 4 * i) * 8 * 896, L_R0 + (w + 4 * i) * 1024);
        }
        if (w == 3) {
            const int hr = up * 8 + jl, i = lane & 15, pg = lane >> 4;
            poll(rws, WS_F2 + ((hr * 16 + i) * 2 + (mt >> 3)) * 128, tag);
            f32x4 s0 = {}, s1 = {};
#pragma unroll
            for (int p = 0; p < 4; ++p) {
                const unsigned vo = (pg * 4 + p) * 8192 + (16 * (mt & 3) + i) * 16, so = WS_KVP + hr * 131072 + (mt >> 2) * 2048;
                s0 += __builtin_bit_cast(f32x4, ld16(rws, vo, so)); s1 += __builtin_bit_cast(f32x4, ld16(rws, vo, so + 1024));
            }
            float v[8] = {s0[0], s0[1], s0[2], s0[3], s1[0], s1[1], s1[2], s1[3]};
#pragma unroll
            for (int k = 0; k < 8; ++k) {
                auto t = __builtin_amdgcn_permlane16_swap(__float_as_uint(v[k]), __float_as_uint(v[k]), false, false);
                v[k] = swap_sum(__uint_as_float(t[0]) + __uint_as_float(t[1]));
            }
            u32x4 o = {pk2(v[0], v[1]), pk2(v[2], v[3]), pk2(v[4], v[5]), pk2(v[6], v[7])};
            const int cl = 16 * (mt & 3) + i;
            if ((cl & 31) == 29) { const unsigned c = ((mt >> 2) & 1) && cl >= 32 ? 0u : 0x3F803F80u; o = (u32x4){0x3F803F80u, 0x3F803F80u, c, c}; }
            if (lane < 16) st16(rws, o, i * 16, WS_KVR + (hr * 256 + mt * 16) * 16);
            post(rws, WS_F3 + (hr * 16 + mt) * 128, tag, lane);
        }
        STAMP(6);
        if (up) {
            poll(rws, WS_F3 + (2 * jl * 16 + (lane & 31)) * 128, tag);
            u32x4 kf[2][4];
#pragma unroll
            for (int hh = 0; hh < 2; ++hh)
#pragma unroll
                for (int f = 0; f < 4; ++f) kf[hh][f] = ld16(rws, lane * 16, WS_KVR + ((2 * jl + hh) * 4 + f) * 1024);
#pragma unroll
            for (int hh = 0; hh < 2; ++hh) {
                f32x16 np = MFMA(asbf(kf[hh][0]), asbf(pe[hh][0]), f32x16{}); np = MFMA(asbf(kf[hh][1]), asbf(pe[hh][1]), np);
                f32x16 nm = MFMA(asbf(kf[hh][2]), asbf(pe[hh][2]), f32x16{}); nm = MFMA(asbf(kf[hh][3]), asbf(pe[hh][3]), nm);
                const float rp = __builtin_amdgcn_rcpf(np[13]), rm = __builtin_amdgcn_rcpf(nm[13]);
                const float rd = __builtin_amdgcn_rcpf(__builtin_fmaf(rp, np[12], rm * nm[12]) + 1e-6f);
                const float cp = from_hi(rp * rd), cm = from_hi(rm * rd);
                f32x16 o;
#pragma unroll
                for (int i = 0; i < 16; ++i) o[i] = __builtin_fmaf(cp, np[i], cm * nm[i]);
                u32x4 o0 = pk8(o, 0), o1 = pk8(o, 1);
                if (hi) { o1[2] = 0; o1[3] = 0; }
                const unsigned so = WS_O + (2u << 20) + ((ttg * 16 + 2 * jl + hh) * 2) * 1024;
                st16(rws, o0, lane * 16, so); st16(rws, o1, lane * 16, so + 1024);
            }
            WAIT_VM(0);
            if (lane < 2) __builtin_amdgcn_raw_buffer_store_b64(tag, rws, WS_FO + (1024 + ttg * 16 + 2 * jl + lane) * 128, 0, 16);
            CBAR();
        }
        STAMP(7);
        if (r < 28) {
            const rsrc_t rout = __builtin_amdgcn_make_buffer_rsrc(out, 0, 2048 * 1792, 0x00020000);
            const int tp = w & 1, kh = w >> 1, tto = rt * 2 + tp;
            const unsigned ta0 = tr_addr128(L_R0, 0, kh * 224 + 4 * (lane >> 5), lane), ta1 = tr_addr128(L_R0, 64, kh * 224 + 4 * (lane >> 5), lane);
#pragma unroll 1
            for (int br = 1; br >= 0; --br) {
                WAIT_VM(0); sig(C_WO0 + br, lane); waitc(C_WO0 + br, 4);
                bf16x8 wf[16][2];
#pragma unroll
                for (int k = 0; k < 16; ++k) {
                    const unsigned ro = ((k >> 1) * 28 + (k & 1) * 16) * 128;
                    wf[k][0] = lds_tr2(ta0 + ro, ta0 + ro + 1024); wf[k][1] = lds_tr2(ta1 + ro, ta1 + ro + 1024);
                }
                if (br) {
                    WAIT_LGKM0(); sig(C_WOFREE, lane); waitc(C_WOFREE, 4);
#pragma unroll
                    for (int i = 0; i < 14; ++i) dma(rwoa, wovo, ct * 128 + (w + 4 * i) * 8 * 896, L_R0 + (w + 4 * i) * 1024);
                }
                STAMP(8 + 3 * (1 - br));
                poll(rws, WS_FO + (br * 1024 + tto * 16 + kh * 8 + (lane & 7)) * 128, tag);
                STAMP(9 + 3 * (1 - br));
                f32x16 c0 = {}, c1 = {};
                u32x4 of[16];
#pragma unroll
                for (int k = 0; k < 16; ++k) of[k] = ld16(rws, lane * 16, WS_O + br * (2u << 20) + ((tto * 16 + kh * 8) * 2 + k) * 1024);
#pragma unroll
                for (int k = 0; k < 16; ++k) { c0 = MFMA(wf[k][0], asbf(of[k]), c0); c1 = MFMA(wf[k][1], asbf(of[k]), c1); }
                const unsigned xb_ = L_R1 + br * 16384;
#pragma unroll
                for (int u = 0; u < 4; ++u) {
                    const f32x4 s = kh ? (f32x4){c0[4 * u], c0[4 * u + 1], c0[4 * u + 2], c0[4 * u + 3]} : (f32x4){c1[4 * u], c1[4 * u + 1], c1[4 * u + 2], c1[4 * u + 3]};
                    lds_wrf4(xb_ + (w * 4 + u) * 1024 + lane * 16, s);
                }
                WAIT_LGKM0(); sig(C_EX0 + br * 2 + tp, lane); waitc(C_EX0 + br * 2 + tp, 2);
                f32x16 c;
#pragma unroll
                for (int u = 0; u < 4; ++u) {
                    const f32x4 s = lds_rdf4(xb_ + ((w ^ 2) * 4 + u) * 1024 + lane * 16);
#pragma unroll
                    for (int i = 0; i < 4; ++i) c[4 * u + i] = (kh ? c1[4 * u + i] : c0[4 * u + i]) + s[i];
                }
                const unsigned vo = (tto * 32 + (lane & 31)) * 1792 + (hi ? 16 : 0), so = br * 896 + ct * 128 + kh * 64;
#pragma unroll
                for (int gp = 0; gp < 2; ++gp) {
                    auto s0 = __builtin_amdgcn_permlane32_swap(pk2(c[8 * gp], c[8 * gp + 1]), pk2(c[8 * gp + 4], c[8 * gp + 5]), false, false);
                    auto s1 = __builtin_amdgcn_permlane32_swap(pk2(c[8 * gp + 2], c[8 * gp + 3]), pk2(c[8 * gp + 6], c[8 * gp + 7]), false, false);
                    __builtin_amdgcn_raw_buffer_store_b128((u32x4){s0[0], s1[0], s0[1], s1[1]}, rout, vo + gp * 32, so, 0);
                }
                STAMP(10 + 3 * (1 - br));
            }
        } else if (b == 255 && w == 0) {
#pragma unroll 1
            for (int i = 0; i < 4; ++i) { const int id = i * 64 + lane; poll(rws, WS_F1 + ((id >> 4) * 64 + (id & 15) * 4 + ((id >> 4) & 3)) * 128, tag); }
            if (lane == 0) __builtin_amdgcn_raw_buffer_store_b64(tag, rws, WS_EPOCH, 0, 16);
        }
        asm volatile(".Lpf_end:");
        return;
    }

    const int w = wid;
    asm volatile("s_waitcnt lgkmcnt(0)\n\ts_barrier" ::: "memory");
    if (w == 0) {
        const u32x2 ep = ldflag(rws, WS_EPOCH);
        const unsigned long long e = (((unsigned long long)ep[1] << 32) | ep[0]) + 1;
        if (lane == 0) lds_wr64(L_TAG, (u32x2){(unsigned)e, (unsigned)(e >> 32)});
        WAIT_VM(0);
        asm volatile("" :: "v"(pfd));
    }
    const int ttg = mt * 4 + ((tt0 + w) & 3);
    u32x4 q0, q1;
    u32x2 tag;
    {
        bf16x8 x[28];
        waitc(C_X0 + w, 4);
        STAMP(1);
        read_x(x, L_R2 + (w & 1) * 28672, lane);
        WAIT_LGKM0(); sig(C_FX0 + w, lane);
        const unsigned tw0 = tr_addr128(L_R0, 0, 8 * (lane >> 5), lane), tw1 = tr_addr128(L_R0, 56, 8 * (lane >> 5), lane);
        f32x16 a0, a1;
        if (up) gemm2<false, true>(x, tw0, tw1, a0, a1); else gemm2<true, true>(x, tw0, tw1, a0, a1);
        WAIT_LGKM0(); sig(C_WFREE, lane);
        waitc(C_FX0, 1);
        tag = rd_tag();
        STAMP(2);
#pragma unroll
        for (int hh = 0; hh < 2; ++hh)
#pragma unroll
            for (int s = 0; s < 2; ++s) {
                u32x4 v = pk8(hh ? a1 : a0, s);
                if (up) { if ((lane & 31) >= 28) { const unsigned c = (lane & 31) == 28 ? 0x3F803F80u : 0u; v = (u32x4){c, c, c, c}; } }
                else if (s == 1 && hi) { v[2] = 0x00003F80u; v[3] = 0u; }
                st16(rws, v, lane * 16, WS_KV + (((2 * jl + hh) * 64 + ttg) * 4 + up * 2 + s) * 1024);
            }
        const unsigned f1 = WS_F1 + ((up * 8 + jl) * 64 + ttg) * 128;
        if (w < 2) post(rws, f1, tag, lane);
        STAMP(3);
        waitc(C_WQ, 4);
        const unsigned tq = L_R1 + (8 * (lane >> 5) + ((lane & 15) >> 2)) * 64 + 8 * (head & 1) + 32 * ((lane >> 4) & 1) + 8 * (lane & 3);
        f32x16 a = {};
#pragma unroll
        for (int ks = 0; ks < 28; ++ks) a = MFMA(lds_tr2(tq + ks * 1024, tq + ks * 1024 + 256), x[ks], a);
        WAIT_LGKM0(); sig(C_QDONE, lane);
        if (w >= 2) post(rws, f1, tag, lane);
#pragma unroll
        for (int i = 0; i < 16; ++i) a[i] *= QSCALE;
        q0 = pk8(a, 0); q1 = pk8(a, 1);
        if (hi) { q1[2] = 0; q1[3] = 0; }
    }
    STAMP(4);
    const int pj = head >> 1, ptt0 = pj & 3;
    const unsigned kvb = WS_KV + head * 64 * 4096;
    const unsigned fo = WS_F1 + (((lane >> 2) & 1) * 8 + pj) * 8192 + (4 * w + (lane & 3)) * 512;
#define KT(i) ((4 * w + (((i) + ml) & 3)) * 4 + ((ptt0 + ((i) >> 2)) & 3))
#define LOADKV(d, i) do { const unsigned so_ = kvb + KT(i) * 4096; d[0] = ld16(rws, lane * 16, so_); d[1] = ld16(rws, lane * 16, so_ + 1024); \
                          d[2] = ld16(rws, lane * 16, so_ + 2048); d[3] = ld16(rws, lane * 16, so_ + 3072); } while (0)
    u32x4 kA[4], kB[4], kC[4], kD[4];
    poll(rws, fo + ptt0 * 128, tag);
    STAMP(5);
    LOADKV(kA, 0); LOADKV(kB, 1);
    bf16x8 Q[4][2];
    {
        f32x16 s = MFMA(asbf(kA[0]), asbf(q0), f32x16{}); s = MFMA(asbf(kA[1]), asbf(q1), s);
        const float m = swap_max(max16(s)) + REF_SHIFT;
        u32x4 q1m = q1;
        if (hi) q1m[2] = pk2(-m, 0.f);
        lds_wr128(L_QX + (w * 2) * 1024 + lane * 16, q0); lds_wr128(L_QX + (w * 2 + 1) * 1024 + lane * 16, q1m);
        WAIT_LGKM0(); sig(C_QX, lane); waitc(C_QX, 4);
#pragma unroll
        for (int a = 0; a < 4; ++a) { Q[a][0] = asbf(lds_rd128(L_QX + (a * 2) * 1024 + lane * 16)); Q[a][1] = asbf(lds_rd128(L_QX + (a * 2 + 1) * 1024 + lane * 16)); }
    }
    STAMP(6);
    f32x16 O[4] = {};
#define TILE(K) _Pragma("unroll") for (int a = 0; a < 4; ++a) { \
        f32x16 s = MFMA(asbf(K[0]), Q[a][0], f32x16{}); s = MFMA(asbf(K[1]), Q[a][1], s); \
        _Pragma("unroll") for (int e_ = 0; e_ < 16; ++e_) s[e_] = __builtin_amdgcn_exp2f(s[e_]); \
        O[a] = MFMA(asbf(K[2]), asbf(pk8(s, 0)), O[a]); O[a] = MFMA(asbf(K[3]), asbf(pk8(s, 1)), O[a]); }
#pragma unroll 1
    for (int it = 0; it < 4; ++it) {
        const int i0 = it * 4, nx = it < 3 ? i0 + 4 : i0;
        const unsigned fn = fo + ((ptt0 + (nx >> 2)) & 3) * 128;
        LOADKV(kC, i0 + 2);
        TILE(kA);
        LOADKV(kD, i0 + 3);
        const u32x2 fl = ldflag(rws, fn);
        TILE(kB);
        if (!tagok(fl, tag)) poll(rws, fn, tag);
        CBAR();
        LOADKV(kA, nx);
        TILE(kC);
        LOADKV(kB, nx + 1);
        TILE(kD);
    }
    STAMP(7);
    waitc(C_VDONE, 4);
    f32x16 o;
#pragma unroll
    for (int a = 0; a < 4; ++a) {
        if (a != w) {
            const int sl = a * 3 + (w > a ? w - 1 : w);
#pragma unroll
            for (int u = 0; u < 4; ++u) lds_wrf4(L_R2 + (sl * 4 + u) * 1024 + lane * 16, (f32x4){O[a][4 * u], O[a][4 * u + 1], O[a][4 * u + 2], O[a][4 * u + 3]});
        } else o = O[a];
    }
    WAIT_LGKM0(); sig(C_CMB, lane); waitc(C_CMB, 4);
#pragma unroll
    for (int p = 0; p < 3; ++p)
#pragma unroll
        for (int u = 0; u < 4; ++u) {
            const f32x4 s = lds_rdf4(L_R2 + ((w * 3 + p) * 4 + u) * 1024 + lane * 16);
#pragma unroll
            for (int i = 0; i < 4; ++i) o[4 * u + i] += s[i];
        }
    float l = from_hi(o[12]);
    {
        float chk = 0.f;
#pragma unroll
        for (int i = 0; i < 16; ++i) chk = __builtin_fmaf(o[i], 0.f, chk);
        bool bad = !(chk == 0.f) || !(l >= 1e-30f && l <= 1e30f);
        if (__any(bad)) {
            float mrun = -3.0e38f;
            o = f32x16{};
#pragma unroll 1
            for (int rk = 0; rk < 4; ++rk) {
                poll(rws, WS_F1 + (((lane >> 4) & 1) * 8 + pj) * 8192 + ((lane & 15) * 4 + rk) * 128, tag);
#pragma unroll 1
                for (int m2 = 0; m2 < 16; ++m2) {
                    const unsigned so = kvb + (m2 * 4 + rk) * 4096;
                    const u32x4 k0 = ld16(rws, lane * 16, so), k1 = ld16(rws, lane * 16, so + 1024), v0 = ld16(rws, lane * 16, so + 2048), v1 = ld16(rws, lane * 16, so + 3072);
                    f32x16 s = MFMA(asbf(k0), asbf(q0), f32x16{}); s = MFMA(asbf(k1), asbf(q1), s);
                    const float mn = __builtin_fmaxf(mrun, swap_max(max16(s))), al = __builtin_amdgcn_exp2f(mrun - mn);
                    mrun = mn;
#pragma unroll
                    for (int i = 0; i < 16; ++i) { s[i] = __builtin_amdgcn_exp2f(s[i] - mn); o[i] *= al; }
                    o = MFMA(asbf(v0), asbf(pk8(s, 0)), o); o = MFMA(asbf(v1), asbf(pk8(s, 1)), o);
                }
            }
            l = from_hi(o[12]);
        }
    }
    const float rl = __builtin_amdgcn_rcpf(l);
#pragma unroll
    for (int i = 0; i < 16; ++i) o[i] *= rl;
    u32x4 o0 = pk8(o, 0), o1 = pk8(o, 1);
    if (hi) { o1[2] = 0; o1[3] = 0; }
    const unsigned so = WS_O + ((ttg * 16 + head) * 2) * 1024;
    st16(rws, o0, lane * 16, so); st16(rws, o1, lane * 16, so + 1024);
    STAMP(8);
    post(rws, WS_FO + (ttg * 16 + head) * 128, tag, lane);
    STAMP(9);
}

extern "C" void kernel_launch(void* const* d_in, const int* in_sizes, int n_in, void* d_out, int out_size, void* d_ws, size_t ws_size,
                              hipStream_t stream) {
    (void)in_sizes; (void)n_in; (void)out_size; (void)ws_size;
    (void)hipFuncSetAttribute((const void*)fused_fwd, hipFuncAttributeMaxDynamicSharedMemorySize, LDS_BYTES);
    fused_fwd<<<256, 512, LDS_BYTES, stream>>>(d_in[0], d_in[1], d_in[2], d_in[3], d_in[4], d_in[5], d_in[6], d_out, d_ws);
}
```

```cpp
#include <hip/hip_runtime.h>
#include <hip/hip_bf16.h>
#include <stdint.h>
#include <type_traits>

typedef __attribute__((ext_vector_type(8))) __bf16 bf16x8;
typedef __attribute__((ext_vector_type(2))) __bf16 bf16x2;
typedef __attribute__((ext_vector_type(2))) float f32x2;
typedef __attribute__((ext_vector_type(4))) float f32x4;
typedef __attribute__((ext_vector_type(16))) float f32x16;
typedef __attribute__((ext_vector_type(4))) unsigned u32x4;
typedef __attribute__((ext_vector_type(2))) unsigned u32x2;
typedef __attribute__((ext_vector_type(4))) short s16x4;
typedef __amdgpu_buffer_rsrc_t rsrc_t;
#define LAS __attribute__((address_space(3)))
#define DI __device__ __forceinline__

constexpr unsigned WS_EPOCH = 0;
constexpr unsigned WS_F1 = 4096;
constexpr unsigned WS_F2 = WS_F1 + 1024 * 128;
constexpr unsigned WS_F3 = WS_F2 + 512 * 128;
constexpr unsigned WS_FO = WS_F3 + 256 * 128;
constexpr unsigned WS_QKV = 1u << 20;
constexpr unsigned WS_KVP = WS_QKV + (6u << 20);
constexpr unsigned WS_KVR = WS_KVP + (2u << 20);
constexpr unsigned WS_O = WS_KVR + (1u << 20);
constexpr unsigned WS_DBG = WS_O + (4u << 20);
constexpr unsigned WS_BYTES = WS_DBG + (1u << 20);

constexpr unsigned SLOT = 37888;
constexpr unsigned L_R0 = 0;
constexpr unsigned L_R1 = 57856;
constexpr unsigned L_R2 = L_R1 + 32768;
constexpr unsigned L_QX = L_R2 + 49152;
constexpr unsigned L_QB = L_R2;
constexpr unsigned L_KB = L_QB + 16384;
constexpr unsigned L_PHI = 4 * SLOT + 64;
constexpr unsigned L_JUNK = L_PHI + 4096;
constexpr unsigned L_CNT = L_JUNK + 1024;
constexpr unsigned L_TAG = L_CNT + 256;
constexpr unsigned LDS_BYTES = L_TAG + 16;
enum { C_LAND0 = 24, C_DONE0 = 32, C_QB = 8, C_GDONE = 9, C_QX = 10, C_PV = 11, C_KVDONE = 12, C_WO0 = 13, C_WOFREE = 15, C_EX0 = 16, C_CMB = 20, C_ATT = 21 };

#define MAP_SEP 0
constexpr unsigned SPIN_CAP = 1u << 18;
constexpr float QSCALE = 0.18898223650461362f * 1.4426950408889634f;
constexpr float LOG2E = 1.4426950408889634f;
constexpr float REF_SHIFT = 100.f;

#define MFMA(a, b, c) __builtin_amdgcn_mfma_f32_32x32x16_bf16((a), (b), (c), 0, 0, 0)
#define CBAR() asm volatile("" ::: "memory")
#define WAIT_VM(n) asm volatile("s_waitcnt vmcnt(" #n ")" ::: "memory")
#define WAIT_LGKM0() asm volatile("s_waitcnt lgkmcnt(0)" ::: "memory")
#define SBAR() __builtin_amdgcn_sched_barrier(0)

#define STAMP_DECL
#define STAMP(i) do {} while (0)
#define STAMPC(i) do {} while (0)
#define STAMP_FLUSH() do {} while (0)
#define FSTAMP(i) do {} while (0)
#define FSTAMPC(i) do {} while (0)
#define PIN(x) asm volatile("" : "+v"(x))

DI unsigned pk2(float a, float b) { f32x2 v = {a, b}; return __builtin_bit_cast(unsigned, __builtin_convertvector(v, bf16x2)); }
DI u32x4 pk8(const f32x16& a, int s) {
    return (u32x4){pk2(a[8 * s], a[8 * s + 1]), pk2(a[8 * s + 2], a[8 * s + 3]), pk2(a[8 * s + 4], a[8 * s + 5]), pk2(a[8 * s + 6], a[8 * s + 7])}; }
DI bf16x8 asbf(u32x4 v) { return __builtin_bit_cast(bf16x8, v); }
DI float swap_max(float m) { auto r = __builtin_amdgcn_permlane32_swap(__float_as_uint(m), __float_as_uint(m), false, false);
    return __builtin_fmaxf(__uint_as_float(r[0]), __uint_as_float(r[1])); }
DI float swap_min(float m) { auto r = __builtin_amdgcn_permlane32_swap(__float_as_uint(m), __float_as_uint(m), false, false);
    return __builtin_fminf(__uint_as_float(r[0]), __uint_as_float(r[1])); }
DI float swap_sum(float m) { auto r = __builtin_amdgcn_permlane32_swap(__float_as_uint(m), __float_as_uint(m), false, false);
    return __uint_as_float(r[0]) + __uint_as_float(r[1]); }
DI float from_hi(float m) { auto r = __builtin_amdgcn_permlane32_swap(__float_as_uint(m), __float_as_uint(m), false, false);
    return __uint_as_float(r[1]); }
DI float max16(const f32x16& s) {
    float a = __builtin_fmaxf(__builtin_fmaxf(s[0], s[1]), s[2]), b = __builtin_fmaxf(__builtin_fmaxf(s[3], s[4]), s[5]);
    a = __builtin_fmaxf(__builtin_fmaxf(a, s[6]), s[7]); b = __builtin_fmaxf(__builtin_fmaxf(b, s[8]), s[9]);
    a = __builtin_fmaxf(__builtin_fmaxf(a, s[10]), s[11]); b = __builtin_fmaxf(__builtin_fmaxf(b, s[12]), s[13]);
    a = __builtin_fmaxf(__builtin_fmaxf(a, s[14]), s[15]); return __builtin_fmaxf(a, b); }
DI float min16(const f32x16& s) {
    float a = __builtin_fminf(__builtin_fminf(s[0], s[1]), s[2]), b = __builtin_fminf(__builtin_fminf(s[3], s[4]), s[5]);
    a = __builtin_fminf(__builtin_fminf(a, s[6]), s[7]); b = __builtin_fminf(__builtin_fminf(b, s[8]), s[9]);
    a = __builtin_fminf(__builtin_fminf(a, s[10]), s[11]); b = __builtin_fminf(__builtin_fminf(b, s[12]), s[13]);
    a = __builtin_fminf(__builtin_fminf(a, s[14]), s[15]); return __builtin_fminf(a, b); }

DI void dma(rsrc_t rs, unsigned voff, unsigned soff, unsigned lds) {
    asm volatile("s_mov_b32 m0, %0\n\ts_nop 0\n\tbuffer_load_dwordx4 %1, %2, %3 offen lds" :: "s"(lds), "v"(voff), "s"(rs), "s"(soff) : "memory"); }
DI u32x4 lds_rd128(unsigned a) { return *(LAS const u32x4*)(uintptr_t)a; }
DI f32x4 lds_rdf4(unsigned a) { return *(LAS const f32x4*)(uintptr_t)a; }
DI void lds_wr128(unsigned a, u32x4 v) { *(LAS u32x4*)(uintptr_t)a = v; }
DI void lds_wrf4(unsigned a, f32x4 v) { *(LAS f32x4*)(uintptr_t)a = v; }
DI void lds_wr64(unsigned a, u32x2 v) { *(LAS u32x2*)(uintptr_t)a = v; }
DI bf16x8 lds_tr2(unsigned a0, unsigned a1) {
    s16x4 lo = __builtin_amdgcn_ds_read_tr16_b64_v4i16((LAS s16x4*)(uintptr_t)a0), hi = __builtin_amdgcn_ds_read_tr16_b64_v4i16((LAS s16x4*)(uintptr_t)a1);
    return __builtin_bit_cast(bf16x8, __builtin_shufflevector(lo, hi, 0, 1, 2, 3, 4, 5, 6, 7)); }
DI void sig(int c, int) {
    asm volatile("s_mov_b64 exec, 1\n\tds_add_u32 %0, %1\n\ts_mov_b64 exec, -1" :: "v"(L_CNT + 4 * c), "v"(1u) : "memory"); }
template <int SLP = 1> DI void waitc(int c, unsigned n) {
    unsigned v, sv, it;
    asm volatile("s_mov_b32 %2, %5\n"
                 "1:\n\tds_read_b32 %0, %3\n\ts_waitcnt lgkmcnt(0)\n\tv_readfirstlane_b32 %1, %0\n\ts_cmp_ge_u32 %1, %4\n\ts_cbranch_scc1 2f\n\t"
                 "s_sleep %6\n\ts_sub_u32 %2, %2, 1\n\ts_cbranch_scc0 1b\n2:"
                 : "=&v"(v), "=&s"(sv), "=&s"(it) : "v"(L_CNT + 4 * c), "i"(n), "i"(SPIN_CAP), "i"(SLP) : "memory", "scc"); }
DI unsigned rd_cnt(int c) { return *(volatile LAS const unsigned*)(uintptr_t)(L_CNT + 4 * c); }
DI u32x2 rd_tag() { const u32x2 t = *(LAS const u32x2*)(uintptr_t)L_TAG;
    return (u32x2){(unsigned)__builtin_amdgcn_readfirstlane(t[0]), (unsigned)__builtin_amdgcn_readfirstlane(t[1])}; }
DI u32x4 ld16(rsrc_t rs, unsigned voff, unsigned soff) { return __builtin_amdgcn_raw_buffer_load_b128(rs, voff, soff, 16); }
template <int AUX> DI void st16a(rsrc_t rs, u32x4 v, unsigned voff, unsigned soff) {
    __builtin_amdgcn_raw_buffer_store_b128(v, rs, voff, soff, AUX); asm volatile("s_nop 1" :: "v"(v) : "memory"); }
DI void st16(rsrc_t rs, u32x4 v, unsigned voff, unsigned soff) { st16a<16>(rs, v, voff, soff); }
DI u32x2 ldflag(rsrc_t rs, unsigned voff) { return __builtin_amdgcn_raw_buffer_load_b64(rs, voff, 0, 16); }
DI bool tagok(u32x2 f, u32x2 tag) {
    return __builtin_amdgcn_ballot_w64(__builtin_bit_cast(unsigned long long, f) == __builtin_bit_cast(unsigned long long, tag)) == ~0ull; }
DI void poll(rsrc_t rs, unsigned voff, u32x2 tag) {
    CBAR();
#pragma clang loop unroll(disable)
    for (unsigned it = 0; it < SPIN_CAP; ++it) { u32x2 f = ldflag(rs, voff); if (tagok(f, tag)) break; __builtin_amdgcn_s_sleep(2); CBAR(); }
    CBAR(); }
DI void post(rsrc_t rs, unsigned off, u32x2 tag, int lane) {
    WAIT_VM(0);
    if (lane == 0) __builtin_amdgcn_raw_buffer_store_b64(tag, rs, off, 0, 16);
    CBAR(); }

DI u32x4 lds_tr2u(unsigned a) { return __builtin_bit_cast(u32x4, lds_tr2(a, a + 336)); }
DI void sigv(unsigned addr) { asm volatile("s_mov_b64 exec, 1\n\tds_add_u32 %0, %1\n\ts_mov_b64 exec, -1" :: "v"(addr), "v"(1u) : "memory"); }
template <int ND, class Start>
DI void gemm6(unsigned xa0, unsigned wa0, unsigned wc0, f32x16 (&acc)[6], Start start) {
    u32x4 f[2][3], x[2][2];
    unsigned xa = xa0, x1 = xa0 ^ 32, x2, x3, wa = wa0, wc = wc0, wan, wcn, ca = L_CNT + 4 * C_LAND0, cv, so = 0, rf;
#pragma unroll
    for (int i = 0; i < 6; ++i) { acc[i] = f32x16{}; PIN(acc[i]); }
    PIN(xa); PIN(x1); PIN(wa); PIN(wc); PIN(ca);
    start();
    waitc<0>(C_LAND0, 4);
    x[0][0] = lds_rd128(xa); SBAR(); f[0][0] = lds_tr2u(wa); SBAR(); x[0][1] = lds_rd128(xa + 4096); SBAR();
    f[0][1] = lds_tr2u(wa + 56); SBAR(); f[0][2] = lds_tr2u(wc); SBAR();
#define G6MM(c, t, i) acc[3 * t + i] = (i >= 3 - ND) ? MFMA(asbf(x[c][t]), asbf(f[c][i]), acc[3 * t + i]) : MFMA(asbf(f[c][i]), asbf(x[c][t]), acc[3 * t + i]); SBAR()
#define G6STEP(c, XA, WA, WC, P0, P2, P5) do { \
        G6MM(c, 0, 0); P0; x[1 - c][0] = lds_rd128(XA); SBAR(); \
        G6MM(c, 1, 0); f[1 - c][0] = lds_tr2u(WA); SBAR(); \
        G6MM(c, 0, 1); x[1 - c][1] = lds_rd128((XA) + 4096); P2; SBAR(); \
        G6MM(c, 1, 1); f[1 - c][1] = lds_tr2u((WA) + 56); SBAR(); \
        G6MM(c, 0, 2); f[1 - c][2] = lds_tr2u(WC); SBAR(); \
        G6MM(c, 1, 2); P5; SBAR(); } while (0)
#define SPIN(x) asm volatile("" : "+s"(x))
#pragma unroll 1
    for (int g = 1; g < 8; ++g) {
        G6STEP(0, x1, wa + 5376, wc + 5376, (void)0, { x2 = xa ^ 64; PIN(x2); }, { x3 = xa ^ 96; PIN(x3); so = (g & 3) * SLOT; SPIN(so); });
        G6STEP(1, x2, wa + 2 * 5376, wc + 2 * 5376, (void)0, { xa = xa0 + so; PIN(xa); },
               { cv = *(volatile LAS const unsigned*)(uintptr_t)(ca + 4); wan = wa0 + so; PIN(wan); });
        G6STEP(0, x3, wa + 3 * 5376, wc + 3 * 5376, (void)0, { wcn = wc0 + so; PIN(wcn); }, { rf = __builtin_amdgcn_readfirstlane(cv); SPIN(rf); x1 = xa ^ 32; PIN(x1); });
        G6STEP(1, xa, wan, wcn, { if (__builtin_expect(rf < 4u, 0)) waitc<0>(C_LAND0 + g, 4); CBAR(); }, { ca += 4; PIN(ca); },
               sigv(ca + 4 * (C_DONE0 - C_LAND0 - 1)));
        wa = wan; wc = wcn;
    }
}
DI unsigned tr_addr128(unsigned base, int cb, int rowgrp, int lane) {
    const int q = (lane & 15) >> 2, byte = cb + 32 * ((lane >> 4) & 1) + 8 * (lane & 3);
    return base + (rowgrp + q) * 128 + ((((byte >> 4) ^ ((q >> 1) << 2)) & 7) << 4) + (byte & 8);
}
DI void feat(f32x16& p, f32x16& e, f32x16& g, bool hi) {
#pragma unroll
    for (int i = 12; i < 16; ++i) p[i] = hi ? p[0] : p[i];
    const float mx = swap_max(max16(p)) * LOG2E, mn = swap_min(min16(p)) * LOG2E;
#pragma unroll
    for (int i = 0; i < 16; i += 2) {
        const f32x2 pp = {p[i], p[i + 1]};
        const f32x2 a = __builtin_elementwise_fma(pp, (f32x2){LOG2E, LOG2E}, (f32x2){-mx, -mx}), c = __builtin_elementwise_fma(pp, (f32x2){-LOG2E, -LOG2E}, (f32x2){mn, mn});
        e[i] = __builtin_amdgcn_exp2f(a[0]); e[i + 1] = __builtin_amdgcn_exp2f(a[1]); g[i] = __builtin_amdgcn_exp2f(c[0]); g[i + 1] = __builtin_amdgcn_exp2f(c[1]);
    }
}

typedef __attribute__((ext_vector_type(16))) unsigned u32x16;
DI rsrc_t mk_rsrc(unsigned lo, unsigned hi, unsigned bytes) {
    return __builtin_amdgcn_make_buffer_rsrc((void*)(((unsigned long long)hi << 32) | lo), 0, bytes, 0x00020000); }
DI void read_wo(bf16x8 (&wf)[16][2], int kh, int rot, int lane) {
    const unsigned ta0 = tr_addr128(L_R0, 0, kh * 224 + 4 * (lane >> 5), lane), ta1 = tr_addr128(L_R0, 64, kh * 224 + 4 * (lane >> 5), lane);
#pragma unroll
    for (int k = 0; k < 8; ++k) {
        const unsigned ro = ((k + rot) & 7) * 28 * 128, b0 = ta0 + ro, b1 = ta1 + ro;
        wf[2 * k][0] = lds_tr2(b0, b0 + 1024); wf[2 * k][1] = lds_tr2(b1, b1 + 1024);
        wf[2 * k + 1][0] = lds_tr2(b0 + 2048, b0 + 3072); wf[2 * k + 1][1] = lds_tr2(b1 + 2048, b1 + 3072);
    }
}
DI u32x4 ones_row(u32x4 v, int lane) {
    if ((lane & 31) >= 28) { const unsigned c = (lane & 31) == 28 ? 0x3F803F80u : 0u; v = (u32x4){c, c, c, c}; }
    return v; }
DI f32x16 phi_pre(u32x4 k0, u32x4 k1, int hh, int lane, bool hi) {
    if (hi) { k1[2] = 0; k1[3] = 0; }
    const unsigned pb = L_PHI + hh * 1568 + (4 * (lane >> 5) + ((lane & 15) >> 2)) * 56 + 32 * ((lane >> 4) & 1) + 8 * (lane & 3);
    bf16x8 f0 = lds_tr2(pb, pb + 8 * 56);
    u32x4 f1 = __builtin_bit_cast(u32x4, lds_tr2(pb + 16 * 56, pb + 24 * 56));
    if (hi) { f1[2] = 0; f1[3] = 0; }
    f32x16 p = MFMA(f0, asbf(k0), f32x16{});
    return MFMA(asbf(f1), asbf(k1), p);
}

__global__ __launch_bounds__(512, 2) void fused_fwd(const void*, const void*, const void*, const void*, const void*, const void*, const void*, void*, void*) {
    STAMP_DECL; STAMP(0); STAMPC(30);
    u32x16 ka; u32x2 kb;
    asm volatile("s_load_dwordx16 %0, %2, 0x0\n\ts_load_dwordx2 %1, %2, 0x40" : "=&s"(ka), "=&s"(kb) : "s"(__builtin_amdgcn_kernarg_segment_ptr()));
    const int tid = threadIdx.x, lane = tid & 63, wid = __builtin_amdgcn_readfirstlane(tid >> 6);
    const int b = blockIdx.x, xcd = b & 7, r = b >> 3, w = wid & 3, role = wid >> 2;
    const int isb = r & 1, ml = (r >> 1) & 3, nl = r >> 3;
    const int mt = (xcd & 3) * 4 + ml, nt = (xcd >> 2) * 4 + nl;
    const bool hi = lane >= 32;
    int g0 = 2 * ml + nl; g0 -= (g0 >= 7) ? 7 : 0;
    const int ttg = mt * 4 + w;
    const int head = 2 * xcd + (r >> 4), qt = r & 15;
    const int ct = r >> 2, rt = xcd * 4 + (r & 3);
    u32x2 tag;
    bf16x8 wf[16][2];
    unsigned pfd = 0;

    unsigned xvo, wvo[6];
    if (role) {
        __builtin_amdgcn_s_setprio(2);
        xvo = (w * 8 + (lane >> 3)) * 896 + ((((lane & 7) ^ (lane >> 4) ^ ((w & 1) << 2)) & 7) << 4);
        PIN(xvo);
#pragma unroll
        for (int i = 0; i < 6; ++i) {
            const int c = 64 * (w + 4 * i) + lane, rho = (c * 3121) >> 16, ch = c - 21 * rho, win = (ch * 37) >> 8;
            const int krow = (rho & ~15) | ((rho & 3) << 2) | ((rho >> 2) & 3);
            wvo[i] = krow * 2688 + ch * 16 + (isb ? win * (896 - 112) : 0);
            PIN(wvo[i]);
        }
    }
    if (wid == 0) {
        unsigned long long pc; unsigned len;
        asm volatile("s_getpc_b64 %0\n.Lpf_begin%=:\n\ts_mov_b32 %1, .Lpf_end-.Lpf_begin%=" : "=s"(pc), "=s"(len));
        const rsrc_t rc = __builtin_amdgcn_make_buffer_rsrc((void*)pc, 0, len, 0x00020000);
        asm volatile("s_nop 4\n\tbuffer_load_dword %0, %1, %2, 0 offen" : "+v"(pfd) : "v"(lane < 12 ? (r * 12 + lane) * 128 : 0x7FFFFFF0), "s"(rc) : "memory");
        *(LAS unsigned*)(uintptr_t)(L_CNT + 4 * lane) = lane == C_LAND0 + 7 ? 4u : 0u;
    }
    asm volatile("s_waitcnt lgkmcnt(0)" : "+s"(ka), "+s"(kb) :: "memory");
    const rsrc_t rws = mk_rsrc(kb[0], kb[1], WS_BYTES);
    u32x4 pe[2][4];
    if (role) {
        const rsrc_t rx = mk_rsrc(isb ? ka[6] : ka[0], isb ? ka[7] : ka[1], 2048 * 896), rw = mk_rsrc(isb ? ka[8] : ka[2], isb ? ka[9] : ka[3], 448 * 2688);
        const unsigned xs0 = mt * 114688, ws0 = (isb ? 112 : 336) * nt;
        auto feed = [&](int g) {
            int ga = g + g0; ga -= (ga >= 7) ? 7 : 0;
            const unsigned xs = xs0 + ga * 128, ws = ws0 + ga * 64 * 2688, l = (g & 3) * SLOT + w * 1024;
#pragma unroll
            for (int j = 0; j < 4; ++j) dma(rx, xvo, xs + j * 32 * 896, l + j * 4096);
#pragma unroll
            for (int j = 0; j < 5; ++j) dma(rw, wvo[j], ws, l + 16384 + j * 4096);
            if (w == 0) dma(rw, wvo[5], ws, l + 16384 + 5 * 4096); };
        FSTAMP(18);
        dma(mk_rsrc(ka[10], ka[11], 16 * 1568), lane * 16, nt * 3136 + w * 1024, L_PHI + w * 1024);
        feed(0); feed(1);
        asm volatile("s_barrier" ::: "memory");
#pragma unroll 1
        for (int g = 0; g < 6; ++g) {
            if (w == 0) WAIT_VM(10); else WAIT_VM(9);
            sig(C_LAND0 + g, lane);
            if (g == 0) STAMP(1);
            if (g < 5) {
                if (g >= 2) waitc<0>(C_DONE0 + g - 2, 4);
                feed(g + 2);
            }
        }
        {
            const rsrc_t rt_ = mk_rsrc(w < 2 ? ka[4] : ka[12], w < 2 ? ka[5] : ka[13], 448 * 896);
            asm volatile("s_mov_b32 m0, %0\n\ts_nop 3\n\tbuffer_load_dword %1, %2, 0 offen lds"
                         :: "s"(L_JUNK), "v"(lane < 49 ? (r * 98 + (w & 1) * 49 + lane) * 128 : 0x7FFFFF00), "s"(rt_) : "memory");
        }
        WAIT_VM(1);
        sig(C_LAND0 + 6, lane);
        STAMP(2);
        tag = rd_tag();
        if (isb) {
            waitc<1>(C_QB + 0, 4);
#pragma unroll
            for (int hh = 0; hh < 2; ++hh) {
                const unsigned qa = L_QB + ((w * 2 + hh) * 2) * 1024 + lane * 16;
                f32x16 p = phi_pre(lds_rd128(qa), lds_rd128(qa + 1024), hh, lane, hi), e, g;
                feat(p, e, g, hi);
                pe[hh][0] = pk8(e, 0); pe[hh][1] = pk8(e, 1); pe[hh][2] = pk8(g, 0); pe[hh][3] = pk8(g, 1);
            }
        }
        STAMP(3);
    } else {
        f32x16 acc[6];
        const int tp = w & 1, sg = w >> 1;
        const int rr = tp * 64 + (lane & 31);
        const unsigned xa0 = rr * 128 + ((((lane >> 5) ^ (rr >> 1)) & 7) << 4);
        const unsigned lb = 16384 + (4 * ((lane & 15) >> 2) + 2 * (lane >> 5)) * 336 + 32 * ((lane >> 4) & 1) + 8 * (lane & 3);
        const unsigned wa0 = lb + 56 * (isb ? 2 - 2 * sg : 3 * sg), wc0 = lb + 56 * (isb ? 4 + sg : 3 * sg + 2);
        const int G = 2 * nt + sg;
        if (w == 0) {
            const u32x2 ep = ldflag(rws, WS_EPOCH);
            const unsigned long long e = (((unsigned long long)ep[1] << 32) | ep[0]) + 1;
            lds_wr64(L_TAG, (u32x2){(unsigned)e, (unsigned)(e >> 32)});
            WAIT_VM(0);
            asm volatile("" :: "v"(pfd));
        }
        auto start = [&]() { asm volatile("s_waitcnt lgkmcnt(0)\n\ts_barrier" ::: "memory"); FSTAMPC(22); FSTAMPC(23); };
        if (isb || G == 10) gemm6<1>(xa0, wa0, wc0, acc, start); else if (G < 10) gemm6<0>(xa0, wa0, wc0, acc, start); else gemm6<3>(xa0, wa0, wc0, acc, start);
        tag = rd_tag();
        FSTAMPC(24);
        STAMP(2);
        if (!isb) {
#pragma unroll
            for (int i = 0; i < 3; ++i) {
                const int sl = 3 * G + i, kind = sl >> 4;
#pragma unroll
                for (int t = 0; t < 2; ++t) {
                    f32x16& a = acc[3 * t + i];
                    if (kind == 0) {
#pragma unroll
                        for (int k = 0; k < 16; ++k) a[k] *= QSCALE;
                    }
                    u32x4 v0 = pk8(a, 0), v1 = pk8(a, 1);
                    if (kind == 2) { v0 = ones_row(v0, lane); v1 = ones_row(v1, lane); }
                    else if (hi) { v1[2] = kind ? 0x00003F80u : 0u; v1[3] = 0u; }
                    const unsigned so = WS_QKV + (((sl & 15) * 64 + mt * 4 + 2 * tp + t) * 6 + kind * 2) * 1024;
                    st16(rws, v0, lane * 16, so); st16(rws, v1, lane * 16 + 1024, so);
                }
            }
            post(rws, WS_F1 + (G * 32 + mt * 2 + tp) * 128, tag, lane);
            waitc<0>(C_DONE0 + 6, 4);
        } else {
            waitc<0>(C_DONE0 + 6, 4);
            u32x4 kf[2][2];
#pragma unroll
            for (int t = 0; t < 2; ++t)
#pragma unroll
                for (int s = 0; s < 2; ++s) {
#pragma unroll
                    for (int hh = 0; hh < 2; ++hh) {
                        const u32x4 v = pk8(acc[3 * t + hh], s);
                        if (t == 0) kf[hh][s] = v;
                        if (sg) lds_wr128(L_QB + (((2 * tp + t) * 2 + hh) * 2 + s) * 1024 + lane * 16, v);
                        else if (t == 1) lds_wr128(L_KB + ((tp * 2 + hh) * 2 + s) * 1024 + lane * 16, v);
                    }
                    lds_wr128(L_R0 + ((sg * 4 + 2 * tp + t) * 2 + s) * 1024 + lane * 16, ones_row(pk8(acc[3 * t + 2], s), lane));
                }
            WAIT_LGKM0(); sig(C_QB, lane);
            if (sg) {
                waitc<0>(C_QB, 4);
#pragma unroll
                for (int hh = 0; hh < 2; ++hh)
#pragma unroll
                    for (int s = 0; s < 2; ++s) kf[hh][s] = lds_rd128(L_KB + ((tp * 2 + hh) * 2 + s) * 1024 + lane * 16);
            }
            const int tl = 2 * tp + sg;
            __builtin_amdgcn_s_setprio(3);
            const int tk = lane & 31, pi = (((tk >> 1) & 1) << 3) | (tk & 1) | (((tk >> 2) & 3) << 1);
#pragma unroll
            for (int hh = 0; hh < 2; ++hh) {
                f32x16 p = phi_pre(kf[hh][0], kf[hh][1], hh, lane, hi), e, g;
                feat(p, e, g, hi);
#pragma unroll
                for (int i = 12; i < 16; ++i) { e[i] = hi ? 0.f : e[i]; g[i] = hi ? 0.f : g[i]; }
                float se = 0.f, sg = 0.f;
#pragma unroll
                for (int i = 0; i < 16; ++i) { se += e[i]; sg += g[i]; }
                se = __builtin_amdgcn_rcpf(swap_sum(se)); sg = __builtin_amdgcn_rcpf(swap_sum(sg));
#pragma unroll
                for (int u = 0; u < 8; ++u) {
                    const int piece = 8 * (u >> 2) + 2 * (u & 3) + (lane >> 5), k = 4 * (u & 3);
                    const f32x16& z = (u >> 2) ? g : e; const float sc = (u >> 2) ? sg : se;
                    lds_wr64(L_R1 + hh * 16384 + (tl * 32 + tk) * 128 + ((piece ^ pi) << 3), (u32x2){pk2(z[k] * sc, z[k + 1] * sc), pk2(z[k + 2] * sc, z[k + 3] * sc)});
                }
            }
            WAIT_LGKM0(); sig(C_PV, lane);
            __builtin_amdgcn_s_setprio(0);
        }
        STAMP(3);
    }
    FSTAMP(4);

    if (role) {
        const int wc = (lane & 7) ^ (((lane >> 4) & 1) << 2);
        const unsigned wovo = (lane >> 3) * 896 + wc * 16;
        if (isb) {
            waitc<0>(C_PV, 4);
            const int hh = w >> 1, ft = w & 1, q = (lane & 15) >> 2, P = 8 * ft + 4 * ((lane >> 4) & 1) + (lane & 3);
            bf16x8 fa[8], fb[8];
#pragma unroll
            for (int st = 0; st < 8; ++st) {
                const int t0 = 16 * (st & 1) + 4 * (lane >> 5) + q, t1 = t0 + 8;
                const int p0 = (((t0 >> 1) & 1) << 3) | (t0 & 1) | (((t0 >> 2) & 3) << 1), p1 = (((t1 >> 1) & 1) << 3) | (t1 & 1) | (((t1 >> 2) & 3) << 1);
                const unsigned ib = L_R1 + hh * 16384 + (st >> 1) * 4096;
                fa[st] = lds_tr2(ib + t0 * 128 + ((P ^ p0) << 3), ib + t1 * 128 + ((P ^ p1) << 3));
                fb[st] = asbf(lds_rd128(L_R0 + ((hh * 4 + (st >> 1)) * 2 + (st & 1)) * 1024 + lane * 16));
            }
            SBAR();
            f32x16 kv = {};
#pragma unroll
            for (int st = 0; st < 8; ++st) kv = MFMA(fa[st], fb[st], kv);
            WAIT_LGKM0(); sig(C_KVDONE, lane);
            const int hk = 2 * nt + hh;
#pragma unroll
            for (int u = 0; u < 4; ++u)
                st16(rws, __builtin_bit_cast(u32x4, (f32x4){kv[4 * u], kv[4 * u + 1], kv[4 * u + 2], kv[4 * u + 3]}), lane * 16 + u * 1024, WS_KVP + ((hk * 16 + mt) * 8 + ft * 4) * 1024);
            FSTAMP(19);
            post(rws, WS_F2 + ((hk * 16 + mt) * 2 + ft) * 128, tag, lane);
            waitc<0>(C_KVDONE, 4);
        }
        STAMP(5);
        if (r < 28) {
            waitc<4>(C_QX, 4);
            if (w == 0) *(LAS u32x2*)(uintptr_t)(L_R0 + 57344 + 8 * lane) = (u32x2){0u, 0u};
            const rsrc_t rwob = mk_rsrc(ka[12], ka[13], 448 * 896);
#pragma unroll
            for (int i = 0; i < 14; ++i) dma(rwob, wovo, ct * 128 + (w + 4 * i) * 8 * 896, L_R0 + (w + 4 * i) * 1024);
            WAIT_VM(0); WAIT_LGKM0(); sig(C_WO0 + 1, lane); waitc<2>(C_WO0 + 1, 4);
            read_wo(wf, w >> 1, ct, lane);
            WAIT_LGKM0(); sig(C_WOFREE, lane);
        }
        if (w == 3) {
            const int hr = head, i = lane & 15, pg = lane >> 4;
            poll(rws, WS_F2 + ((hr * 16 + i) * 2 + (qt >> 3)) * 128, tag);
            u32x4 pl[8];
#pragma unroll
            for (int p = 0; p < 4; ++p) {
                const unsigned vo = (pg * 4 + p) * 8192 + (16 * (qt & 3) + i) * 16, so = WS_KVP + hr * 131072 + (qt >> 2) * 2048;
                pl[2 * p] = ld16(rws, vo, so); pl[2 * p + 1] = ld16(rws, vo + 1024, so);
            }
            SBAR();
            f32x4 s0 = {}, s1 = {};
#pragma unroll
            for (int p = 0; p < 4; ++p) { s0 += __builtin_bit_cast(f32x4, pl[2 * p]); s1 += __builtin_bit_cast(f32x4, pl[2 * p + 1]); }
            float v[8] = {s0[0], s0[1], s0[2], s0[3], s1[0], s1[1], s1[2], s1[3]};
#pragma unroll
            for (int k = 0; k < 8; ++k) {
                auto t = __builtin_amdgcn_permlane16_swap(__float_as_uint(v[k]), __float_as_uint(v[k]), false, false);
                v[k] = swap_sum(__uint_as_float(t[0]) + __uint_as_float(t[1]));
            }
            u32x4 o = {pk2(v[0], v[1]), pk2(v[2], v[3]), pk2(v[4], v[5]), pk2(v[6], v[7])};
            const int cl = 16 * (qt & 3) + i;
            if ((cl & 31) == 29) { const unsigned c = ((qt >> 2) & 1) && cl >= 32 ? 0u : 0x3F803F80u; o = (u32x4){0x3F803F80u, 0x3F803F80u, c, c}; }
            st16(rws, o, lane < 16 ? i * 16 : 0x7FFFFF00u, WS_KVR + (hr * 256 + qt * 16) * 16);
            post(rws, WS_F3 + (hr * 16 + qt) * 128, tag, lane);
        }
        STAMP(6);
        if (r < 28) {
            const rsrc_t rwoa = mk_rsrc(ka[4], ka[5], 448 * 896);
            waitc<2>(C_WOFREE, 4);
#pragma unroll
            for (int i = 0; i < 14; ++i) dma(rwoa, wovo, ct * 128 + (w + 4 * i) * 8 * 896, L_R0 + (w + 4 * i) * 1024);
            WAIT_VM(0); sig(C_WO0, lane);
        }
        if (isb) {
            poll(rws, WS_F3 + (2 * nt * 16 + (lane & 31)) * 128, tag);
            u32x4 kf[2][4];
#pragma unroll
            for (int hh = 0; hh < 2; ++hh)
#pragma unroll
                for (int f = 0; f < 4; ++f) kf[hh][f] = ld16(rws, lane * 16 + f * 1024, WS_KVR + (2 * nt + hh) * 4096);
            SBAR();
#pragma unroll
            for (int hh = 0; hh < 2; ++hh) {
                f32x16 np = MFMA(asbf(kf[hh][0]), asbf(pe[hh][0]), f32x16{}); np = MFMA(asbf(kf[hh][1]), asbf(pe[hh][1]), np);
                f32x16 nm = MFMA(asbf(kf[hh][2]), asbf(pe[hh][2]), f32x16{}); nm = MFMA(asbf(kf[hh][3]), asbf(pe[hh][3]), nm);
                const float rp = __builtin_amdgcn_rcpf(np[13]), rm = __builtin_amdgcn_rcpf(nm[13]);
                const float rd = __builtin_amdgcn_rcpf(__builtin_fmaf(rp, np[12], rm * nm[12]) + 1e-6f);
                const float cp = from_hi(rp * rd), cm = from_hi(rm * rd);
                f32x16 o;
#pragma unroll
                for (int i = 0; i < 16; ++i) o[i] = __builtin_fmaf(cp, np[i], cm * nm[i]);
                u32x4 o0 = pk8(o, 0), o1 = pk8(o, 1);
                if (hi) { o1[2] = 0; o1[3] = 0; }
                const unsigned so = WS_O + (2u << 20) + ((ttg * 16 + 2 * nt + hh) * 2) * 1024;
                st16(rws, o0, lane * 16, so); st16(rws, o1, lane * 16 + 1024, so);
            }
            WAIT_VM(0);
            if (lane < 2) __builtin_amdgcn_raw_buffer_store_b64(tag, rws, WS_FO + (1024 + ttg * 16 + 2 * nt + lane) * 128, 0, 16);
            CBAR();
        }
        STAMP(7);
        if (b == 255 && w == 0) {
#pragma unroll 1
            for (int i = 0; i < 4; ++i) poll(rws, WS_F3 + (i * 64 + lane) * 128, tag);
            if (lane == 0) __builtin_amdgcn_raw_buffer_store_b64(tag, rws, WS_EPOCH, 0, 16);
        }
    } else {
        const int tq = qt * 4 + w;
        const int gq = (head * 43) >> 7, gk = ((16 + head) * 43) >> 7, gv = ((32 + head) * 43) >> 7;
        poll(rws, WS_F1 + (lane >= 16 ? gq * 32 + (tq >> 1) : ((lane & 8) ? gv : gk) * 32 + 8 * w + (lane & 7)) * 128, tag);
        STAMP(5);
        const unsigned hb = WS_QKV + head * 64 * 6144, kvb = hb + 2048;
        const unsigned kb0 = kvb + (16 * w + (qt & 3)) * 6144, kb1 = kvb + (16 * w + ((qt + 1) & 3)) * 6144,
                       kb2 = kvb + (16 * w + ((qt + 2) & 3)) * 6144, kb3 = kvb + (16 * w + ((qt + 3) & 3)) * 6144;
        const int rk0 = qt >> 2;
        unsigned vc = lane * 16 + rk0 * 24576;
        u32x4 kA[4], kB[4], kC[4], kD[4];
#define LD(d, f, v, sb) d[f] = ld16(rws, (v) + (f) * 1024, sb)
        u32x4 q0 = ld16(rws, lane * 16, hb + tq * 6144), q1 = ld16(rws, lane * 16 + 1024, hb + tq * 6144);
        LD(kA, 0, vc, kb0); LD(kA, 1, vc, kb0);
        SBAR();
        f32x16 O[4] = {};
        u32x4 pA0 = {}, pA1 = {}, pB0, pB1;
        kD[2] = (u32x4){}; kD[3] = (u32x4){};
        PIN(O[0]); PIN(O[1]); PIN(O[2]); PIN(O[3]); PIN(pA0); PIN(pA1); PIN(kD[2]); PIN(kD[3]);
        SBAR();
        bf16x8 Q[4][2];
        {
            f32x16 s = MFMA(asbf(kA[0]), asbf(q0), f32x16{}); s = MFMA(asbf(kA[1]), asbf(q1), s);
            SBAR(); FSTAMP(13);
            LD(kA, 2, vc, kb0); LD(kA, 3, vc, kb0); LD(kB, 0, vc, kb1); LD(kB, 1, vc, kb1); LD(kB, 2, vc, kb1); LD(kB, 3, vc, kb1);
            LD(kC, 0, vc, kb2); LD(kC, 1, vc, kb2); LD(kC, 2, vc, kb2); LD(kC, 3, vc, kb2);
            SBAR();
            lds_wr128(L_QX + (w * 2) * 1024 + lane * 16, q0);
            const float m = swap_max(max16(s)) + REF_SHIFT;
            u32x4 q1m = q1;
            if (hi) q1m[2] = pk2(-m, 0.f);
            lds_wr128(L_QX + (w * 2 + 1) * 1024 + lane * 16, q1m);
            WAIT_LGKM0(); sig(C_QX, lane); FSTAMP(14); waitc<0>(C_QX, 4);
#pragma unroll
            for (int a = 0; a < 4; ++a) { Q[a][0] = asbf(lds_rd128(L_QX + (a * 2) * 1024 + lane * 16)); Q[a][1] = asbf(lds_rd128(L_QX + (a * 2 + 1) * 1024 + lane * 16)); }
        }
        STAMP(6); STAMPC(20);
        f32x16 sA, sB;
        sA = MFMA(asbf(kA[0]), Q[0][0], f32x16{}); sA = MFMA(asbf(kA[1]), Q[0][1], sA);
        PIN(Q[1][0]); PIN(Q[1][1]); PIN(Q[2][0]); PIN(Q[2][1]); PIN(Q[3][0]); PIN(Q[3][1]);
#define EX4(S, i) S[i] = __builtin_amdgcn_exp2f(S[i]); S[i + 1] = __builtin_amdgcn_exp2f(S[i + 1]); S[i + 2] = __builtin_amdgcn_exp2f(S[i + 2]); S[i + 3] = __builtin_amdgcn_exp2f(S[i + 3])
#define STAGE(SC, SN, PC0, PC1, PP0, PP1, KN, an, VP, ap, F0, F1, F2, F3) do { \
        SN = MFMA(asbf(KN[0]), Q[an][0], f32x16{}); SBAR(); F0; SBAR(); EX4(SC, 0); PIN(SC); SBAR(); \
        SN = MFMA(asbf(KN[1]), Q[an][1], SN); SBAR(); F1; SBAR(); EX4(SC, 4); PC0[0] = pk2(SC[0], SC[1]); PC0[1] = pk2(SC[2], SC[3]); PIN(SC); PIN(PC0); SBAR(); \
        O[ap] = MFMA(asbf(VP[2]), asbf(PP0), O[ap]); SBAR(); F2; SBAR(); EX4(SC, 8); PC0[2] = pk2(SC[4], SC[5]); PC0[3] = pk2(SC[6], SC[7]); PIN(SC); PIN(PC0); SBAR(); \
        O[ap] = MFMA(asbf(VP[3]), asbf(PP1), O[ap]); SBAR(); F3; SBAR(); EX4(SC, 12); PC1[0] = pk2(SC[8], SC[9]); PC1[1] = pk2(SC[10], SC[11]); \
        PC1[2] = pk2(SC[12], SC[13]); PC1[3] = pk2(SC[14], SC[15]); PIN(PC1); SBAR(); } while (0)
#define NOP (void)0
#define ARRIVE(K) asm volatile("" : "+v"(K[0]), "+v"(K[1]), "+v"(K[2]), "+v"(K[3]))
#define TILE(KP, KC, KN, V, SB, G3) do { \
        STAGE(sA, sB, pB0, pB1, pA0, pA1, KC, 1, KP, 3, NOP, NOP, NOP, NOP); \
        STAGE(sB, sA, pA0, pA1, pB0, pB1, KC, 2, KC, 0, LD(KP, 0, V, SB), LD(KP, 1, V, SB), LD(KP, 2, V, SB), LD(KP, 3, V, SB)); \
        STAGE(sA, sB, pB0, pB1, pA0, pA1, KC, 3, KC, 1, NOP, NOP, NOP, ARRIVE(KN)); \
        STAGE(sB, sA, pA0, pA1, pB0, pB1, KN, 0, KC, 2, G3, NOP, NOP, NOP); } while (0)
#pragma unroll 1
        for (int it = 0; it < 4; ++it) {
            const unsigned vn = it < 3 ? lane * 16 + ((rk0 + it + 1) & 3) * 24576 : 0x7FFF0000u;
            TILE(kD, kA, kB, vc, kb3, NOP);
            TILE(kA, kB, kC, vn, kb0, NOP);
            TILE(kB, kC, kD, vn, kb1, NOP);
            TILE(kC, kD, kA, vn, kb2, NOP);
            vc = vn;
        }
        O[3] = MFMA(asbf(kD[2]), asbf(pA0), O[3]); O[3] = MFMA(asbf(kD[3]), asbf(pA1), O[3]);
        sig(C_ATT, lane);
        STAMP(7); STAMPC(21);
        f32x16 o;
#pragma unroll
        for (int a = 0; a < 4; ++a) {
            if (a != w) {
                const int sl = a * 3 + (w > a ? w - 1 : w);
#pragma unroll
                for (int u = 0; u < 4; ++u) lds_wrf4(L_R2 + (sl * 4 + u) * 1024 + lane * 16, (f32x4){O[a][4 * u], O[a][4 * u + 1], O[a][4 * u + 2], O[a][4 * u + 3]});
            } else o = O[a];
        }
        WAIT_LGKM0(); sig(C_CMB, lane); waitc<0>(C_CMB, 4);
        FSTAMPC(28);
        {
            f32x4 pr[12];
#pragma unroll
            for (int k = 0; k < 12; ++k) pr[k] = lds_rdf4(L_R2 + (w * 12 + k) * 1024 + lane * 16);
            SBAR();
#pragma unroll
            for (int k = 0; k < 12; ++k)
#pragma unroll
                for (int i = 0; i < 4; ++i) o[4 * (k & 3) + i] += pr[k][i];
        }
        float l = from_hi(o[12]);
        {
            f32x2 chk = {0.f, 0.f};
#pragma unroll
            for (int i = 0; i < 16; i += 2) chk = __builtin_elementwise_fma((f32x2){o[i], o[i + 1]}, (f32x2){0.f, 0.f}, chk);
            bool bad = !(chk[0] + chk[1] == 0.f) || !(l >= 1e-32f && l <= 1e30f);
            if (__any(bad)) {
                float mrun = -3.0e38f;
                o = f32x16{};
#pragma unroll 1
                for (int k2 = 0; k2 < 4; ++k2) {
                    poll(rws, WS_F1 + (((lane & 8) ? gv : gk) * 32 + 8 * k2 + (lane & 7)) * 128, tag);
#pragma unroll 1
                    for (int m2 = 0; m2 < 16; ++m2) {
                        const unsigned so = kvb + (k2 * 16 + m2) * 6144;
                        const u32x4 k0 = ld16(rws, lane * 16, so), k1 = ld16(rws, lane * 16 + 1024, so), v0 = ld16(rws, lane * 16 + 2048, so), v1 = ld16(rws, lane * 16 + 3072, so);
                        f32x16 s = MFMA(asbf(k0), asbf(q0), f32x16{}); s = MFMA(asbf(k1), asbf(q1), s);
                        const float mn = __builtin_fmaxf(mrun, swap_max(max16(s))), al = __builtin_amdgcn_exp2f(mrun - mn);
                        mrun = mn;
#pragma unroll
                        for (int i = 0; i < 16; ++i) { s[i] = __builtin_amdgcn_exp2f(s[i] - mn); o[i] *= al; }
                        o = MFMA(asbf(v0), asbf(pk8(s, 0)), o); o = MFMA(asbf(v1), asbf(pk8(s, 1)), o);
                    }
                }
                l = from_hi(o[12]);
            }
        }
        const float rl = __builtin_amdgcn_rcpf(l);
#pragma unroll
        for (int i = 0; i < 16; ++i) o[i] *= rl;
        u32x4 o0 = pk8(o, 0), o1 = pk8(o, 1);
        if (hi) { o1[2] = 0; o1[3] = 0; }
        const unsigned so = WS_O + ((tq * 16 + head) * 2) * 1024;
        st16(rws, o0, lane * 16, so); st16(rws, o1, lane * 16 + 1024, so);
        STAMP(8); FSTAMPC(27);
        if (r < 28) { waitc<0>(C_WO0, 4); read_wo(wf, w >> 1, ct, lane); }
        post(rws, WS_FO + (tq * 16 + head) * 128, tag, lane);
        FSTAMP(9);
    }

    if (r < 28) {
        const int br = role, tp = w & 1, kh = w >> 1, tto = rt * 2 + tp;
        const rsrc_t rout = mk_rsrc(ka[14], ka[15], 2048 * 1792);
        poll(rws, WS_FO + (br * 1024 + tto * 16 + kh * 8 + (lane & 7)) * 128, tag);
        STAMP(10);
        u32x4 of[16];
        const unsigned ob = WS_O + br * (2u << 20) + (tto * 16 + kh * 8) * 2048;
#pragma unroll
        for (int k = 0; k < 16; ++k) of[k] = ld16(rws, lane * 16 + (k & 1) * 1024, ob + (((k >> 1) + ct) & 7) * 2048);
        SBAR();
        if (role) waitc<6>(C_ATT, 4);
        f32x16 c0 = {}, c1 = {};
#pragma unroll
        for (int k = 0; k < 16; ++k) { c0 = MFMA(wf[k][0], asbf(of[k]), c0); c1 = MFMA(wf[k][1], asbf(of[k]), c1); }
        FSTAMP(11);
        const unsigned xb_ = L_R1 + br * 16384;
        if (kh) { f32x16 t = c0; c0 = c1; c1 = t; }
#pragma unroll
        for (int u = 0; u < 4; ++u) lds_wrf4(xb_ + (w * 4 + u) * 1024 + lane * 16, (f32x4){c1[4 * u], c1[4 * u + 1], c1[4 * u + 2], c1[4 * u + 3]});
        WAIT_LGKM0(); sig(C_EX0 + br * 2 + tp, lane); waitc<0>(C_EX0 + br * 2 + tp, 2);
#pragma unroll
        for (int u = 0; u < 4; ++u) {
            const f32x4 s = lds_rdf4(xb_ + ((w ^ 2) * 4 + u) * 1024 + lane * 16);
#pragma unroll
            for (int i = 0; i < 4; ++i) c0[4 * u + i] += s[i];
        }
        const unsigned vo = (tto * 32 + (lane & 31)) * 1792 + (hi ? 16 : 0), so = br * 896 + ct * 128 + kh * 64;
#pragma unroll
        for (int gp = 0; gp < 2; ++gp) {
            auto s0 = __builtin_amdgcn_permlane32_swap(pk2(c0[8 * gp], c0[8 * gp + 1]), pk2(c0[8 * gp + 4], c0[8 * gp + 5]), false, false);
            auto s1 = __builtin_amdgcn_permlane32_swap(pk2(c0[8 * gp + 2], c0[8 * gp + 3]), pk2(c0[8 * gp + 6], c0[8 * gp + 7]), false, false);
            st16a<0>(rout, (u32x4){s0[0], s1[0], s0[1], s1[1]}, vo + gp * 32, so);
        }
        STAMP(12);
    }
    STAMP_FLUSH();
    asm volatile(".Lpf_end:");
}

extern "C" void kernel_launch(void* const* d_in, const int* in_sizes, int n_in, void* d_out, int out_size, void* d_ws, size_t ws_size,
                              hipStream_t stream) {
    (void)in_sizes; (void)n_in; (void)out_size; (void)ws_size;
    (void)hipFuncSetAttribute((const void*)fused_fwd, hipFuncAttributeMaxDynamicSharedMemorySize, LDS_BYTES);
    fused_fwd<<<256, 512, LDS_BYTES, stream>>>(d_in[0], d_in[1], d_in[2], d_in[3], d_in[4], d_in[5], d_in[6], d_out, d_ws);
}
```

```cpp
#define STAMPS 1
#include <hip/hip_runtime.h>
#include <hip/hip_bf16.h>
#include <stdint.h>
#include <type_traits>

typedef __attribute__((ext_vector_type(8))) __bf16 bf16x8;
typedef __attribute__((ext_vector_type(2))) __bf16 bf16x2;
typedef __attribute__((ext_vector_type(2))) float f32x2;
typedef __attribute__((ext_vector_type(4))) float f32x4;
typedef __attribute__((ext_vector_type(16))) float f32x16;
typedef __attribute__((ext_vector_type(4))) unsigned u32x4;
typedef __attribute__((ext_vector_type(2))) unsigned u32x2;
typedef __attribute__((ext_vector_type(4))) short s16x4;
typedef __amdgpu_buffer_rsrc_t rsrc_t;
#define LAS __attribute__((address_space(3)))
#define DI __device__ __forceinline__

constexpr unsigned WS_EPOCH = 0;
constexpr unsigned WS_F1 = 4096;
constexpr unsigned WS_F2 = WS_F1 + 1024 * 128;
constexpr unsigned WS_F3 = WS_F2 + 512 * 128;
constexpr unsigned WS_FO = WS_F3 + 256 * 128;
constexpr unsigned WS_QKV = 1u << 20;
constexpr unsigned WS_KVP = WS_QKV + (6u << 20);
constexpr unsigned WS_KVR = WS_KVP + (2u << 20);
constexpr unsigned WS_O = WS_KVR + (1u << 20);
constexpr unsigned WS_DBG = WS_O + (4u << 20);
constexpr unsigned WS_BYTES = WS_DBG + (1u << 20);

constexpr unsigned SLOT = 37888;
constexpr unsigned L_R0 = 0;
constexpr unsigned L_R1 = 57856;
constexpr unsigned L_R2 = L_R1 + 32768;
constexpr unsigned L_QX = L_R2 + 49152;
constexpr unsigned L_QB = L_R2;
constexpr unsigned L_KB = L_QB + 16384;
constexpr unsigned L_PHI = 4 * SLOT + 64;
constexpr unsigned L_JUNK = L_PHI + 4096;
constexpr unsigned L_CNT = L_JUNK + 1024;
constexpr unsigned L_TAG = L_CNT + 256;
constexpr unsigned LDS_BYTES = L_TAG + 16;
enum { C_LAND0 = 24, C_DONE0 = 32, C_QB = 8, C_GDONE = 9, C_QX = 10, C_PV = 11, C_KVDONE = 12, C_WO0 = 13, C_WOFREE = 15, C_EX0 = 16, C_CMB = 20, C_ATT = 21 };

#define MAP_SEP 0
constexpr unsigned SPIN_CAP = 1u << 18;
constexpr float QSCALE = 0.18898223650461362f * 1.4426950408889634f;
constexpr float LOG2E = 1.4426950408889634f;
constexpr float REF_SHIFT = 100.f;

#define MFMA(a, b, c) __builtin_amdgcn_mfma_f32_32x32x16_bf16((a), (b), (c), 0, 0, 0)
#define CBAR() asm volatile("" ::: "memory")
#define WAIT_VM(n) asm volatile("s_waitcnt vmcnt(" #n ")" ::: "memory")
#define WAIT_LGKM0() asm volatile("s_waitcnt lgkmcnt(0)" ::: "memory")
#define SBAR() __builtin_amdgcn_sched_barrier(0)

#define STAMP_DECL unsigned stv = 0
#define STAMP(i) do { unsigned long long t_; asm volatile("s_memrealtime %0\n\ts_waitcnt lgkmcnt(0)" : "=s"(t_) :: "memory"); \
    asm volatile("v_writelane_b32 %0, %1, " #i : "+v"(stv) : "s"((unsigned)t_)); } while (0)
#define STAMPC(i) do { unsigned long long t_; asm volatile("s_memtime %0\n\ts_waitcnt lgkmcnt(0)" : "=s"(t_) :: "memory"); \
    asm volatile("v_writelane_b32 %0, %1, " #i : "+v"(stv) : "s"((unsigned)t_)); } while (0)
#define STAMP_FLUSH() do { STAMPC(31); STAMP(29); if (lane < 32) __builtin_amdgcn_raw_buffer_store_b32(stv, rws, WS_DBG + ((blockIdx.x * 8 + wid) * 32 + lane) * 4, 0, 0); } while (0)
#define FSTAMP(i) do {} while (0)
#define FSTAMPC(i) do {} while (0)
#define PIN(x) asm volatile("" : "+v"(x))

DI unsigned pk2(float a, float b) { f32x2 v = {a, b}; return __builtin_bit_cast(unsigned, __builtin_convertvector(v, bf16x2)); }
DI u32x4 pk8(const f32x16& a, int s) {
    return (u32x4){pk2(a[8 * s], a[8 * s + 1]), pk2(a[8 * s + 2], a[8 * s + 3]), pk2(a[8 * s + 4], a[8 * s + 5]), pk2(a[8 * s + 6], a[8 * s + 7])}; }
DI bf16x8 asbf(u32x4 v) { return __builtin_bit_cast(bf16x8, v); }
DI float swap_max(float m) { auto r = __builtin_amdgcn_permlane32_swap(__float_as_uint(m), __float_as_uint(m), false, false);
    return __builtin_fmaxf(__uint_as_float(r[0]), __uint_as_float(r[1])); }
DI float swap_min(float m) { auto r = __builtin_amdgcn_permlane32_swap(__float_as_uint(m), __float_as_uint(m), false, false);
    return __builtin_fminf(__uint_as_float(r[0]), __uint_as_float(r[1])); }
DI float swap_sum(float m) { auto r = __builtin_amdgcn_permlane32_swap(__float_as_uint(m), __float_as_uint(m), false, false);
    return __uint_as_float(r[0]) + __uint_as_float(r[1]); }
DI float from_hi(float m) { auto r = __builtin_amdgcn_permlane32_swap(__float_as_uint(m), __float_as_uint(m), false, false);
    return __uint_as_float(r[1]); }
DI float max16(const f32x16& s) {
    float a = __builtin_fmaxf(__builtin_fmaxf(s[0], s[1]), s[2]), b = __builtin_fmaxf(__builtin_fmaxf(s[3], s[4]), s[5]);
    a = __builtin_fmaxf(__builtin_fmaxf(a, s[6]), s[7]); b = __builtin_fmaxf(__builtin_fmaxf(b, s[8]), s[9]);
    a = __builtin_fmaxf(__builtin_fmaxf(a, s[10]), s[11]); b = __builtin_fmaxf(__builtin_fmaxf(b, s[12]), s[13]);
    a = __builtin_fmaxf(__builtin_fmaxf(a, s[14]), s[15]); return __builtin_fmaxf(a, b); }
DI float min16(const f32x16& s) {
    float a = __builtin_fminf(__builtin_fminf(s[0], s[1]), s[2]), b = __builtin_fminf(__builtin_fminf(s[3], s[4]), s[5]);
    a = __builtin_fminf(__builtin_fminf(a, s[6]), s[7]); b = __builtin_fminf(__builtin_fminf(b, s[8]), s[9]);
    a = __builtin_fminf(__builtin_fminf(a, s[10]), s[11]); b = __builtin_fminf(__builtin_fminf(b, s[12]), s[13]);
    a = __builtin_fminf(__builtin_fminf(a, s[14]), s[15]); return __builtin_fminf(a, b); }

DI void dma(rsrc_t rs, unsigned voff, unsigned soff, unsigned lds) {
    asm volatile("s_mov_b32 m0, %0\n\ts_nop 0\n\tbuffer_load_dwordx4 %1, %2, %3 offen lds" :: "s"(lds), "v"(voff), "s"(rs), "s"(soff) : "memory"); }
DI u32x4 lds_rd128(unsigned a) { return *(LAS const u32x4*)(uintptr_t)a; }
DI f32x4 lds_rdf4(unsigned a) { return *(LAS const f32x4*)(uintptr_t)a; }
DI void lds_wr128(unsigned a, u32x4 v) { *(LAS u32x4*)(uintptr_t)a = v; }
DI void lds_wrf4(unsigned a, f32x4 v) { *(LAS f32x4*)(uintptr_t)a = v; }
DI void lds_wr64(unsigned a, u32x2 v) { *(LAS u32x2*)(uintptr_t)a = v; }
DI bf16x8 lds_tr2(unsigned a0, unsigned a1) {
    s16x4 lo = __builtin_amdgcn_ds_read_tr16_b64_v4i16((LAS s16x4*)(uintptr_t)a0), hi = __builtin_amdgcn_ds_read_tr16_b64_v4i16((LAS s16x4*)(uintptr_t)a1);
    return __builtin_bit_cast(bf16x8, __builtin_shufflevector(lo, hi, 0, 1, 2, 3, 4, 5, 6, 7)); }
DI void sig(int c, int) {
    asm volatile("s_mov_b64 exec, 1\n\tds_add_u32 %0, %1\n\ts_mov_b64 exec, -1" :: "v"(L_CNT + 4 * c), "v"(1u) : "memory"); }
template <int SLP = 1> DI void waitc(int c, unsigned n) {
    unsigned v, sv, it;
    asm volatile("s_mov_b32 %2, %5\n"
                 "1:\n\tds_read_b32 %0, %3\n\ts_waitcnt lgkmcnt(0)\n\tv_readfirstlane_b32 %1, %0\n\ts_cmp_ge_u32 %1, %4\n\ts_cbranch_scc1 2f\n\t"
                 "s_sleep %6\n\ts_sub_u32 %2, %2, 1\n\ts_cbranch_scc0 1b\n2:"
                 : "=&v"(v), "=&s"(sv), "=&s"(it) : "v"(L_CNT + 4 * c), "i"(n), "i"(SPIN_CAP), "i"(SLP) : "memory", "scc"); }
DI unsigned rd_cnt(int c) { return *(volatile LAS const unsigned*)(uintptr_t)(L_CNT + 4 * c); }
DI u32x2 rd_tag() { const u32x2 t = *(LAS const u32x2*)(uintptr_t)L_TAG;
    return (u32x2){(unsigned)__builtin_amdgcn_readfirstlane(t[0]), (unsigned)__builtin_amdgcn_readfirstlane(t[1])}; }
DI u32x4 ld16(rsrc_t rs, unsigned voff, unsigned soff) { return __builtin_amdgcn_raw_buffer_load_b128(rs, voff, soff, 16); }
template <int AUX> DI void st16a(rsrc_t rs, u32x4 v, unsigned voff, unsigned soff) {
    __builtin_amdgcn_raw_buffer_store_b128(v, rs, voff, soff, AUX); asm volatile("s_nop 1" :: "v"(v) : "memory"); }
DI void st16(rsrc_t rs, u32x4 v, unsigned voff, unsigned soff) { st16a<16>(rs, v, voff, soff); }
DI u32x2 ldflag(rsrc_t rs, unsigned voff) { return __builtin_amdgcn_raw_buffer_load_b64(rs, voff, 0, 16); }
DI bool tagok(u32x2 f, u32x2 tag) {
    return __builtin_amdgcn_ballot_w64(__builtin_bit_cast(unsigned long long, f) == __builtin_bit_cast(unsigned long long, tag)) == ~0ull; }
DI void poll(rsrc_t rs, unsigned voff, u32x2 tag) {
    CBAR();
#pragma clang loop unroll(disable)
    for (unsigned it = 0; it < SPIN_CAP; ++it) { u32x2 f = ldflag(rs, voff); if (tagok(f, tag)) break; __builtin_amdgcn_s_sleep(2); CBAR(); }
    CBAR(); }
DI void post(rsrc_t rs, unsigned off, u32x2 tag, int lane) {
    WAIT_VM(0);
    if (lane == 0) __builtin_amdgcn_raw_buffer_store_b64(tag, rs, off, 0, 16);
    CBAR(); }

DI u32x4 lds_tr2u(unsigned a) { return __builtin_bit_cast(u32x4, lds_tr2(a, a + 336)); }
DI void sigv(unsigned addr) { asm volatile("s_mov_b64 exec, 1\n\tds_add_u32 %0, %1\n\ts_mov_b64 exec, -1" :: "v"(addr), "v"(1u) : "memory"); }
template <int ND, class Start>
DI void gemm6(unsigned xa0, unsigned wa0, unsigned wc0, f32x16 (&acc)[6], Start start) {
    u32x4 f[2][3], x[2][2];
    unsigned xa = xa0, x1 = xa0 ^ 32, x2, x3, wa = wa0, wc = wc0, wan, wcn, ca = L_CNT + 4 * C_LAND0, cv, so = 0, rf;
#pragma unroll
    for (int i = 0; i < 6; ++i) { acc[i] = f32x16{}; PIN(acc[i]); }
    PIN(xa); PIN(x1); PIN(wa); PIN(wc); PIN(ca);
    start();
    waitc<0>(C_LAND0, 4);
    x[0][0] = lds_rd128(xa); SBAR(); f[0][0] = lds_tr2u(wa); SBAR(); x[0][1] = lds_rd128(xa + 4096); SBAR();
    f[0][1] = lds_tr2u(wa + 56); SBAR(); f[0][2] = lds_tr2u(wc); SBAR();
#define G6MM(c, t, i) acc[3 * t + i] = (i >= 3 - ND) ? MFMA(asbf(x[c][t]), asbf(f[c][i]), acc[3 * t + i]) : MFMA(asbf(f[c][i]), asbf(x[c][t]), acc[3 * t + i]); SBAR()
#define G6STEP(c, XA, WA, WC, P0, P2, P5) do { \
        G6MM(c, 0, 0); P0; x[1 - c][0] = lds_rd128(XA); SBAR(); \
        G6MM(c, 1, 0); f[1 - c][0] = lds_tr2u(WA); SBAR(); \
        G6MM(c, 0, 1); x[1 - c][1] = lds_rd128((XA) + 4096); P2; SBAR(); \
        G6MM(c, 1, 1); f[1 - c][1] = lds_tr2u((WA) + 56); SBAR(); \
        G6MM(c, 0, 2); f[1 - c][2] = lds_tr2u(WC); SBAR(); \
        G6MM(c, 1, 2); P5; SBAR(); } while (0)
#define SPIN(x) asm volatile("" : "+s"(x))
#pragma unroll 1
    for (int g = 1; g < 8; ++g) {
        G6STEP(0, x1, wa + 5376, wc + 5376, (void)0, { x2 = xa ^ 64; PIN(x2); }, { x3 = xa ^ 96; PIN(x3); so = (g & 3) * SLOT; SPIN(so); });
        G6STEP(1, x2, wa + 2 * 5376, wc + 2 * 5376, (void)0, { xa = xa0 + so; PIN(xa); },
               { cv = *(volatile LAS const unsigned*)(uintptr_t)(ca + 4); wan = wa0 + so; PIN(wan); });
        G6STEP(0, x3, wa + 3 * 5376, wc + 3 * 5376, (void)0, { wcn = wc0 + so; PIN(wcn); }, { rf = __builtin_amdgcn_readfirstlane(cv); SPIN(rf); x1 = xa ^ 32; PIN(x1); });
        G6STEP(1, xa, wan, wcn, { if (__builtin_expect(rf < 4u, 0)) waitc<0>(C_LAND0 + g, 4); CBAR(); }, { ca += 4; PIN(ca); },
               sigv(ca + 4 * (C_DONE0 - C_LAND0 - 1)));
        wa = wan; wc = wcn;
    }
}
DI unsigned tr_addr128(unsigned base, int cb, int rowgrp, int lane) {
    const int q = (lane & 15) >> 2, byte = cb + 32 * ((lane >> 4) & 1) + 8 * (lane & 3);
    return base + (rowgrp + q) * 128 + ((((byte >> 4) ^ ((q >> 1) << 2)) & 7) << 4) + (byte & 8);
}
DI void feat(f32x16& p, f32x16& e, f32x16& g, bool hi) {
#pragma unroll
    for (int i = 12; i < 16; ++i) p[i] = hi ? p[0] : p[i];
    const float mx = swap_max(max16(p)) * LOG2E, mn = swap_min(min16(p)) * LOG2E;
#pragma unroll
    for (int i = 0; i < 16; i += 2) {
        const f32x2 pp = {p[i], p[i + 1]};
        const f32x2 a = __builtin_elementwise_fma(pp, (f32x2){LOG2E, LOG2E}, (f32x2){-mx, -mx}), c = __builtin_elementwise_fma(pp, (f32x2){-LOG2E, -LOG2E}, (f32x2){mn, mn});
        e[i] = __builtin_amdgcn_exp2f(a[0]); e[i + 1] = __builtin_amdgcn_exp2f(a[1]); g[i] = __builtin_amdgcn_exp2f(c[0]); g[i + 1] = __builtin_amdgcn_exp2f(c[1]);
    }
}

typedef __attribute__((ext_vector_type(16))) unsigned u32x16;
DI rsrc_t mk_rsrc(unsigned lo, unsigned hi, unsigned bytes) {
    return __builtin_amdgcn_make_buffer_rsrc((void*)(((unsigned long long)hi << 32) | lo), 0, bytes, 0x00020000); }
DI void read_wo(bf16x8 (&wf)[16][2], int kh, int rot, int lane) {
    const unsigned ta0 = tr_addr128(L_R0, 0, kh * 224 + 4 * (lane >> 5), lane), ta1 = tr_addr128(L_R0, 64, kh * 224 + 4 * (lane >> 5), lane);
#pragma unroll
    for (int k = 0; k < 8; ++k) {
        const unsigned ro = ((k + rot) & 7) * 28 * 128, b0 = ta0 + ro, b1 = ta1 + ro;
        wf[2 * k][0] = lds_tr2(b0, b0 + 1024); wf[2 * k][1] = lds_tr2(b1, b1 + 1024);
        wf[2 * k + 1][0] = lds_tr2(b0 + 2048, b0 + 3072); wf[2 * k + 1][1] = lds_tr2(b1 + 2048, b1 + 3072);
    }
}
DI u32x4 ones_row(u32x4 v, int lane) {
    if ((lane & 31) >= 28) { const unsigned c = (lane & 31) == 28 ? 0x3F803F80u : 0u; v = (u32x4){c, c, c, c}; }
    return v; }
DI f32x16 phi_pre(u32x4 k0, u32x4 k1, int hh, int lane, bool hi) {
    if (hi) { k1[2] = 0; k1[3] = 0; }
    const unsigned pb = L_PHI + hh * 1568 + (4 * (lane >> 5) + ((lane & 15) >> 2)) * 56 + 32 * ((lane >> 4) & 1) + 8 * (lane & 3);
    bf16x8 f0 = lds_tr2(pb, pb + 8 * 56);
    u32x4 f1 = __builtin_bit_cast(u32x4, lds_tr2(pb + 16 * 56, pb + 24 * 56));
    if (hi) { f1[2] = 0; f1[3] = 0; }
    f32x16 p = MFMA(f0, asbf(k0), f32x16{});
    return MFMA(asbf(f1), asbf(k1), p);
}

__global__ __launch_bounds__(512, 2) void fused_fwd(const void*, const void*, const void*, const void*, const void*, const void*, const void*, void*, void*) {
    STAMP_DECL; STAMP(0); STAMPC(30);
    u32x16 ka; u32x2 kb;
    asm volatile("s_load_dwordx16 %0, %2, 0x0\n\ts_load_dwordx2 %1, %2, 0x40" : "=&s"(ka), "=&s"(kb) : "s"(__builtin_amdgcn_kernarg_segment_ptr()));
    const int tid = threadIdx.x, lane = tid & 63, wid = __builtin_amdgcn_readfirstlane(tid >> 6);
    const int b = blockIdx.x, xcd = b & 7, r = b >> 3, w = wid & 3, role = wid >> 2;
    const int isb = r & 1, ml = (r >> 1) & 3, nl = r >> 3;
    const int mt = (xcd & 3) * 4 + ml, nt = (xcd >> 2) * 4 + nl;
    const bool hi = lane >= 32;
    int g0 = 2 * ml + nl; g0 -= (g0 >= 7) ? 7 : 0;
    const int ttg = mt * 4 + w;
    const int head = 2 * xcd + (r >> 4), qt = r & 15;
    const int ct = r >> 2, rt = xcd * 4 + (r & 3);
    u32x2 tag;
    bf16x8 wf[16][2];
    unsigned pfd = 0;

    unsigned xvo, wvo[6];
    if (role) {
        __builtin_amdgcn_s_setprio(2);
        xvo = (w * 8 + (lane >> 3)) * 896 + ((((lane & 7) ^ (lane >> 4) ^ ((w & 1) << 2)) & 7) << 4);
        PIN(xvo);
#pragma unroll
        for (int i = 0; i < 6; ++i) {
            const int c = 64 * (w + 4 * i) + lane, rho = (c * 3121) >> 16, ch = c - 21 * rho, win = (ch * 37) >> 8;
            const int krow = (rho & ~15) | ((rho & 3) << 2) | ((rho >> 2) & 3);
            wvo[i] = krow * 2688 + ch * 16 + (isb ? win * (896 - 112) : 0);
            PIN(wvo[i]);
        }
    }
    if (wid == 0) {
        unsigned long long pc; unsigned len;
        asm volatile("s_getpc_b64 %0\n.Lpf_begin%=:\n\ts_mov_b32 %1, .Lpf_end-.Lpf_begin%=" : "=s"(pc), "=s"(len));
        const rsrc_t rc = __builtin_amdgcn_make_buffer_rsrc((void*)pc, 0, len, 0x00020000);
        asm volatile("s_nop 4\n\tbuffer_load_dword %0, %1, %2, 0 offen" : "+v"(pfd) : "v"(lane < 12 ? (r * 12 + lane) * 128 : 0x7FFFFFF0), "s"(rc) : "memory");
        *(LAS unsigned*)(uintptr_t)(L_CNT + 4 * lane) = lane == C_LAND0 + 7 ? 4u : 0u;
    }
    asm volatile("s_waitcnt lgkmcnt(0)" : "+s"(ka), "+s"(kb) :: "memory");
    const rsrc_t rws = mk_rsrc(kb[0], kb[1], WS_BYTES);
    u32x4 pe[2][4];
    if (role) {
        const rsrc_t rx = mk_rsrc(isb ? ka[6] : ka[0], isb ? ka[7] : ka[1], 2048 * 896), rw = mk_rsrc(isb ? ka[8] : ka[2], isb ? ka[9] : ka[3], 448 * 2688);
        const unsigned xs0 = mt * 114688, ws0 = (isb ? 112 : 336) * nt;
        auto feed = [&](int g) {
            int ga = g + g0; ga -= (ga >= 7) ? 7 : 0;
            const unsigned xs = xs0 + ga * 128, ws = ws0 + ga * 64 * 2688, l = (g & 3) * SLOT + w * 1024;
#pragma unroll
            for (int j = 0; j < 4; ++j) dma(rx, xvo, xs + j * 32 * 896, l + j * 4096);
#pragma unroll
            for (int j = 0; j < 5; ++j) dma(rw, wvo[j], ws, l + 16384 + j * 4096);
            if (w == 0) dma(rw, wvo[5], ws, l + 16384 + 5 * 4096); };
        FSTAMP(18);
        dma(mk_rsrc(ka[10], ka[11], 16 * 1568), lane * 16, nt * 3136 + w * 1024, L_PHI + w * 1024);
        feed(0); feed(1);
        asm volatile("s_barrier" ::: "memory");
#pragma unroll 1
        for (int g = 0; g < 6; ++g) {
            if (w == 0) WAIT_VM(10); else WAIT_VM(9);
            sig(C_LAND0 + g, lane);
            if (g == 0) STAMP(1);
            if (g < 5) {
                if (g >= 2) waitc<0>(C_DONE0 + g - 2, 4);
                feed(g + 2);
            }
        }
        {
            const rsrc_t rt_ = mk_rsrc(w < 2 ? ka[4] : ka[12], w < 2 ? ka[5] : ka[13], 448 * 896);
            asm volatile("s_mov_b32 m0, %0\n\ts_nop 3\n\tbuffer_load_dword %1, %2, 0 offen lds"
                         :: "s"(L_JUNK), "v"(lane < 49 ? (r * 98 + (w & 1) * 49 + lane) * 128 : 0x7FFFFF00), "s"(rt_) : "memory");
        }
        WAIT_VM(1);
        sig(C_LAND0 + 6, lane);
        STAMP(2);
        tag = rd_tag();
        if (isb) {
            waitc<1>(C_QB + 0, 4);
#pragma unroll
            for (int hh = 0; hh < 2; ++hh) {
                const unsigned qa = L_QB + ((w * 2 + hh) * 2) * 1024 + lane * 16;
                f32x16 p = phi_pre(lds_rd128(qa), lds_rd128(qa + 1024), hh, lane, hi), e, g;
                feat(p, e, g, hi);
                pe[hh][0] = pk8(e, 0); pe[hh][1] = pk8(e, 1); pe[hh][2] = pk8(g, 0); pe[hh][3] = pk8(g, 1);
            }
        }
        STAMP(3);
    } else {
        f32x16 acc[6];
        const int tp = w & 1, sg = w >> 1;
        const int rr = tp * 64 + (lane & 31);
        const unsigned xa0 = rr * 128 + ((((lane >> 5) ^ (rr >> 1)) & 7) << 4);
        const unsigned lb = 16384 + (4 * ((lane & 15) >> 2) + 2 * (lane >> 5)) * 336 + 32 * ((lane >> 4) & 1) + 8 * (lane & 3);
        const unsigned wa0 = lb + 56 * (isb ? 2 - 2 * sg : 3 * sg), wc0 = lb + 56 * (isb ? 4 + sg : 3 * sg + 2);
        const int G = 2 * nt + sg;
        if (w == 0) {
            const u32x2 ep = ldflag(rws, WS_EPOCH);
            const unsigned long long e = (((unsigned long long)ep[1] << 32) | ep[0]) + 1;
            lds_wr64(L_TAG, (u32x2){(unsigned)e, (unsigned)(e >> 32)});
            WAIT_VM(0);
            asm volatile("" :: "v"(pfd));
        }
        auto start = [&]() { asm volatile("s_waitcnt lgkmcnt(0)\n\ts_barrier" ::: "memory"); FSTAMPC(22); FSTAMPC(23); };
        if (isb || G == 10) gemm6<1>(xa0, wa0, wc0, acc, start); else if (G < 10) gemm6<0>(xa0, wa0, wc0, acc, start); else gemm6<3>(xa0, wa0, wc0, acc, start);
        tag = rd_tag();
        FSTAMPC(24);
        STAMP(2);
        if (!isb) {
#pragma unroll
            for (int i = 0; i < 3; ++i) {
                const int sl = 3 * G + i, kind = sl >> 4;
#pragma unroll
                for (int t = 0; t < 2; ++t) {
                    f32x16& a = acc[3 * t + i];
                    if (kind == 0) {
#pragma unroll
                        for (int k = 0; k < 16; ++k) a[k] *= QSCALE;
                    }
                    u32x4 v0 = pk8(a, 0), v1 = pk8(a, 1);
                    if (kind == 2) { v0 = ones_row(v0, lane); v1 = ones_row(v1, lane); }
                    else if (hi) { v1[2] = kind ? 0x00003F80u : 0u; v1[3] = 0u; }
                    const unsigned so = WS_QKV + (((sl & 15) * 64 + mt * 4 + 2 * tp + t) * 6 + kind * 2) * 1024;
                    st16(rws, v0, lane * 16, so); st16(rws, v1, lane * 16 + 1024, so);
                }
            }
            post(rws, WS_F1 + (G * 32 + mt * 2 + tp) * 128, tag, lane);
            waitc<0>(C_DONE0 + 6, 4);
        } else {
            waitc<0>(C_DONE0 + 6, 4);
            u32x4 kf[2][2];
#pragma unroll
            for (int t = 0; t < 2; ++t)
#pragma unroll
                for (int s = 0; s < 2; ++s) {
#pragma unroll
                    for (int hh = 0; hh < 2; ++hh) {
                        const u32x4 v = pk8(acc[3 * t + hh], s);
                        if (t == 0) kf[hh][s] = v;
                        if (sg) lds_wr128(L_QB + (((2 * tp + t) * 2 + hh) * 2 + s) * 1024 + lane * 16, v);
                        else if (t == 1) lds_wr128(L_KB + ((tp * 2 + hh) * 2 + s) * 1024 + lane * 16, v);
                    }
                    lds_wr128(L_R0 + ((sg * 4 + 2 * tp + t) * 2 + s) * 1024 + lane * 16, ones_row(pk8(acc[3 * t + 2], s), lane));
                }
            WAIT_LGKM0(); sig(C_QB, lane);
            if (sg) {
                waitc<0>(C_QB, 4);
#pragma unroll
                for (int hh = 0; hh < 2; ++hh)
#pragma unroll
                    for (int s = 0; s < 2; ++s) kf[hh][s] = lds_rd128(L_KB + ((tp * 2 + hh) * 2 + s) * 1024 + lane * 16);
            }
            const int tl = 2 * tp + sg;
            __builtin_amdgcn_s_setprio(3);
            const int tk = lane & 31, pi = (((tk >> 1) & 1) << 3) | (tk & 1) | (((tk >> 2) & 3) << 1);
#pragma unroll
            for (int hh = 0; hh < 2; ++hh) {
                f32x16 p = phi_pre(kf[hh][0], kf[hh][1], hh, lane, hi), e, g;
                feat(p, e, g, hi);
#pragma unroll
                for (int i = 12; i < 16; ++i) { e[i] = hi ? 0.f : e[i]; g[i] = hi ? 0.f : g[i]; }
                float se = 0.f, sg = 0.f;
#pragma unroll
                for (int i = 0; i < 16; ++i) { se += e[i]; sg += g[i]; }
                se = __builtin_amdgcn_rcpf(swap_sum(se)); sg = __builtin_amdgcn_rcpf(swap_sum(sg));
#pragma unroll
                for (int u = 0; u < 8; ++u) {
                    const int piece = 8 * (u >> 2) + 2 * (u & 3) + (lane >> 5), k = 4 * (u & 3);
                    const f32x16& z = (u >> 2) ? g : e; const float sc = (u >> 2) ? sg : se;
                    lds_wr64(L_R1 + hh * 16384 + (tl * 32 + tk) * 128 + ((piece ^ pi) << 3), (u32x2){pk2(z[k] * sc, z[k + 1] * sc), pk2(z[k + 2] * sc, z[k + 3] * sc)});
                }
            }
            WAIT_LGKM0(); sig(C_PV, lane);
            __builtin_amdgcn_s_setprio(0);
        }
        STAMP(3);
    }
    FSTAMP(4);

    if (role) {
        const int wc = (lane & 7) ^ (((lane >> 4) & 1) << 2);
        const unsigned wovo = (lane >> 3) * 896 + wc * 16;
        if (isb) {
            waitc<0>(C_PV, 4);
            const int hh = w >> 1, ft = w & 1, q = (lane & 15) >> 2, P = 8 * ft + 4 * ((lane >> 4) & 1) + (lane & 3);
            bf16x8 fa[8], fb[8];
#pragma unroll
            for (int st = 0; st < 8; ++st) {
                const int t0 = 16 * (st & 1) + 4 * (lane >> 5) + q, t1 = t0 + 8;
                const int p0 = (((t0 >> 1) & 1) << 3) | (t0 & 1) | (((t0 >> 2) & 3) << 1), p1 = (((t1 >> 1) & 1) << 3) | (t1 & 1) | (((t1 >> 2) & 3) << 1);
                const unsigned ib = L_R1 + hh * 16384 + (st >> 1) * 4096;
                fa[st] = lds_tr2(ib + t0 * 128 + ((P ^ p0) << 3), ib + t1 * 128 + ((P ^ p1) << 3));
                fb[st] = asbf(lds_rd128(L_R0 + ((hh * 4 + (st >> 1)) * 2 + (st & 1)) * 1024 + lane * 16));
            }
            SBAR();
            f32x16 kv = {};
#pragma unroll
            for (int st = 0; st < 8; ++st) kv = MFMA(fa[st], fb[st], kv);
            WAIT_LGKM0(); sig(C_KVDONE, lane);
            const int hk = 2 * nt + hh;
#pragma unroll
            for (int u = 0; u < 4; ++u)
                st16(rws, __builtin_bit_cast(u32x4, (f32x4){kv[4 * u], kv[4 * u + 1], kv[4 * u + 2], kv[4 * u + 3]}), lane * 16 + u * 1024, WS_KVP + ((hk * 16 + mt) * 8 + ft * 4) * 1024);
            FSTAMP(19);
            post(rws, WS_F2 + ((hk * 16 + mt) * 2 + ft) * 128, tag, lane);
            waitc<0>(C_KVDONE, 4);
        }
        STAMP(5);
        if (r < 28) {
            waitc<4>(C_QX, 4);
            if (w == 0) *(LAS u32x2*)(uintptr_t)(L_R0 + 57344 + 8 * lane) = (u32x2){0u, 0u};
            const rsrc_t rwob = mk_rsrc(ka[12], ka[13], 448 * 896);
#pragma unroll
            for (int i = 0; i < 14; ++i) dma(rwob, wovo, ct * 128 + (w + 4 * i) * 8 * 896, L_R0 + (w + 4 * i) * 1024);
            WAIT_VM(0); WAIT_LGKM0(); sig(C_WO0 + 1, lane); waitc<2>(C_WO0 + 1, 4);
            read_wo(wf, w >> 1, ct, lane);
            WAIT_LGKM0(); sig(C_WOFREE, lane);
        }
        if (w == 3) {
            const int hr = head, i = lane & 15, pg = lane >> 4;
            poll(rws, WS_F2 + ((hr * 16 + i) * 2 + (qt >> 3)) * 128, tag);
            u32x4 pl[8];
#pragma unroll
            for (int p = 0; p < 4; ++p) {
                const unsigned vo = (pg * 4 + p) * 8192 + (16 * (qt & 3) + i) * 16, so = WS_KVP + hr * 131072 + (qt >> 2) * 2048;
                pl[2 * p] = ld16(rws, vo, so); pl[2 * p + 1] = ld16(rws, vo + 1024, so);
            }
            SBAR();
            f32x4 s0 = {}, s1 = {};
#pragma unroll
            for (int p = 0; p < 4; ++p) { s0 += __builtin_bit_cast(f32x4, pl[2 * p]); s1 += __builtin_bit_cast(f32x4, pl[2 * p + 1]); }
            float v[8] = {s0[0], s0[1], s0[2], s0[3], s1[0], s1[1], s1[2], s1[3]};
#pragma unroll
            for (int k = 0; k < 8; ++k) {
                auto t = __builtin_amdgcn_permlane16_swap(__float_as_uint(v[k]), __float_as_uint(v[k]), false, false);
                v[k] = swap_sum(__uint_as_float(t[0]) + __uint_as_float(t[1]));
            }
            u32x4 o = {pk2(v[0], v[1]), pk2(v[2], v[3]), pk2(v[4], v[5]), pk2(v[6], v[7])};
            const int cl = 16 * (qt & 3) + i;
            if ((cl & 31) == 29) { const unsigned c = ((qt >> 2) & 1) && cl >= 32 ? 0u : 0x3F803F80u; o = (u32x4){0x3F803F80u, 0x3F803F80u, c, c}; }
            st16(rws, o, lane < 16 ? i * 16 : 0x7FFFFF00u, WS_KVR + (hr * 256 + qt * 16) * 16);
            post(rws, WS_F3 + (hr * 16 + qt) * 128, tag, lane);
        }
        STAMP(6);
        if (r < 28) {
            const rsrc_t rwoa = mk_rsrc(ka[4], ka[5], 448 * 896);
            waitc<2>(C_WOFREE, 4);
#pragma unroll
            for (int i = 0; i < 14; ++i) dma(rwoa, wovo, ct * 128 + (w + 4 * i) * 8 * 896, L_R0 + (w + 4 * i) * 1024);
            WAIT_VM(0); sig(C_WO0, lane);
        }
        if (isb) {
            poll(rws, WS_F3 + (2 * nt * 16 + (lane & 31)) * 128, tag);
            u32x4 kf[2][4];
#pragma unroll
            for (int hh = 0; hh < 2; ++hh)
#pragma unroll
                for (int f = 0; f < 4; ++f) kf[hh][f] = ld16(rws, lane * 16 + f * 1024, WS_KVR + (2 * nt + hh) * 4096);
            SBAR();
#pragma unroll
            for (int hh = 0; hh < 2; ++hh) {
                f32x16 np = MFMA(asbf(kf[hh][0]), asbf(pe[hh][0]), f32x16{}); np = MFMA(asbf(kf[hh][1]), asbf(pe[hh][1]), np);
                f32x16 nm = MFMA(asbf(kf[hh][2]), asbf(pe[hh][2]), f32x16{}); nm = MFMA(asbf(kf[hh][3]), asbf(pe[hh][3]), nm);
                const float rp = __builtin_amdgcn_rcpf(np[13]), rm = __builtin_amdgcn_rcpf(nm[13]);
                const float rd = __builtin_amdgcn_rcpf(__builtin_fmaf(rp, np[12], rm * nm[12]) + 1e-6f);
                const float cp = from_hi(rp * rd), cm = from_hi(rm * rd);
                f32x16 o;
#pragma unroll
                for (int i = 0; i < 16; ++i) o[i] = __builtin_fmaf(cp, np[i], cm * nm[i]);
                u32x4 o0 = pk8(o, 0), o1 = pk8(o, 1);
                if (hi) { o1[2] = 0; o1[3] = 0; }
                const unsigned so = WS_O + (2u << 20) + ((ttg * 16 + 2 * nt + hh) * 2) * 1024;
                st16(rws, o0, lane * 16, so); st16(rws, o1, lane * 16 + 1024, so);
            }
            WAIT_VM(0);
            if (lane < 2) __builtin_amdgcn_raw_buffer_store_b64(tag, rws, WS_FO + (1024 + ttg * 16 + 2 * nt + lane) * 128, 0, 16);
            CBAR();
        }
        STAMP(7);
        if (b == 255 && w == 0) {
#pragma unroll 1
            for (int i = 0; i < 4; ++i) poll(rws, WS_F3 + (i * 64 + lane) * 128, tag);
            if (lane == 0) __builtin_amdgcn_raw_buffer_store_b64(tag, rws, WS_EPOCH, 0, 16);
        }
    } else {
        const int tq = qt * 4 + w;
        const int gq = (head * 43) >> 7, gk = ((16 + head) * 43) >> 7, gv = ((32 + head) * 43) >> 7;
        poll(rws, WS_F1 + (lane >= 16 ? gq * 32 + (tq >> 1) : ((lane & 8) ? gv : gk) * 32 + 8 * w + (lane & 7)) * 128, tag);
        STAMP(5);
        const unsigned hb = WS_QKV + head * 64 * 6144, kvb = hb + 2048;
        const unsigned kb0 = kvb + (16 * w + (qt & 3)) * 6144, kb1 = kvb + (16 * w + ((qt + 1) & 3)) * 6144,
                       kb2 = kvb + (16 * w + ((qt + 2) & 3)) * 6144, kb3 = kvb + (16 * w + ((qt + 3) & 3)) * 6144;
        const int rk0 = qt >> 2;
        unsigned vc = lane * 16 + rk0 * 24576;
        u32x4 kA[4], kB[4], kC[4], kD[4];
#define LD(d, f, v, sb) d[f] = ld16(rws, (v) + (f) * 1024, sb)
        u32x4 q0 = ld16(rws, lane * 16, hb + tq * 6144), q1 = ld16(rws, lane * 16 + 1024, hb + tq * 6144);
        LD(kA, 0, vc, kb0); LD(kA, 1, vc, kb0);
        SBAR();
        f32x16 O[4] = {};
        u32x4 pA0 = {}, pA1 = {}, pB0, pB1;
        kD[2] = (u32x4){}; kD[3] = (u32x4){};
        PIN(O[0]); PIN(O[1]); PIN(O[2]); PIN(O[3]); PIN(pA0); PIN(pA1); PIN(kD[2]); PIN(kD[3]);
        SBAR();
        bf16x8 Q[4][2];
        {
            f32x16 s = MFMA(asbf(kA[0]), asbf(q0), f32x16{}); s = MFMA(asbf(kA[1]), asbf(q1), s);
            SBAR(); FSTAMP(13);
            LD(kA, 2, vc, kb0); LD(kA, 3, vc, kb0); LD(kB, 0, vc, kb1); LD(kB, 1, vc, kb1); LD(kB, 2, vc, kb1); LD(kB, 3, vc, kb1);
            LD(kC, 0, vc, kb2); LD(kC, 1, vc, kb2); LD(kC, 2, vc, kb2); LD(kC, 3, vc, kb2);
            SBAR();
            lds_wr128(L_QX + (w * 2) * 1024 + lane * 16, q0);
            const float m = swap_max(max16(s)) + REF_SHIFT;
            u32x4 q1m = q1;
            if (hi) q1m[2] = pk2(-m, 0.f);
            lds_wr128(L_QX + (w * 2 + 1) * 1024 + lane * 16, q1m);
            WAIT_LGKM0(); sig(C_QX, lane); FSTAMP(14); waitc<0>(C_QX, 4);
#pragma unroll
            for (int a = 0; a < 4; ++a) { Q[a][0] = asbf(lds_rd128(L_QX + (a * 2) * 1024 + lane * 16)); Q[a][1] = asbf(lds_rd128(L_QX + (a * 2 + 1) * 1024 + lane * 16)); }
        }
        STAMP(6); STAMPC(20);
        f32x16 sA, sB;
        sA = MFMA(asbf(kA[0]), Q[0][0], f32x16{}); sA = MFMA(asbf(kA[1]), Q[0][1], sA);
        PIN(Q[1][0]); PIN(Q[1][1]); PIN(Q[2][0]); PIN(Q[2][1]); PIN(Q[3][0]); PIN(Q[3][1]);
#define EX4(S, i) S[i] = __builtin_amdgcn_exp2f(S[i]); S[i + 1] = __builtin_amdgcn_exp2f(S[i + 1]); S[i + 2] = __builtin_amdgcn_exp2f(S[i + 2]); S[i + 3] = __builtin_amdgcn_exp2f(S[i + 3])
#define STAGE(SC, SN, PC0, PC1, PP0, PP1, KN, an, VP, ap, F0, F1, F2, F3) do { \
        SN = MFMA(asbf(KN[0]), Q[an][0], f32x16{}); SBAR(); F0; SBAR(); EX4(SC, 0); PIN(SC); SBAR(); \
        SN = MFMA(asbf(KN[1]), Q[an][1], SN); SBAR(); F1; SBAR(); EX4(SC, 4); PC0[0] = pk2(SC[0], SC[1]); PC0[1] = pk2(SC[2], SC[3]); PIN(SC); PIN(PC0); SBAR(); \
        O[ap] = MFMA(asbf(VP[2]), asbf(PP0), O[ap]); SBAR(); F2; SBAR(); EX4(SC, 8); PC0[2] = pk2(SC[4], SC[5]); PC0[3] = pk2(SC[6], SC[7]); PIN(SC); PIN(PC0); SBAR(); \
        O[ap] = MFMA(asbf(VP[3]), asbf(PP1), O[ap]); SBAR(); F3; SBAR(); EX4(SC, 12); PC1[0] = pk2(SC[8], SC[9]); PC1[1] = pk2(SC[10], SC[11]); \
        PC1[2] = pk2(SC[12], SC[13]); PC1[3] = pk2(SC[14], SC[15]); PIN(PC1); SBAR(); } while (0)
#define NOP (void)0
#define ARRIVE(K) asm volatile("" : "+v"(K[0]), "+v"(K[1]), "+v"(K[2]), "+v"(K[3]))
#define TILE(KP, KC, KN, V, SB, G3) do { \
        STAGE(sA, sB, pB0, pB1, pA0, pA1, KC, 1, KP, 3, NOP, NOP, NOP, NOP); \
        STAGE(sB, sA, pA0, pA1, pB0, pB1, KC, 2, KC, 0, LD(KP, 0, V, SB), LD(KP, 1, V, SB), LD(KP, 2, V, SB), LD(KP, 3, V, SB)); \
        STAGE(sA, sB, pB0, pB1, pA0, pA1, KC, 3, KC, 1, NOP, NOP, NOP, ARRIVE(KN)); \
        STAGE(sB, sA, pA0, pA1, pB0, pB1, KN, 0, KC, 2, G3, NOP, NOP, NOP); } while (0)
#pragma unroll 1
        for (int it = 0; it < 4; ++it) {
            const unsigned vn = it < 3 ? lane * 16 + ((rk0 + it + 1) & 3) * 24576 : 0x7FFF0000u;
            TILE(kD, kA, kB, vc, kb3, NOP);
            TILE(kA, kB, kC, vn, kb0, NOP);
            TILE(kB, kC, kD, vn, kb1, NOP);
            TILE(kC, kD, kA, vn, kb2, NOP);
            vc = vn;
        }
        O[3] = MFMA(asbf(kD[2]), asbf(pA0), O[3]); O[3] = MFMA(asbf(kD[3]), asbf(pA1), O[3]);
        sig(C_ATT, lane);
        STAMP(7); STAMPC(21);
        f32x16 o;
#pragma unroll
        for (int a = 0; a < 4; ++a) {
            if (a != w) {
                const int sl = a * 3 + (w > a ? w - 1 : w);
#pragma unroll
                for (int u = 0; u < 4; ++u) lds_wrf4(L_R2 + (sl * 4 + u) * 1024 + lane * 16, (f32x4){O[a][4 * u], O[a][4 * u + 1], O[a][4 * u + 2], O[a][4 * u + 3]});
            } else o = O[a];
        }
        WAIT_LGKM0(); sig(C_CMB, lane); waitc<0>(C_CMB, 4);
        FSTAMPC(28);
        {
            f32x4 pr[12];
#pragma unroll
            for (int k = 0; k < 12; ++k) pr[k] = lds_rdf4(L_R2 + (w * 12 + k) * 1024 + lane * 16);
            SBAR();
#pragma unroll
            for (int k = 0; k < 12; ++k)
#pragma unroll
                for (int i = 0; i < 4; ++i) o[4 * (k & 3) + i] += pr[k][i];
        }
        float l = from_hi(o[12]);
        {
            f32x2 chk = {0.f, 0.f};
#pragma unroll
            for (int i = 0; i < 16; i += 2) chk = __builtin_elementwise_fma((f32x2){o[i], o[i + 1]}, (f32x2){0.f, 0.f}, chk);
            bool bad = !(chk[0] + chk[1] == 0.f) || !(l >= 1e-32f && l <= 1e30f);
            if (__any(bad)) {
                float mrun = -3.0e38f;
                o = f32x16{};
#pragma unroll 1
                for (int k2 = 0; k2 < 4; ++k2) {
                    poll(rws, WS_F1 + (((lane & 8) ? gv : gk) * 32 + 8 * k2 + (lane & 7)) * 128, tag);
#pragma unroll 1
                    for (int m2 = 0; m2 < 16; ++m2) {
                        const unsigned so = kvb + (k2 * 16 + m2) * 6144;
                        const u32x4 k0 = ld16(rws, lane * 16, so), k1 = ld16(rws, lane * 16 + 1024, so), v0 = ld16(rws, lane * 16 + 2048, so), v1 = ld16(rws, lane * 16 + 3072, so);
                        f32x16 s = MFMA(asbf(k0), asbf(q0), f32x16{}); s = MFMA(asbf(k1), asbf(q1), s);
                        const float mn = __builtin_fmaxf(mrun, swap_max(max16(s))), al = __builtin_amdgcn_exp2f(mrun - mn);
                        mrun = mn;
#pragma unroll
                        for (int i = 0; i < 16; ++i) { s[i] = __builtin_amdgcn_exp2f(s[i] - mn); o[i] *= al; }
                        o = MFMA(asbf(v0), asbf(pk8(s, 0)), o); o = MFMA(asbf(v1), asbf(pk8(s, 1)), o);
                    }
                }
                l = from_hi(o[12]);
            }
        }
        const float rl = __builtin_amdgcn_rcpf(l);
#pragma unroll
        for (int i = 0; i < 16; ++i) o[i] *= rl;
        u32x4 o0 = pk8(o, 0), o1 = pk8(o, 1);
        if (hi) { o1[2] = 0; o1[3] = 0; }
        const unsigned so = WS_O + ((tq * 16 + head) * 2) * 1024;
        st16(rws, o0, lane * 16, so); st16(rws, o1, lane * 16 + 1024, so);
        STAMP(8); FSTAMPC(27);
        if (r < 28) { waitc<0>(C_WO0, 4); read_wo(wf, w >> 1, ct, lane); }
        post(rws, WS_FO + (tq * 16 + head) * 128, tag, lane);
        FSTAMP(9);
    }

    if (r < 28) {
        const int br = role, tp = w & 1, kh = w >> 1, tto = rt * 2 + tp;
        const rsrc_t rout = mk_rsrc(ka[14], ka[15], 2048 * 1792);
        poll(rws, WS_FO + (br * 1024 + tto * 16 + kh * 8 + (lane & 7)) * 128, tag);
        STAMP(10);
        u32x4 of[16];
        const unsigned ob = WS_O + br * (2u << 20) + (tto * 16 + kh * 8) * 2048;
#pragma unroll
        for (int k = 0; k < 16; ++k) of[k] = ld16(rws, lane * 16 + (k & 1) * 1024, ob + (((k >> 1) + ct) & 7) * 2048);
        SBAR();
        if (role) waitc<6>(C_ATT, 4);
        f32x16 c0 = {}, c1 = {};
#pragma unroll
        for (int k = 0; k < 16; ++k) { c0 = MFMA(wf[k][0], asbf(of[k]), c0); c1 = MFMA(wf[k][1], asbf(of[k]), c1); }
        FSTAMP(11);
        const unsigned xb_ = L_R1 + br * 16384;
        if (kh) { f32x16 t = c0; c0 = c1; c1 = t; }
#pragma unroll
        for (int u = 0; u < 4; ++u) lds_wrf4(xb_ + (w * 4 + u) * 1024 + lane * 16, (f32x4){c1[4 * u], c1[4 * u + 1], c1[4 * u + 2], c1[4 * u + 3]});
        WAIT_LGKM0(); sig(C_EX0 + br * 2 + tp, lane); waitc<0>(C_EX0 + br * 2 + tp, 2);
#pragma unroll
        for (int u = 0; u < 4; ++u) {
            const f32x4 s = lds_rdf4(xb_ + ((w ^ 2) * 4 + u) * 1024 + lane * 16);
#pragma unroll
            for (int i = 0; i < 4; ++i) c0[4 * u + i] += s[i];
        }
        const unsigned vo = (tto * 32 + (lane & 31)) * 1792 + (hi ? 16 : 0), so = br * 896 + ct * 128 + kh * 64;
#pragma unroll
        for (int gp = 0; gp < 2; ++gp) {
            auto s0 = __builtin_amdgcn_permlane32_swap(pk2(c0[8 * gp], c0[8 * gp + 1]), pk2(c0[8 * gp + 4], c0[8 * gp + 5]), false, false);
            auto s1 = __builtin_amdgcn_permlane32_swap(pk2(c0[8 * gp + 2], c0[8 * gp + 3]), pk2(c0[8 * gp + 6], c0[8 * gp + 7]), false, false);
            st16a<0>(rout, (u32x4){s0[0], s1[0], s0[1], s1[1]}, vo + gp * 32, so);
        }
        STAMP(12);
    }
    STAMP_FLUSH();
    if (wid == 1 * 4) { const unsigned d_ = ((unsigned)__builtin_amdgcn_readlane(stv, 1) - (unsigned)__builtin_amdgcn_readlane(stv, 0)) / 1;
        unsigned long long r0_ = __builtin_amdgcn_s_memrealtime(), dur_ = (d_ > 20000u ? 20000u : d_) * 100ull;
        if (__builtin_amdgcn_readlane(stv, 1) != 0) while (__builtin_amdgcn_s_memrealtime() - r0_ < dur_) __builtin_amdgcn_s_sleep(64); }
    asm volatile(".Lpf_end:");
}

extern "C" void kernel_launch(void* const* d_in, const int* in_sizes, int n_in, void* d_out, int out_size, void* d_ws, size_t ws_size,
                              hipStream_t stream) {
    (void)in_sizes; (void)n_in; (void)out_size; (void)ws_size;
    (void)hipFuncSetAttribute((const void*)fused_fwd, hipFuncAttributeMaxDynamicSharedMemorySize, LDS_BYTES);
    fused_fwd<<<256, 512, LDS_BYTES, stream>>>(d_in[0], d_in[1], d_in[2], d_in[3], d_in[4], d_in[5], d_in[6], d_out, d_ws);
}
```

```cpp
#include <hip/hip_runtime.h>
#include <hip/hip_bf16.h>
#include <stdint.h>
#include <type_traits>

typedef __attribute__((ext_vector_type(8))) __bf16 bf16x8;
typedef __attribute__((ext_vector_type(2))) __bf16 bf16x2;
typedef __attribute__((ext_vector_type(2))) float f32x2;
typedef __attribute__((ext_vector_type(4))) float f32x4;
typedef __attribute__((ext_vector_type(16))) float f32x16;
typedef __attribute__((ext_vector_type(4))) unsigned u32x4;
typedef __attribute__((ext_vector_type(2))) unsigned u32x2;
typedef __attribute__((ext_vector_type(4))) short s16x4;
typedef __amdgpu_buffer_rsrc_t rsrc_t;
#define LAS __attribute__((address_space(3)))
#define DI __device__ __forceinline__

constexpr unsigned WS_EPOCH = 0;
constexpr unsigned WS_F1 = 4096;
constexpr unsigned WS_F2 = WS_F1 + 1024 * 128;
constexpr unsigned WS_F3 = WS_F2 + 512 * 128;
constexpr unsigned WS_FO = WS_F3 + 256 * 128;
constexpr unsigned WS_QKV = 1u << 20;
constexpr unsigned WS_KVP = WS_QKV + (6u << 20);
constexpr unsigned WS_KVR = WS_KVP + (2u << 20);
constexpr unsigned WS_O = WS_KVR + (1u << 20);
constexpr unsigned WS_DBG = WS_O + (4u << 20);
constexpr unsigned WS_BYTES = WS_DBG + (1u << 20);

constexpr unsigned SLOT = 37888;
constexpr unsigned L_R0 = 0;
constexpr unsigned L_R1 = 57856;
constexpr unsigned L_R2 = L_R1 + 32768;
constexpr unsigned L_QX = L_R2 + 49152;
constexpr unsigned L_QB = L_R2;
constexpr unsigned L_KB = L_QB + 16384;
constexpr unsigned L_PHI = 4 * SLOT + 64;
constexpr unsigned L_JUNK = L_PHI + 4096;
constexpr unsigned L_CNT = L_JUNK + 1024;
constexpr unsigned L_TAG = L_CNT + 256;
constexpr unsigned LDS_BYTES = L_TAG + 16;
enum { C_LAND0 = 24, C_DONE0 = 32, C_QB = 8, C_GDONE = 9, C_QX = 10, C_PV = 11, C_KVDONE = 12, C_WO0 = 13, C_WOFREE = 15, C_EX0 = 16, C_CMB = 20, C_ATT = 21 };

#define MAP_SEP 0
constexpr unsigned SPIN_CAP = 1u << 18;
constexpr float QSCALE = 0.18898223650461362f * 1.4426950408889634f;
constexpr float LOG2E = 1.4426950408889634f;
constexpr float REF_SHIFT = 100.f;

#define MFMA(a, b, c) __builtin_amdgcn_mfma_f32_32x32x16_bf16((a), (b), (c), 0, 0, 0)
#define CBAR() asm volatile("" ::: "memory")
#define WAIT_VM(n) asm volatile("s_waitcnt vmcnt(" #n ")" ::: "memory")
#define WAIT_LGKM0() asm volatile("s_waitcnt lgkmcnt(0)" ::: "memory")
#define SBAR() __builtin_amdgcn_sched_barrier(0)

#define STAMP_DECL
#define STAMP(i) do {} while (0)
#define STAMPC(i) do {} while (0)
#define STAMP_FLUSH() do {} while (0)
#define FSTAMP(i) do {} while (0)
#define FSTAMPC(i) do {} while (0)
#define PIN(x) asm volatile("" : "+v"(x))

DI unsigned pk2(float a, float b) { f32x2 v = {a, b}; return __builtin_bit_cast(unsigned, __builtin_convertvector(v, bf16x2)); }
DI u32x4 pk8(const f32x16& a, int s) {
    return (u32x4){pk2(a[8 * s], a[8 * s + 1]), pk2(a[8 * s + 2], a[8 * s + 3]), pk2(a[8 * s + 4], a[8 * s + 5]), pk2(a[8 * s + 6], a[8 * s + 7])}; }
DI bf16x8 asbf(u32x4 v) { return __builtin_bit_cast(bf16x8, v); }
DI float swap_max(float m) { auto r = __builtin_amdgcn_permlane32_swap(__float_as_uint(m), __float_as_uint(m), false, false);
    return __builtin_fmaxf(__uint_as_float(r[0]), __uint_as_float(r[1])); }
DI float swap_min(float m) { auto r = __builtin_amdgcn_permlane32_swap(__float_as_uint(m), __float_as_uint(m), false, false);
    return __builtin_fminf(__uint_as_float(r[0]), __uint_as_float(r[1])); }
DI float swap_sum(float m) { auto r = __builtin_amdgcn_permlane32_swap(__float_as_uint(m), __float_as_uint(m), false, false);
    return __uint_as_float(r[0]) + __uint_as_float(r[1]); }
DI float from_hi(float m) { auto r = __builtin_amdgcn_permlane32_swap(__float_as_uint(m), __float_as_uint(m), false, false);
    return __uint_as_float(r[1]); }
DI float max16(const f32x16& s) {
    float a = __builtin_fmaxf(__builtin_fmaxf(s[0], s[1]), s[2]), b = __builtin_fmaxf(__builtin_fmaxf(s[3], s[4]), s[5]);
    a = __builtin_fmaxf(__builtin_fmaxf(a, s[6]), s[7]); b = __builtin_fmaxf(__builtin_fmaxf(b, s[8]), s[9]);
    a = __builtin_fmaxf(__builtin_fmaxf(a, s[10]), s[11]); b = __builtin_fmaxf(__builtin_fmaxf(b, s[12]), s[13]);
    a = __builtin_fmaxf(__builtin_fmaxf(a, s[14]), s[15]); return __builtin_fmaxf(a, b); }
DI float min16(const f32x16& s) {
    float a = __builtin_fminf(__builtin_fminf(s[0], s[1]), s[2]), b = __builtin_fminf(__builtin_fminf(s[3], s[4]), s[5]);
    a = __builtin_fminf(__builtin_fminf(a, s[6]), s[7]); b = __builtin_fminf(__builtin_fminf(b, s[8]), s[9]);
    a = __builtin_fminf(__builtin_fminf(a, s[10]), s[11]); b = __builtin_fminf(__builtin_fminf(b, s[12]), s[13]);
    a = __builtin_fminf(__builtin_fminf(a, s[14]), s[15]); return __builtin_fminf(a, b); }

DI void dma(rsrc_t rs, unsigned voff, unsigned soff, unsigned lds) {
    asm volatile("s_mov_b32 m0, %0\n\ts_nop 0\n\tbuffer_load_dwordx4 %1, %2, %3 offen lds" :: "s"(lds), "v"(voff), "s"(rs), "s"(soff) : "memory"); }
DI u32x4 lds_rd128(unsigned a) { return *(LAS const u32x4*)(uintptr_t)a; }
DI f32x4 lds_rdf4(unsigned a) { return *(LAS const f32x4*)(uintptr_t)a; }
DI void lds_wr128(unsigned a, u32x4 v) { *(LAS u32x4*)(uintptr_t)a = v; }
DI void lds_wrf4(unsigned a, f32x4 v) { *(LAS f32x4*)(uintptr_t)a = v; }
DI void lds_wr64(unsigned a, u32x2 v) { *(LAS u32x2*)(uintptr_t)a = v; }
DI bf16x8 lds_tr2(unsigned a0, unsigned a1) {
    s16x4 lo = __builtin_amdgcn_ds_read_tr16_b64_v4i16((LAS s16x4*)(uintptr_t)a0), hi = __builtin_amdgcn_ds_read_tr16_b64_v4i16((LAS s16x4*)(uintptr_t)a1);
    return __builtin_bit_cast(bf16x8, __builtin_shufflevector(lo, hi, 0, 1, 2, 3, 4, 5, 6, 7)); }
DI void sig(int c, int) {
    asm volatile("s_mov_b64 exec, 1\n\tds_add_u32 %0, %1\n\ts_mov_b64 exec, -1" :: "v"(L_CNT + 4 * c), "v"(1u) : "memory"); }
template <int SLP = 1> DI void waitc(int c, unsigned n) {
    unsigned v, sv, it;
    asm volatile("s_mov_b32 %2, %5\n"
                 "1:\n\tds_read_b32 %0, %3\n\ts_waitcnt lgkmcnt(0)\n\tv_readfirstlane_b32 %1, %0\n\ts_cmp_ge_u32 %1, %4\n\ts_cbranch_scc1 2f\n\t"
                 "s_sleep %6\n\ts_sub_u32 %2, %2, 1\n\ts_cbranch_scc0 1b\n2:"
                 : "=&v"(v), "=&s"(sv), "=&s"(it) : "v"(L_CNT + 4 * c), "i"(n), "i"(SPIN_CAP), "i"(SLP) : "memory", "scc"); }
DI unsigned rd_cnt(int c) { return *(volatile LAS const unsigned*)(uintptr_t)(L_CNT + 4 * c); }
DI u32x2 rd_tag() { const u32x2 t = *(LAS const u32x2*)(uintptr_t)L_TAG;
    return (u32x2){(unsigned)__builtin_amdgcn_readfirstlane(t[0]), (unsigned)__builtin_amdgcn_readfirstlane(t[1])}; }
DI u32x4 ld16(rsrc_t rs, unsigned voff, unsigned soff) { return __builtin_amdgcn_raw_buffer_load_b128(rs, voff, soff, 16); }
template <int AUX> DI void st16a(rsrc_t rs, u32x4 v, unsigned voff, unsigned soff) {
    __builtin_amdgcn_raw_buffer_store_b128(v, rs, voff, soff, AUX); asm volatile("s_nop 1" :: "v"(v) : "memory"); }
DI void st16(rsrc_t rs, u32x4 v, unsigned voff, unsigned soff) { st16a<16>(rs, v, voff, soff); }
DI u32x2 ldflag(rsrc_t rs, unsigned voff) { return __builtin_amdgcn_raw_buffer_load_b64(rs, voff, 0, 16); }
DI bool tagok(u32x2 f, u32x2 tag) {
    return __builtin_amdgcn_ballot_w64(__builtin_bit_cast(unsigned long long, f) == __builtin_bit_cast(unsigned long long, tag)) == ~0ull; }
DI void poll(rsrc_t rs, unsigned voff, u32x2 tag) {
    CBAR();
#pragma clang loop unroll(disable)
    for (unsigned it = 0; it < SPIN_CAP; ++it) { u32x2 f = ldflag(rs, voff); if (tagok(f, tag)) break; __builtin_amdgcn_s_sleep(2); CBAR(); }
    CBAR(); }
DI void post(rsrc_t rs, unsigned off, u32x2 tag, int lane) {
    WAIT_VM(0);
    if (lane == 0) __builtin_amdgcn_raw_buffer_store_b64(tag, rs, off, 0, 16);
    CBAR(); }

DI u32x4 lds_tr2u(unsigned a) { return __builtin_bit_cast(u32x4, lds_tr2(a, a + 336)); }
DI void sigv(unsigned addr) { asm volatile("s_mov_b64 exec, 1\n\tds_add_u32 %0, %1\n\ts_mov_b64 exec, -1" :: "v"(addr), "v"(1u) : "memory"); }
template <int ND, class Start>
DI void gemm6(unsigned xa0, unsigned wa0, unsigned wc0, f32x16 (&acc)[6], Start start) {
    u32x4 f[2][3], x[2][2];
    unsigned xa = xa0, x1 = xa0 ^ 32, x2, x3, wa = wa0, wc = wc0, wan, wcn, ca = L_CNT + 4 * C_LAND0, cv, so = 0, rf;
#pragma unroll
    for (int i = 0; i < 6; ++i) { acc[i] = f32x16{}; PIN(acc[i]); }
    PIN(xa); PIN(x1); PIN(wa); PIN(wc); PIN(ca);
    start();
    waitc<0>(C_LAND0, 4);
    x[0][0] = lds_rd128(xa); SBAR(); f[0][0] = lds_tr2u(wa); SBAR(); x[0][1] = lds_rd128(xa + 4096); SBAR();
    f[0][1] = lds_tr2u(wa + 56); SBAR(); f[0][2] = lds_tr2u(wc); SBAR();
#define G6MM(c, t, i) acc[3 * t + i] = (i >= 3 - ND) ? MFMA(asbf(x[c][t]), asbf(f[c][i]), acc[3 * t + i]) : MFMA(asbf(f[c][i]), asbf(x[c][t]), acc[3 * t + i]); SBAR()
#define G6STEP(c, XA, WA, WC, P0, P2, P5) do { \
        G6MM(c, 0, 0); P0; x[1 - c][0] = lds_rd128(XA); SBAR(); \
        G6MM(c, 1, 0); f[1 - c][0] = lds_tr2u(WA); SBAR(); \
        G6MM(c, 0, 1); x[1 - c][1] = lds_rd128((XA) + 4096); P2; SBAR(); \
        G6MM(c, 1, 1); f[1 - c][1] = lds_tr2u((WA) + 56); SBAR(); \
        G6MM(c, 0, 2); f[1 - c][2] = lds_tr2u(WC); SBAR(); \
        G6MM(c, 1, 2); P5; SBAR(); } while (0)
#define SPIN(x) asm volatile("" : "+s"(x))
#pragma unroll 1
    for (int g = 1; g < 8; ++g) {
        G6STEP(0, x1, wa + 5376, wc + 5376, (void)0, { x2 = xa ^ 64; PIN(x2); }, { x3 = xa ^ 96; PIN(x3); so = (g & 3) * SLOT; SPIN(so); });
        G6STEP(1, x2, wa + 2 * 5376, wc + 2 * 5376, (void)0, { xa = xa0 + so; PIN(xa); },
               { cv = *(volatile LAS const unsigned*)(uintptr_t)(ca + 4); wan = wa0 + so; PIN(wan); });
        G6STEP(0, x3, wa + 3 * 5376, wc + 3 * 5376, (void)0, { wcn = wc0 + so; PIN(wcn); }, { rf = __builtin_amdgcn_readfirstlane(cv); SPIN(rf); x1 = xa ^ 32; PIN(x1); });
        G6STEP(1, xa, wan, wcn, { if (__builtin_expect(rf < 4u, 0)) waitc<0>(C_LAND0 + g, 4); CBAR(); }, { ca += 4; PIN(ca); },
               sigv(ca + 4 * (C_DONE0 - C_LAND0 - 1)));
        wa = wan; wc = wcn;
    }
}
DI unsigned tr_addr128(unsigned base, int cb, int rowgrp, int lane) {
    const int q = (lane & 15) >> 2, byte = cb + 32 * ((lane >> 4) & 1) + 8 * (lane & 3);
    return base + (rowgrp + q) * 128 + ((((byte >> 4) ^ ((q >> 1) << 2)) & 7) << 4) + (byte & 8);
}
DI void feat(f32x16& p, f32x16& e, f32x16& g, bool hi) {
#pragma unroll
    for (int i = 12; i < 16; ++i) p[i] = hi ? p[0] : p[i];
    const float mx = swap_max(max16(p)) * LOG2E, mn = swap_min(min16(p)) * LOG2E;
#pragma unroll
    for (int i = 0; i < 16; i += 2) {
        const f32x2 pp = {p[i], p[i + 1]};
        const f32x2 a = __builtin_elementwise_fma(pp, (f32x2){LOG2E, LOG2E}, (f32x2){-mx, -mx}), c = __builtin_elementwise_fma(pp, (f32x2){-LOG2E, -LOG2E}, (f32x2){mn, mn});
        e[i] = __builtin_amdgcn_exp2f(a[0]); e[i + 1] = __builtin_amdgcn_exp2f(a[1]); g[i] = __builtin_amdgcn_exp2f(c[0]); g[i + 1] = __builtin_amdgcn_exp2f(c[1]);
    }
}

typedef __attribute__((ext_vector_type(16))) unsigned u32x16;
DI rsrc_t mk_rsrc(unsigned lo, unsigned hi, unsigned bytes) {
    return __builtin_amdgcn_make_buffer_rsrc((void*)(((unsigned long long)hi << 32) | lo), 0, bytes, 0x00020000); }
DI void read_wo(bf16x8 (&wf)[16][2], int kh, int rot, int lane) {
    const unsigned ta0 = tr_addr128(L_R0, 0, kh * 224 + 4 * (lane >> 5), lane), ta1 = tr_addr128(L_R0, 64, kh * 224 + 4 * (lane >> 5), lane);
#pragma unroll
    for (int k = 0; k < 8; ++k) {
        const unsigned ro = ((k + rot) & 7) * 28 * 128, b0 = ta0 + ro, b1 = ta1 + ro;
        wf[2 * k][0] = lds_tr2(b0, b0 + 1024); wf[2 * k][1] = lds_tr2(b1, b1 + 1024);
        wf[2 * k + 1][0] = lds_tr2(b0 + 2048, b0 + 3072); wf[2 * k + 1][1] = lds_tr2(b1 + 2048, b1 + 3072);
    }
}
DI u32x4 ones_row(u32x4 v, int lane) {
    if ((lane & 31) >= 28) { const unsigned c = (lane & 31) == 28 ? 0x3F803F80u : 0u; v = (u32x4){c, c, c, c}; }
    return v; }
DI f32x16 phi_pre(u32x4 k0, u32x4 k1, int hh, int lane, bool hi) {
    if (hi) { k1[2] = 0; k1[3] = 0; }
    const unsigned pb = L_PHI + hh * 1568 + (4 * (lane >> 5) + ((lane & 15) >> 2)) * 56 + 32 * ((lane >> 4) & 1) + 8 * (lane & 3);
    bf16x8 f0 = lds_tr2(pb, pb + 8 * 56);
    u32x4 f1 = __builtin_bit_cast(u32x4, lds_tr2(pb + 16 * 56, pb + 24 * 56));
    if (hi) { f1[2] = 0; f1[3] = 0; }
    f32x16 p = MFMA(f0, asbf(k0), f32x16{});
    return MFMA(asbf(f1), asbf(k1), p);
}

__global__ __launch_bounds__(512, 2) void fused_fwd(const void*, const void*, const void*, const void*, const void*, const void*, const void*, void*, void*) {
    STAMP_DECL; STAMP(0); STAMPC(30);
    u32x16 ka; u32x2 kb;
    asm volatile("s_load_dwordx16 %0, %2, 0x0\n\ts_load_dwordx2 %1, %2, 0x40" : "=&s"(ka), "=&s"(kb) : "s"(__builtin_amdgcn_kernarg_segment_ptr()));
    const int tid = threadIdx.x, lane = tid & 63, wid = __builtin_amdgcn_readfirstlane(tid >> 6);
    const int b = blockIdx.x, xcd = b & 7, r = b >> 3, w = wid & 3, role = wid >> 2;
    const int isb = r & 1, ml = (r >> 1) & 3, nl = r >> 3;
    const int mt = (xcd & 3) * 4 + ml, nt = (xcd >> 2) * 4 + nl;
    const bool hi = lane >= 32;
    int g0 = 2 * ml + nl; g0 -= (g0 >= 7) ? 7 : 0;
    const int ttg = mt * 4 + w;
    const int head = 2 * xcd + (r >> 4), qt = r & 15;
    const int ct = r >> 2, rt = xcd * 4 + (r & 3);
    u32x2 tag;
    bf16x8 wf[16][2];
    unsigned pfd = 0;

    unsigned xvo, wvo[6];
    if (role) {
        __builtin_amdgcn_s_setprio(2);
        xvo = (w * 8 + (lane >> 3)) * 896 + ((((lane & 7) ^ (lane >> 4) ^ ((w & 1) << 2)) & 7) << 4);
        PIN(xvo);
#pragma unroll
        for (int i = 0; i < 6; ++i) {
            const int c = 64 * (w + 4 * i) + lane, rho = (c * 3121) >> 16, ch = c - 21 * rho, win = (ch * 37) >> 8;
            const int krow = (rho & ~15) | ((rho & 3) << 2) | ((rho >> 2) & 3);
            wvo[i] = krow * 2688 + ch * 16 + (isb ? win * (896 - 112) : 0);
            PIN(wvo[i]);
        }
    }
    if (wid == 0) {
        unsigned long long pc; unsigned len;
        asm volatile("s_getpc_b64 %0\n.Lpf_begin%=:\n\ts_mov_b32 %1, .Lpf_end-.Lpf_begin%=" : "=s"(pc), "=s"(len));
        const rsrc_t rc = __builtin_amdgcn_make_buffer_rsrc((void*)pc, 0, len, 0x00020000);
        asm volatile("s_nop 4\n\tbuffer_load_dword %0, %1, %2, 0 offen" : "+v"(pfd) : "v"(lane < 12 ? (r * 12 + lane) * 128 : 0x7FFFFFF0), "s"(rc) : "memory");
        *(LAS unsigned*)(uintptr_t)(L_CNT + 4 * lane) = lane == C_LAND0 + 7 ? 4u : 0u;
    }
    asm volatile("s_waitcnt lgkmcnt(0)" : "+s"(ka), "+s"(kb) :: "memory");
    const rsrc_t rws = mk_rsrc(kb[0], kb[1], WS_BYTES);
    u32x4 pe[2][4];
    if (role) {
        const rsrc_t rx = mk_rsrc(isb ? ka[6] : ka[0], isb ? ka[7] : ka[1], 2048 * 896), rw = mk_rsrc(isb ? ka[8] : ka[2], isb ? ka[9] : ka[3], 448 * 2688);
        const unsigned xs0 = mt * 114688, ws0 = (isb ? 112 : 336) * nt;
        auto feed = [&](int g) {
            int ga = g + g0; ga -= (ga >= 7) ? 7 : 0;
            const unsigned xs = xs0 + ga * 128, ws = ws0 + ga * 64 * 2688, l = (g & 3) * SLOT + w * 1024;
#pragma unroll
            for (int j = 0; j < 4; ++j) dma(rx, xvo, xs + j * 32 * 896, l + j * 4096);
#pragma unroll
            for (int j = 0; j < 5; ++j) dma(rw, wvo[j], ws, l + 16384 + j * 4096);
            if (w == 0) dma(rw, wvo[5], ws, l + 16384 + 5 * 4096); };
        FSTAMP(18);
        dma(mk_rsrc(ka[10], ka[11], 16 * 1568), lane * 16, nt * 3136 + w * 1024, L_PHI + w * 1024);
        feed(0); feed(1);
        asm volatile("s_barrier" ::: "memory");
#pragma unroll 1
        for (int g = 0; g < 6; ++g) {
            if (w == 0) WAIT_VM(10); else WAIT_VM(9);
            sig(C_LAND0 + g, lane);
            if (g == 0) STAMP(1);
            if (g < 5) {
                if (g >= 2) waitc<0>(C_DONE0 + g - 2, 4);
                feed(g + 2);
            }
        }
        {
            const rsrc_t rt_ = mk_rsrc(w < 2 ? ka[4] : ka[12], w < 2 ? ka[5] : ka[13], 448 * 896);
            asm volatile("s_mov_b32 m0, %0\n\ts_nop 3\n\tbuffer_load_dword %1, %2, 0 offen lds"
                         :: "s"(L_JUNK), "v"(lane < 49 ? (r * 98 + (w & 1) * 49 + lane) * 128 : 0x7FFFFF00), "s"(rt_) : "memory");
        }
        WAIT_VM(1);
        sig(C_LAND0 + 6, lane);
        STAMP(2);
        tag = rd_tag();
        if (isb) {
            waitc<1>(C_QB + 0, 4);
#pragma unroll
            for (int hh = 0; hh < 2; ++hh) {
                const unsigned qa = L_QB + ((w * 2 + hh) * 2) * 1024 + lane * 16;
                f32x16 p = phi_pre(lds_rd128(qa), lds_rd128(qa + 1024), hh, lane, hi), e, g;
                feat(p, e, g, hi);
                pe[hh][0] = pk8(e, 0); pe[hh][1] = pk8(e, 1); pe[hh][2] = pk8(g, 0); pe[hh][3] = pk8(g, 1);
            }
        }
        STAMP(3);
    } else {
        f32x16 acc[6];
        const int tp = w & 1, sg = w >> 1;
        const int rr = tp * 64 + (lane & 31);
        const unsigned xa0 = rr * 128 + ((((lane >> 5) ^ (rr >> 1)) & 7) << 4);
        const unsigned lb = 16384 + (4 * ((lane & 15) >> 2) + 2 * (lane >> 5)) * 336 + 32 * ((lane >> 4) & 1) + 8 * (lane & 3);
        const unsigned wa0 = lb + 56 * (isb ? 2 - 2 * sg : 3 * sg), wc0 = lb + 56 * (isb ? 4 + sg : 3 * sg + 2);
        const int G = 2 * nt + sg;
        if (w == 0) {
            const u32x2 ep = ldflag(rws, WS_EPOCH);
            const unsigned long long e = (((unsigned long long)ep[1] << 32) | ep[0]) + 1;
            lds_wr64(L_TAG, (u32x2){(unsigned)e, (unsigned)(e >> 32)});
            WAIT_VM(0);
            asm volatile("" :: "v"(pfd));
        }
        auto start = [&]() { asm volatile("s_waitcnt lgkmcnt(0)\n\ts_barrier" ::: "memory"); FSTAMPC(22); FSTAMPC(23); };
        if (isb || G == 10) gemm6<1>(xa0, wa0, wc0, acc, start); else if (G < 10) gemm6<0>(xa0, wa0, wc0, acc, start); else gemm6<3>(xa0, wa0, wc0, acc, start);
        tag = rd_tag();
        FSTAMPC(24);
        STAMP(2);
        if (!isb) {
#pragma unroll
            for (int i = 0; i < 3; ++i) {
                const int sl = 3 * G + i, kind = sl >> 4;
#pragma unroll
                for (int t = 0; t < 2; ++t) {
                    f32x16& a = acc[3 * t + i];
                    if (kind == 0) {
#pragma unroll
                        for (int k = 0; k < 16; ++k) a[k] *= QSCALE;
                    }
                    u32x4 v0 = pk8(a, 0), v1 = pk8(a, 1);
                    if (kind == 2) { v0 = ones_row(v0, lane); v1 = ones_row(v1, lane); }
                    else if (hi) { v1[2] = kind ? 0x00003F80u : 0u; v1[3] = 0u; }
                    const unsigned so = WS_QKV + (((sl & 15) * 64 + mt * 4 + 2 * tp + t) * 6 + kind * 2) * 1024;
                    st16(rws, v0, lane * 16, so); st16(rws, v1, lane * 16 + 1024, so);
                }
            }
            post(rws, WS_F1 + (G * 32 + mt * 2 + tp) * 128, tag, lane);
            waitc<0>(C_DONE0 + 6, 4);
        } else {
            waitc<0>(C_DONE0 + 6, 4);
            u32x4 kf[2][2];
#pragma unroll
            for (int t = 0; t < 2; ++t)
#pragma unroll
                for (int s = 0; s < 2; ++s) {
#pragma unroll
                    for (int hh = 0; hh < 2; ++hh) {
                        const u32x4 v = pk8(acc[3 * t + hh], s);
                        if (t == 0) kf[hh][s] = v;
                        if (sg) lds_wr128(L_QB + (((2 * tp + t) * 2 + hh) * 2 + s) * 1024 + lane * 16, v);
                        else if (t == 1) lds_wr128(L_KB + ((tp * 2 + hh) * 2 + s) * 1024 + lane * 16, v);
                    }
                    lds_wr128(L_R0 + ((sg * 4 + 2 * tp + t) * 2 + s) * 1024 + lane * 16, ones_row(pk8(acc[3 * t + 2], s), lane));
                }
            WAIT_LGKM0(); sig(C_QB, lane);
            if (sg) {
                waitc<0>(C_QB, 4);
#pragma unroll
                for (int hh = 0; hh < 2; ++hh)
#pragma unroll
                    for (int s = 0; s < 2; ++s) kf[hh][s] = lds_rd128(L_KB + ((tp * 2 + hh) * 2 + s) * 1024 + lane * 16);
            }
            const int tl = 2 * tp + sg;
            __builtin_amdgcn_s_setprio(3);
            const int tk = lane & 31, pi = (((tk >> 1) & 1) << 3) | (tk & 1) | (((tk >> 2) & 3) << 1);
#pragma unroll
            for (int hh = 0; hh < 2; ++hh) {
                f32x16 p = phi_pre(kf[hh][0], kf[hh][1], hh, lane, hi), e, g;
                feat(p, e, g, hi);
#pragma unroll
                for (int i = 12; i < 16; ++i) { e[i] = hi ? 0.f : e[i]; g[i] = hi ? 0.f : g[i]; }
                float se = 0.f, sg = 0.f;
#pragma unroll
                for (int i = 0; i < 16; ++i) { se += e[i]; sg += g[i]; }
                se = __builtin_amdgcn_rcpf(swap_sum(se)); sg = __builtin_amdgcn_rcpf(swap_sum(sg));
#pragma unroll
                for (int u = 0; u < 8; ++u) {
                    const int piece = 8 * (u >> 2) + 2 * (u & 3) + (lane >> 5), k = 4 * (u & 3);
                    const f32x16& z = (u >> 2) ? g : e; const float sc = (u >> 2) ? sg : se;
                    lds_wr64(L_R1 + hh * 16384 + (tl * 32 + tk) * 128 + ((piece ^ pi) << 3), (u32x2){pk2(z[k] * sc, z[k + 1] * sc), pk2(z[k + 2] * sc, z[k + 3] * sc)});
                }
            }
            WAIT_LGKM0(); sig(C_PV, lane);
            __builtin_amdgcn_s_setprio(0);
        }
        STAMP(3);
    }
    FSTAMP(4);

    if (role) {
        const int wc = (lane & 7) ^ (((lane >> 4) & 1) << 2);
        const unsigned wovo = (lane >> 3) * 896 + wc * 16;
        if (isb) {
            waitc<0>(C_PV, 4);
            const int hh = w >> 1, ft = w & 1, q = (lane & 15) >> 2, P = 8 * ft + 4 * ((lane >> 4) & 1) + (lane & 3);
            bf16x8 fa[8], fb[8];
#pragma unroll
            for (int st = 0; st < 8; ++st) {
                const int t0 = 16 * (st & 1) + 4 * (lane >> 5) + q, t1 = t0 + 8;
                const int p0 = (((t0 >> 1) & 1) << 3) | (t0 & 1) | (((t0 >> 2) & 3) << 1), p1 = (((t1 >> 1) & 1) << 3) | (t1 & 1) | (((t1 >> 2) & 3) << 1);
                const unsigned ib = L_R1 + hh * 16384 + (st >> 1) * 4096;
                fa[st] = lds_tr2(ib + t0 * 128 + ((P ^ p0) << 3), ib + t1 * 128 + ((P ^ p1) << 3));
                fb[st] = asbf(lds_rd128(L_R0 + ((hh * 4 + (st >> 1)) * 2 + (st & 1)) * 1024 + lane * 16));
            }
            SBAR();
            f32x16 kv = {};
#pragma unroll
            for (int st = 0; st < 8; ++st) kv = MFMA(fa[st], fb[st], kv);
            WAIT_LGKM0(); sig(C_KVDONE, lane);
            const int hk = 2 * nt + hh;
#pragma unroll
            for (int u = 0; u < 4; ++u)
                st16(rws, __builtin_bit_cast(u32x4, (f32x4){kv[4 * u], kv[4 * u + 1], kv[4 * u + 2], kv[4 * u + 3]}), lane * 16 + u * 1024, WS_KVP + ((hk * 16 + mt) * 8 + ft * 4) * 1024);
            FSTAMP(19);
            post(rws, WS_F2 + ((hk * 16 + mt) * 2 + ft) * 128, tag, lane);
            waitc<0>(C_KVDONE, 4);
        }
        STAMP(5);
        if (r < 28) {
            waitc<4>(C_QX, 4);
            if (w == 0) *(LAS u32x2*)(uintptr_t)(L_R0 + 57344 + 8 * lane) = (u32x2){0u, 0u};
            const rsrc_t rwob = mk_rsrc(ka[12], ka[13], 448 * 896);
#pragma unroll
            for (int i = 0; i < 14; ++i) dma(rwob, wovo, ct * 128 + (w + 4 * i) * 8 * 896, L_R0 + (w + 4 * i) * 1024);
            WAIT_VM(0); WAIT_LGKM0(); sig(C_WO0 + 1, lane); waitc<2>(C_WO0 + 1, 4);
            read_wo(wf, w >> 1, ct, lane);
            WAIT_LGKM0(); sig(C_WOFREE, lane);
        }
        if (w == 3) {
            const int hr = head, i = lane & 15, pg = lane >> 4;
            poll(rws, WS_F2 + ((hr * 16 + i) * 2 + (qt >> 3)) * 128, tag);
            u32x4 pl[8];
#pragma unroll
            for (int p = 0; p < 4; ++p) {
                const unsigned vo = (pg * 4 + p) * 8192 + (16 * (qt & 3) + i) * 16, so = WS_KVP + hr * 131072 + (qt >> 2) * 2048;
                pl[2 * p] = ld16(rws, vo, so); pl[2 * p + 1] = ld16(rws, vo + 1024, so);
            }
            SBAR();
            f32x4 s0 = {}, s1 = {};
#pragma unroll
            for (int p = 0; p < 4; ++p) { s0 += __builtin_bit_cast(f32x4, pl[2 * p]); s1 += __builtin_bit_cast(f32x4, pl[2 * p + 1]); }
            float v[8] = {s0[0], s0[1], s0[2], s0[3], s1[0], s1[1], s1[2], s1[3]};
#pragma unroll
            for (int k = 0; k < 8; ++k) {
                auto t = __builtin_amdgcn_permlane16_swap(__float_as_uint(v[k]), __float_as_uint(v[k]), false, false);
                v[k] = swap_sum(__uint_as_float(t[0]) + __uint_as_float(t[1]));
            }
            u32x4 o = {pk2(v[0], v[1]), pk2(v[2], v[3]), pk2(v[4], v[5]), pk2(v[6], v[7])};
            const int cl = 16 * (qt & 3) + i;
            if ((cl & 31) == 29) { const unsigned c = ((qt >> 2) & 1) && cl >= 32 ? 0u : 0x3F803F80u; o = (u32x4){0x3F803F80u, 0x3F803F80u, c, c}; }
            st16(rws, o, lane < 16 ? i * 16 : 0x7FFFFF00u, WS_KVR + (hr * 256 + qt * 16) * 16);
            post(rws, WS_F3 + (hr * 16 + qt) * 128, tag, lane);
        }
        STAMP(6);
        if (r < 28) {
            const rsrc_t rwoa = mk_rsrc(ka[4], ka[5], 448 * 896);
            waitc<2>(C_WOFREE, 4);
#pragma unroll
            for (int i = 0; i < 14; ++i) dma(rwoa, wovo, ct * 128 + (w + 4 * i) * 8 * 896, L_R0 + (w + 4 * i) * 1024);
            WAIT_VM(0); sig(C_WO0, lane);
        }
        if (isb) {
            poll(rws, WS_F3 + (2 * nt * 16 + (lane & 31)) * 128, tag);
            u32x4 kf[2][4];
#pragma unroll
            for (int hh = 0; hh < 2; ++hh)
#pragma unroll
                for (int f = 0; f < 4; ++f) kf[hh][f] = ld16(rws, lane * 16 + f * 1024, WS_KVR + (2 * nt + hh) * 4096);
            SBAR();
#pragma unroll
            for (int hh = 0; hh < 2; ++hh) {
                f32x16 np = MFMA(asbf(kf[hh][0]), asbf(pe[hh][0]), f32x16{}); np = MFMA(asbf(kf[hh][1]), asbf(pe[hh][1]), np);
                f32x16 nm = MFMA(asbf(kf[hh][2]), asbf(pe[hh][2]), f32x16{}); nm = MFMA(asbf(kf[hh][3]), asbf(pe[hh][3]), nm);
                const float rp = __builtin_amdgcn_rcpf(np[13]), rm = __builtin_amdgcn_rcpf(nm[13]);
                const float rd = __builtin_amdgcn_rcpf(__builtin_fmaf(rp, np[12], rm * nm[12]) + 1e-6f);
                const float cp = from_hi(rp * rd), cm = from_hi(rm * rd);
                f32x16 o;
#pragma unroll
                for (int i = 0; i < 16; ++i) o[i] = __builtin_fmaf(cp, np[i], cm * nm[i]);
                u32x4 o0 = pk8(o, 0), o1 = pk8(o, 1);
                if (hi) { o1[2] = 0; o1[3] = 0; }
                const unsigned so = WS_O + (2u << 20) + ((ttg * 16 + 2 * nt + hh) * 2) * 1024;
                st16(rws, o0, lane * 16, so); st16(rws, o1, lane * 16 + 1024, so);
            }
            WAIT_VM(0);
            if (lane < 2) __builtin_amdgcn_raw_buffer_store_b64(tag, rws, WS_FO + (1024 + ttg * 16 + 2 * nt + lane) * 128, 0, 16);
            CBAR();
        }
        STAMP(7);
        if (b == 255 && w == 0) {
#pragma unroll 1
            for (int i = 0; i < 4; ++i) poll(rws, WS_F3 + (i * 64 + lane) * 128, tag);
            if (lane == 0) __builtin_amdgcn_raw_buffer_store_b64(tag, rws, WS_EPOCH, 0, 16);
        }
    } else {
        const int tq = qt * 4 + w;
        const int gq = (head * 43) >> 7, gk = ((16 + head) * 43) >> 7, gv = ((32 + head) * 43) >> 7;
        poll(rws, WS_F1 + (lane >= 16 ? gq * 32 + (tq >> 1) : ((lane & 8) ? gv : gk) * 32 + 8 * w + (lane & 7)) * 128, tag);
        STAMP(5);
        const unsigned hb = WS_QKV + head * 64 * 6144, kvb = hb + 2048;
        const unsigned kb0 = kvb + (16 * w + (qt & 3)) * 6144, kb1 = kvb + (16 * w + ((qt + 1) & 3)) * 6144,
                       kb2 = kvb + (16 * w + ((qt + 2) & 3)) * 6144, kb3 = kvb + (16 * w + ((qt + 3) & 3)) * 6144;
        const int rk0 = qt >> 2;
        unsigned vc = lane * 16 + rk0 * 24576;
        u32x4 kA[4], kB[4], kC[4], kD[4];
#define LD(d, f, v, sb) d[f] = ld16(rws, (v) + (f) * 1024, sb)
        u32x4 q0 = ld16(rws, lane * 16, hb + tq * 6144), q1 = ld16(rws, lane * 16 + 1024, hb + tq * 6144);
        LD(kA, 0, vc, kb0); LD(kA, 1, vc, kb0);
        SBAR();
        f32x16 O[4] = {};
        u32x4 pA0 = {}, pA1 = {}, pB0, pB1;
        kD[2] = (u32x4){}; kD[3] = (u32x4){};
        PIN(O[0]); PIN(O[1]); PIN(O[2]); PIN(O[3]); PIN(pA0); PIN(pA1); PIN(kD[2]); PIN(kD[3]);
        SBAR();
        bf16x8 Q[4][2];
        {
            f32x16 s = MFMA(asbf(kA[0]), asbf(q0), f32x16{}); s = MFMA(asbf(kA[1]), asbf(q1), s);
            SBAR(); FSTAMP(13);
            LD(kA, 2, vc, kb0); LD(kA, 3, vc, kb0); LD(kB, 0, vc, kb1); LD(kB, 1, vc, kb1); LD(kB, 2, vc, kb1); LD(kB, 3, vc, kb1);
            LD(kC, 0, vc, kb2); LD(kC, 1, vc, kb2); LD(kC, 2, vc, kb2); LD(kC, 3, vc, kb2);
            SBAR();
            lds_wr128(L_QX + (w * 2) * 1024 + lane * 16, q0);
            const float m = swap_max(max16(s)) + REF_SHIFT;
            u32x4 q1m = q1;
            if (hi) q1m[2] = pk2(-m, 0.f);
            lds_wr128(L_QX + (w * 2 + 1) * 1024 + lane * 16, q1m);
            WAIT_LGKM0(); sig(C_QX, lane); FSTAMP(14); waitc<0>(C_QX, 4);
#pragma unroll
            for (int a = 0; a < 4; ++a) { Q[a][0] = asbf(lds_rd128(L_QX + (a * 2) * 1024 + lane * 16)); Q[a][1] = asbf(lds_rd128(L_QX + (a * 2 + 1) * 1024 + lane * 16)); }
        }
        STAMP(6); STAMPC(20);
        f32x16 sA, sB;
        sA = MFMA(asbf(kA[0]), Q[0][0], f32x16{}); sA = MFMA(asbf(kA[1]), Q[0][1], sA);
        PIN(Q[1][0]); PIN(Q[1][1]); PIN(Q[2][0]); PIN(Q[2][1]); PIN(Q[3][0]); PIN(Q[3][1]);
#define EX4(S, i) S[i] = __builtin_amdgcn_exp2f(S[i]); S[i + 1] = __builtin_amdgcn_exp2f(S[i + 1]); S[i + 2] = __builtin_amdgcn_exp2f(S[i + 2]); S[i + 3] = __builtin_amdgcn_exp2f(S[i + 3])
#define STAGE(SC, SN, PC0, PC1, PP0, PP1, KN, an, VP, ap, F0, F1, F2, F3) do { \
        SN = MFMA(asbf(KN[0]), Q[an][0], f32x16{}); SBAR(); F0; SBAR(); EX4(SC, 0); PIN(SC); SBAR(); \
        SN = MFMA(asbf(KN[1]), Q[an][1], SN); SBAR(); F1; SBAR(); EX4(SC, 4); PC0[0] = pk2(SC[0], SC[1]); PC0[1] = pk2(SC[2], SC[3]); PIN(SC); PIN(PC0); SBAR(); \
        O[ap] = MFMA(asbf(VP[2]), asbf(PP0), O[ap]); SBAR(); F2; SBAR(); EX4(SC, 8); PC0[2] = pk2(SC[4], SC[5]); PC0[3] = pk2(SC[6], SC[7]); PIN(SC); PIN(PC0); SBAR(); \
        O[ap] = MFMA(asbf(VP[3]), asbf(PP1), O[ap]); SBAR(); F3; SBAR(); EX4(SC, 12); PC1[0] = pk2(SC[8], SC[9]); PC1[1] = pk2(SC[10], SC[11]); \
        PC1[2] = pk2(SC[12], SC[13]); PC1[3] = pk2(SC[14], SC[15]); PIN(PC1); SBAR(); } while (0)
#define NOP (void)0
#define ARRIVE(K) asm volatile("" : "+v"(K[0]), "+v"(K[1]), "+v"(K[2]), "+v"(K[3]))
#define TILE(KP, KC, KN, V, SB, G3) do { \
        STAGE(sA, sB, pB0, pB1, pA0, pA1, KC, 1, KP, 3, NOP, NOP, NOP, NOP); \
        STAGE(sB, sA, pA0, pA1, pB0, pB1, KC, 2, KC, 0, LD(KP, 0, V, SB), LD(KP, 1, V, SB), LD(KP, 2, V, SB), LD(KP, 3, V, SB)); \
        STAGE(sA, sB, pB0, pB1, pA0, pA1, KC, 3, KC, 1, NOP, NOP, NOP, ARRIVE(KN)); \
        STAGE(sB, sA, pA0, pA1, pB0, pB1, KN, 0, KC, 2, G3, NOP, NOP, NOP); } while (0)
#pragma unroll 1
        for (int it = 0; it < 4; ++it) {
            const unsigned vn = it < 3 ? lane * 16 + ((rk0 + it + 1) & 3) * 24576 : 0x7FFF0000u;
            TILE(kD, kA, kB, vc, kb3, NOP);
            TILE(kA, kB, kC, vn, kb0, NOP);
            TILE(kB, kC, kD, vn, kb1, NOP);
            TILE(kC, kD, kA, vn, kb2, NOP);
            vc = vn;
        }
        O[3] = MFMA(asbf(kD[2]), asbf(pA0), O[3]); O[3] = MFMA(asbf(kD[3]), asbf(pA1), O[3]);
        sig(C_ATT, lane);
        STAMP(7); STAMPC(21);
        f32x16 o;
#pragma unroll
        for (int a = 0; a < 4; ++a) {
            if (a != w) {
                const int sl = a * 3 + (w > a ? w - 1 : w);
#pragma unroll
                for (int u = 0; u < 4; ++u) lds_wrf4(L_R2 + (sl * 4 + u) * 1024 + lane * 16, (f32x4){O[a][4 * u], O[a][4 * u + 1], O[a][4 * u + 2], O[a][4 * u + 3]});
            } else o = O[a];
        }
        WAIT_LGKM0(); sig(C_CMB, lane); waitc<0>(C_CMB, 4);
        FSTAMPC(28);
        {
            f32x4 pr[12];
#pragma unroll
            for (int k = 0; k < 12; ++k) pr[k] = lds_rdf4(L_R2 + (w * 12 + k) * 1024 + lane * 16);
            SBAR();
#pragma unroll
            for (int k = 0; k < 12; ++k)
#pragma unroll
                for (int i = 0; i < 4; ++i) o[4 * (k & 3) + i] += pr[k][i];
        }
        float l = from_hi(o[12]);
        {
            f32x2 chk = {0.f, 0.f};
#pragma unroll
            for (int i = 0; i < 16; i += 2) chk = __builtin_elementwise_fma((f32x2){o[i], o[i + 1]}, (f32x2){0.f, 0.f}, chk);
            bool bad = !(chk[0] + chk[1] == 0.f) || !(l >= 1e-32f && l <= 1e30f);
            if (__any(bad)) {
                float mrun = -3.0e38f;
                o = f32x16{};
#pragma unroll 1
                for (int k2 = 0; k2 < 4; ++k2) {
                    poll(rws, WS_F1 + (((lane & 8) ? gv : gk) * 32 + 8 * k2 + (lane & 7)) * 128, tag);
#pragma unroll 1
                    for (int m2 = 0; m2 < 16; ++m2) {
                        const unsigned so = kvb + (k2 * 16 + m2) * 6144;
                        const u32x4 k0 = ld16(rws, lane * 16, so), k1 = ld16(rws, lane * 16 + 1024, so), v0 = ld16(rws, lane * 16 + 2048, so), v1 = ld16(rws, lane * 16 + 3072, so);
                        f32x16 s = MFMA(asbf(k0), asbf(q0), f32x16{}); s = MFMA(asbf(k1), asbf(q1), s);
                        const float mn = __builtin_fmaxf(mrun, swap_max(max16(s))), al = __builtin_amdgcn_exp2f(mrun - mn);
                        mrun = mn;
#pragma unroll
                        for (int i = 0; i < 16; ++i) { s[i] = __builtin_amdgcn_exp2f(s[i] - mn); o[i] *= al; }
                        o = MFMA(asbf(v0), asbf(pk8(s, 0)), o); o = MFMA(asbf(v1), asbf(pk8(s, 1)), o);
                    }
                }
                l = from_hi(o[12]);
            }
        }
        const float rl = __builtin_amdgcn_rcpf(l);
#pragma unroll
        for (int i = 0; i < 16; ++i) o[i] *= rl;
        u32x4 o0 = pk8(o, 0), o1 = pk8(o, 1);
        if (hi) { o1[2] = 0; o1[3] = 0; }
        const unsigned so = WS_O + ((tq * 16 + head) * 2) * 1024;
        st16(rws, o0, lane * 16, so); st16(rws, o1, lane * 16 + 1024, so);
        STAMP(8); FSTAMPC(27);
        if (r < 28) { waitc<0>(C_WO0, 4); read_wo(wf, w >> 1, ct, lane); }
        post(rws, WS_FO + (tq * 16 + head) * 128, tag, lane);
        FSTAMP(9);
    }

    if (r < 28) {
        const int br = role, tp = w & 1, kh = w >> 1, tto = rt * 2 + tp;
        const rsrc_t rout = mk_rsrc(ka[14], ka[15], 2048 * 1792);
        poll(rws, WS_FO + (br * 1024 + tto * 16 + kh * 8 + (lane & 7)) * 128, tag);
        STAMP(10);
        u32x4 of[16];
        const unsigned ob = WS_O + br * (2u << 20) + (tto * 16 + kh * 8) * 2048;
#pragma unroll
        for (int k = 0; k < 16; ++k) of[k] = ld16(rws, lane * 16 + (k & 1) * 1024, ob + (((k >> 1) + ct) & 7) * 2048);
        SBAR();
        if (role) waitc<6>(C_ATT, 4);
        f32x16 c0 = {}, c1 = {};
#pragma unroll
        for (int k = 0; k < 16; ++k) { c0 = MFMA(wf[k][0], asbf(of[k]), c0); c1 = MFMA(wf[k][1], asbf(of[k]), c1); }
        FSTAMP(11);
        const unsigned xb_ = L_R1 + br * 16384;
        if (kh) { f32x16 t = c0; c0 = c1; c1 = t; }
#pragma unroll
        for (int u = 0; u < 4; ++u) lds_wrf4(xb_ + (w * 4 + u) * 1024 + lane * 16, (f32x4){c1[4 * u], c1[4 * u + 1], c1[4 * u + 2], c1[4 * u + 3]});
        WAIT_LGKM0(); sig(C_EX0 + br * 2 + tp, lane); waitc<0>(C_EX0 + br * 2 + tp, 2);
#pragma unroll
        for (int u = 0; u < 4; ++u) {
            const f32x4 s = lds_rdf4(xb_ + ((w ^ 2) * 4 + u) * 1024 + lane * 16);
#pragma unroll
            for (int i = 0; i < 4; ++i) c0[4 * u + i] += s[i];
        }
        const unsigned vo = (tto * 32 + (lane & 31)) * 1792 + (hi ? 16 : 0), so = br * 896 + ct * 128 + kh * 64;
#pragma unroll
        for (int gp = 0; gp < 2; ++gp) {
            auto s0 = __builtin_amdgcn_permlane32_swap(pk2(c0[8 * gp], c0[8 * gp + 1]), pk2(c0[8 * gp + 4], c0[8 * gp + 5]), false, false);
            auto s1 = __builtin_amdgcn_permlane32_swap(pk2(c0[8 * gp + 2], c0[8 * gp + 3]), pk2(c0[8 * gp + 6], c0[8 * gp + 7]), false, false);
            st16a<2>(rout, (u32x4){s0[0], s1[0], s0[1], s1[1]}, vo + gp * 32, so);
        }
        STAMP(12);
    }
    STAMP_FLUSH();
    asm volatile(".Lpf_end:");
}

extern "C" void kernel_launch(void* const* d_in, const int* in_sizes, int n_in, void* d_out, int out_size, void* d_ws, size_t ws_size,
                              hipStream_t stream) {
    (void)in_sizes; (void)n_in; (void)out_size; (void)ws_size;
    (void)hipFuncSetAttribute((const void*)fused_fwd, hipFuncAttributeMaxDynamicSharedMemorySize, LDS_BYTES);
    fused_fwd<<<256, 512, LDS_BYTES, stream>>>(d_in[0], d_in[1], d_in[2], d_in[3], d_in[4], d_in[5], d_in[6], d_out, d_ws);
}
```

```cpp
#include <hip/hip_runtime.h>
#include <hip/hip_bf16.h>
#include <stdint.h>
#include <type_traits>

typedef __attribute__((ext_vector_type(8))) __bf16 bf16x8;
typedef __attribute__((ext_vector_type(2))) __bf16 bf16x2;
typedef __attribute__((ext_vector_type(2))) float f32x2;
typedef __attribute__((ext_vector_type(4))) float f32x4;
typedef __attribute__((ext_vector_type(16))) float f32x16;
typedef __attribute__((ext_vector_type(4))) unsigned u32x4;
typedef __attribute__((ext_vector_type(2))) unsigned u32x2;
typedef __attribute__((ext_vector_type(4))) short s16x4;
typedef __amdgpu_buffer_rsrc_t rsrc_t;
#define LAS __attribute__((address_space(3)))
#define DI __device__ __forceinline__

constexpr unsigned WS_EPOCH = 0;
constexpr unsigned WS_F1 = 4096;
constexpr unsigned WS_F2 = WS_F1 + 1024 * 128;
constexpr unsigned WS_F3 = WS_F2 + 512 * 128;
constexpr unsigned WS_FO = WS_F3 + 256 * 128;
constexpr unsigned WS_QKV = 1u << 20;
constexpr unsigned WS_KVP = WS_QKV + (6u << 20);
constexpr unsigned WS_KVR = WS_KVP + (2u << 20);
constexpr unsigned WS_O = WS_KVR + (1u << 20);
constexpr unsigned WS_DBG = WS_O + (4u << 20);
constexpr unsigned WS_BYTES = WS_DBG + (1u << 20);

constexpr unsigned SLOT = 37888;
constexpr unsigned L_R0 = 0;
constexpr unsigned L_R1 = 57856;
constexpr unsigned L_R2 = L_R1 + 32768;
constexpr unsigned L_QX = L_R2 + 49152;
constexpr unsigned L_QB = L_R2;
constexpr unsigned L_PHI = 4 * SLOT + 64;
constexpr unsigned L_JUNK = L_PHI + 4096;
constexpr unsigned L_CNT = L_JUNK + 1024;
constexpr unsigned L_TAG = L_CNT + 256;
constexpr unsigned LDS_BYTES = L_TAG + 16;
enum { C_LAND0 = 24, C_DONE0 = 32, C_QB = 4, C_QX = 10, C_PV = 11, C_KVDONE = 12, C_WO0 = 13, C_WOFREE = 15, C_EX0 = 16, C_CMB = 20, C_ATT = 21, C_GATH = 22 };

#define MAP_SEP 0
#define PNX 3
#define PNW 3
#define FASTG 2
#define ROT 1
#define LATE1 0
#define HSLP 16
#define WOWIN 2
constexpr unsigned SPIN_CAP = 1u << 16;
constexpr float QSCALE = 0.18898223650461362f * 1.4426950408889634f;
constexpr float LOG2E = 1.4426950408889634f;
constexpr float REF_SHIFT = 100.f;

#define MFMA(a, b, c) __builtin_amdgcn_mfma_f32_32x32x16_bf16((a), (b), (c), 0, 0, 0)
#define CBAR() asm volatile("" ::: "memory")
#define WAIT_VM(n) asm volatile("s_waitcnt vmcnt(%0)" :: "i"(n) : "memory")
#define WAIT_LGKM0() asm volatile("s_waitcnt lgkmcnt(0)" ::: "memory")
#define SBAR() __builtin_amdgcn_sched_barrier(0)

#define STAMP_DECL
#define STAMP(i) do {} while (0)
#define STAMPC(i) do {} while (0)
#define STAMP_FLUSH() do {} while (0)
#define FSTAMP(i) do {} while (0)
#define FSTAMPC(i) do {} while (0)
#define PIN(x) asm volatile("" : "+v"(x))

DI unsigned pk2(float a, float b) { f32x2 v = {a, b}; return __builtin_bit_cast(unsigned, __builtin_convertvector(v, bf16x2)); }
DI u32x4 pk8(const f32x16& a, int s) {
    return (u32x4){pk2(a[8 * s], a[8 * s + 1]), pk2(a[8 * s + 2], a[8 * s + 3]), pk2(a[8 * s + 4], a[8 * s + 5]), pk2(a[8 * s + 6], a[8 * s + 7])}; }
DI bf16x8 asbf(u32x4 v) { return __builtin_bit_cast(bf16x8, v); }
DI float swap_max(float m) { auto r = __builtin_amdgcn_permlane32_swap(__float_as_uint(m), __float_as_uint(m), false, false);
    return __builtin_fmaxf(__uint_as_float(r[0]), __uint_as_float(r[1])); }
DI float swap_min(float m) { auto r = __builtin_amdgcn_permlane32_swap(__float_as_uint(m), __float_as_uint(m), false, false);
    return __builtin_fminf(__uint_as_float(r[0]), __uint_as_float(r[1])); }
DI float swap_sum(float m) { auto r = __builtin_amdgcn_permlane32_swap(__float_as_uint(m), __float_as_uint(m), false, false);
    return __uint_as_float(r[0]) + __uint_as_float(r[1]); }
DI float from_hi(float m) { auto r = __builtin_amdgcn_permlane32_swap(__float_as_uint(m), __float_as_uint(m), false, false);
    return __uint_as_float(r[1]); }
DI float max16(const f32x16& s) {
    float a = __builtin_fmaxf(__builtin_fmaxf(s[0], s[1]), s[2]), b = __builtin_fmaxf(__builtin_fmaxf(s[3], s[4]), s[5]);
    a = __builtin_fmaxf(__builtin_fmaxf(a, s[6]), s[7]); b = __builtin_fmaxf(__builtin_fmaxf(b, s[8]), s[9]);
    a = __builtin_fmaxf(__builtin_fmaxf(a, s[10]), s[11]); b = __builtin_fmaxf(__builtin_fmaxf(b, s[12]), s[13]);
    a = __builtin_fmaxf(__builtin_fmaxf(a, s[14]), s[15]); return __builtin_fmaxf(a, b); }
DI float min16(const f32x16& s) {
    float a = __builtin_fminf(__builtin_fminf(s[0], s[1]), s[2]), b = __builtin_fminf(__builtin_fminf(s[3], s[4]), s[5]);
    a = __builtin_fminf(__builtin_fminf(a, s[6]), s[7]); b = __builtin_fminf(__builtin_fminf(b, s[8]), s[9]);
    a = __builtin_fminf(__builtin_fminf(a, s[10]), s[11]); b = __builtin_fminf(__builtin_fminf(b, s[12]), s[13]);
    a = __builtin_fminf(__builtin_fminf(a, s[14]), s[15]); return __builtin_fminf(a, b); }

DI void dma(rsrc_t rs, unsigned voff, unsigned soff, unsigned lds) {
    asm volatile("s_mov_b32 m0, %0\n\ts_nop 0\n\tbuffer_load_dwordx4 %1, %2, %3 offen lds" :: "s"(lds), "v"(voff), "s"(rs), "s"(soff) : "memory"); }
DI u32x4 lds_rd128(unsigned a) { return *(LAS const u32x4*)(uintptr_t)a; }
DI f32x4 lds_rdf4(unsigned a) { return *(LAS const f32x4*)(uintptr_t)a; }
DI void lds_wr128(unsigned a, u32x4 v) { *(LAS u32x4*)(uintptr_t)a = v; }
DI void lds_wrf4(unsigned a, f32x4 v) { *(LAS f32x4*)(uintptr_t)a = v; }
DI void lds_wr64(unsigned a, u32x2 v) { *(LAS u32x2*)(uintptr_t)a = v; }
DI bf16x8 lds_tr2(unsigned a0, unsigned a1) {
    s16x4 lo = __builtin_amdgcn_ds_read_tr16_b64_v4i16((LAS s16x4*)(uintptr_t)a0), hi = __builtin_amdgcn_ds_read_tr16_b64_v4i16((LAS s16x4*)(uintptr_t)a1);
    return __builtin_bit_cast(bf16x8, __builtin_shufflevector(lo, hi, 0, 1, 2, 3, 4, 5, 6, 7)); }
DI void sig(int c, int) {
    asm volatile("s_mov_b64 exec, 1\n\tds_add_u32 %0, %1\n\ts_mov_b64 exec, -1" :: "v"(L_CNT + 4 * c), "v"(1u) : "memory"); }
template <int SLP = 1> DI void waitc(int c, unsigned n) {
    unsigned v, sv, it;
    asm volatile("s_mov_b32 %2, %5\n"
                 "1:\n\tds_read_b32 %0, %3\n\ts_waitcnt lgkmcnt(0)\n\tv_readfirstlane_b32 %1, %0\n\ts_cmp_ge_u32 %1, %4\n\ts_cbranch_scc1 2f\n\t"
                 "s_sleep %6\n\ts_sub_u32 %2, %2, 1\n\ts_cbranch_scc0 1b\n2:"
                 : "=&v"(v), "=&s"(sv), "=&s"(it) : "v"(L_CNT + 4 * c), "i"(n), "i"(SPIN_CAP), "i"(SLP) : "memory", "scc"); }
DI unsigned rd_cnt(int c) { return *(volatile LAS const unsigned*)(uintptr_t)(L_CNT + 4 * c); }
DI u32x2 rd_tag() { const u32x2 t = *(LAS const u32x2*)(uintptr_t)L_TAG;
    return (u32x2){(unsigned)__builtin_amdgcn_readfirstlane(t[0]), (unsigned)__builtin_amdgcn_readfirstlane(t[1])}; }
DI u32x4 ld16(rsrc_t rs, unsigned voff, unsigned soff) { return __builtin_amdgcn_raw_buffer_load_b128(rs, voff, soff, 16); }
template <int AUX> DI void st16a(rsrc_t rs, u32x4 v, unsigned voff, unsigned soff) {
    __builtin_amdgcn_raw_buffer_store_b128(v, rs, voff, soff, AUX); asm volatile("s_nop 1" :: "v"(v) : "memory"); }
DI void st16(rsrc_t rs, u32x4 v, unsigned voff, unsigned soff) { st16a<16>(rs, v, voff, soff); }
DI u32x2 ldflag(rsrc_t rs, unsigned voff) { return __builtin_amdgcn_raw_buffer_load_b64(rs, voff, 0, 16); }
DI bool tagok(u32x2 f, u32x2 tag) {
    return __builtin_amdgcn_ballot_w64(__builtin_bit_cast(unsigned long long, f) == __builtin_bit_cast(unsigned long long, tag)) == ~0ull; }
template <int SLP = 2> DI void poll(rsrc_t rs, unsigned voff, u32x2 tag) {
    CBAR();
#pragma clang loop unroll(disable)
    for (unsigned it = 0; it < SPIN_CAP; ++it) { u32x2 f = ldflag(rs, voff); if (tagok(f, tag)) break; __builtin_amdgcn_s_sleep(SLP); CBAR(); }
    CBAR(); }
DI void post(rsrc_t rs, unsigned off, u32x2 tag, int lane) {
    WAIT_VM(0);
    if (lane == 0) __builtin_amdgcn_raw_buffer_store_b64(tag, rs, off, 0, 16);
    CBAR(); }

#define GB(n, k) "v[176+20*" #n "+4*" #k ":179+20*" #n "+4*" #k "]"
#define GT(A, n, t, i) "v_mfma_f32_32x32x16_bf16 %[" #A "], " GB(n, (2+i)) ", " GB(n, t) ", %[" #A "]\n\t"
#define GD(A, n, t, i) "v_mfma_f32_32x32x16_bf16 %[" #A "], " GB(n, t) ", " GB(n, (2+i)) ", %[" #A "]\n\t"
#define GX(n, t, X) "ds_read_b128 " GB(n, t) ", " X " offset:4096*" #t "\n\t"
#define GF(n, i, W, O) "ds_read_b64_tr_b16 v[176+20*" #n "+8+4*" #i ":177+20*" #n "+8+4*" #i "], " W " offset:" O "\n\t" \
                       "ds_read_b64_tr_b16 v[178+20*" #n "+8+4*" #i ":179+20*" #n "+8+4*" #i "], " W " offset:" O "+336\n\t"
#define GSTEP(M0, M1, M2, c, n, X, O, P0, P0B, P2, P4, P5) \
    M0(a0, c, 0, 0) P0 GX(n, 0, X) P0B M0(a3, c, 1, 0) GF(n, 0, "%[wa]", O) M1(a1, c, 0, 1) GX(n, 1, X) P2 M1(a4, c, 1, 1) GF(n, 1, "%[wb]", O) \
    M2(a2, c, 0, 2) GF(n, 2, "%[wc]", O) P4 M2(a5, c, 1, 2) P5
#define GLOOP(M0, M1, M2) \
    GX(0, 0, "%[xa]") GF(0, 0, "%[wa]", "0") GX(0, 1, "%[xa]") GF(0, 1, "%[wb]", "0") GF(0, 2, "%[wc]", "0") \
    GX(1, 0, "v170") GF(1, 0, "%[wa]", "5376") GX(1, 1, "v170") GF(1, 1, "%[wb]", "5376") GF(1, 2, "%[wc]", "5376") \
    "s_waitcnt lgkmcnt(8)\n" \
    "1:\n\t" \
    GSTEP(M0, M1, M2, 0, 2, "v171", "10752", "", "ds_read_b128 v[166:169], %[la]\n\t", "v_xor_b32 v172, 0x60, %[xa]\n\t", "s_and_b32 %[so], %[g], 3\n\t", \
          "s_mul_i32 %[so], %[so], %[slot]\n\ts_waitcnt lgkmcnt(9)\n\t") \
    GSTEP(M0, M1, M2, 1, 3, "v172", "16128", "", "v_add_u32 %[xa], %[so], %[xa0]\n\ts_waitcnt lgkmcnt(8)\n\t", "v_min3_u32 v173, v166, v167, v168\n\tv_xor_b32 v170, 32, %[xa]\n\t", \
          "v_min_u32 v173, v173, v169\n\tv_add_u32 %[wa], %[so], %[wa0]\n\t", "v_add_u32 %[wb], %[so], %[wb0]\n\tv_readfirstlane_b32 %[rf], v173\n\tv_add_u32 %[wc], %[so], %[wc0]\n\ts_waitcnt lgkmcnt(8)\n\t") \
    GSTEP(M0, M1, M2, 2, 0, "%[xa]", "0", "s_cmp_gt_u32 %[rf], %[g]\n\ts_cbranch_scc0 3f\n2:\n\t", "", "v_xor_b32 v171, 64, %[xa]\n\t", "", \
          "s_waitcnt lgkmcnt(8)\n\tv_mov_b32 v174, %[g]\n\t") \
    GSTEP(M0, M1, M2, 3, 1, "v170", "5376", "", "s_add_u32 %[g], %[g], 1\n\t", "s_cmp_lt_u32 %[g], 8\n\t", "", \
          "s_waitcnt lgkmcnt(8)\n\tds_write_b32 %[da], v174\n\ts_cbranch_scc1 1b\n\t") \
    "s_branch 4f\n" \
    "3:\n\ts_mov_b32 %[so], %[cap]\n" \
    "5:\n\tds_read_b128 v[166:169], %[la]\n\ts_waitcnt lgkmcnt(0)\n\tv_min3_u32 v173, v166, v167, v168\n\tv_min_u32 v173, v173, v169\n\ts_nop 0\n\tv_readfirstlane_b32 %[rf], v173\n\ts_cmp_gt_u32 %[rf], %[g]\n\ts_cbranch_scc1 2b\n\t" \
    "s_sub_u32 %[so], %[so], 1\n\ts_cbranch_scc0 5b\n\ts_branch 2b\n" \
    "4:\n\ts_waitcnt lgkmcnt(0)\n\ts_nop 7\n\ts_nop 3"
template <int ND, class Start>
DI void gemm6(unsigned xa0, unsigned wa0, unsigned wb0, unsigned wc0, int w, f32x16 (&acc)[6], Start start) {
    unsigned xa = xa0, wa = wa0, wb = wb0, wc = wc0, g = 1, so, rf;
    const unsigned la = L_CNT + 4 * C_LAND0, da = L_CNT + 4 * (C_DONE0 + w);
#pragma unroll
    for (int i = 0; i < 6; ++i) { acc[i] = f32x16{}; PIN(acc[i]); }
    PIN(xa); PIN(wa); PIN(wb); PIN(wc);
    start();
#define GASM(M0, M1, M2) asm volatile("v_xor_b32 v170, 32, %[xa]\n\tv_xor_b32 v171, 64, %[xa]\n\t" GLOOP(M0, M1, M2) \
        : [a0] "+v"(acc[0]), [a1] "+v"(acc[1]), [a2] "+v"(acc[2]), [a3] "+v"(acc[3]), [a4] "+v"(acc[4]), [a5] "+v"(acc[5]), \
          [xa] "+v"(xa), [wa] "+v"(wa), [wb] "+v"(wb), [wc] "+v"(wc), [g] "+s"(g), [so] "=&s"(so), [rf] "=&s"(rf) \
        : [xa0] "v"(xa0), [wa0] "v"(wa0), [wb0] "v"(wb0), [wc0] "v"(wc0), [la] "v"(la), [da] "v"(da), [slot] "i"(SLOT), [cap] "i"(SPIN_CAP) \
        : "memory", "scc", "v166", "v167", "v168", "v169", "v170", "v171", "v172", "v173", "v174", "v175", "v176", "v177", "v178", "v179", "v180", "v181", "v182", "v183", "v184", "v185", "v186", "v187", "v188", "v189", "v190", "v191", "v192", "v193", "v194", "v195", "v196", "v197", "v198", "v199", "v200", "v201", "v202", "v203", "v204", "v205", "v206", "v207", "v208", "v209", "v210", "v211", "v212", "v213", "v214", "v215", "v216", "v217", "v218", "v219", "v220", "v221", "v222", "v223", "v224", "v225", "v226", "v227", "v228", "v229", "v230", "v231", "v232", "v233", "v234", "v235", "v236", "v237", "v238", "v239", "v240", "v241", "v242", "v243", "v244", "v245", "v246", "v247", "v248", "v249", "v250", "v251", "v252", "v253", "v254", "v255")
    if constexpr (ND == 0) GASM(GT, GT, GT); else if constexpr (ND == 1) GASM(GT, GT, GD); else GASM(GD, GD, GD);
}
DI unsigned min4(u32x4 d) { return __builtin_amdgcn_readfirstlane(min(min(d[0], d[1]), min(d[2], d[3]))); }
DI u32x4 rd4(int c) { return *(volatile LAS const u32x4*)(uintptr_t)(L_CNT + 4 * c); }
DI void wait4(int c, unsigned n) {
    CBAR();
#pragma clang loop unroll(disable)
    for (unsigned it = 0; it < SPIN_CAP; ++it) if (min4(rd4(c)) >= n) break;
    CBAR(); }
template <int P> DI void dma_x4(rsrc_t rs, unsigned voff, unsigned soff, unsigned lds) {
    asm volatile("s_mov_b32 m0, %1\n\ts_nop %4\n\tbuffer_load_dwordx4 %2, %3, %0 offen lds\n\t"
                 "s_add_u32 m0, m0, 0x1000\n\ts_add_u32 %0, %0, 28672\n\ts_nop %4\n\tbuffer_load_dwordx4 %2, %3, %0 offen lds\n\t"
                 "s_add_u32 m0, m0, 0x1000\n\ts_add_u32 %0, %0, 28672\n\ts_nop %4\n\tbuffer_load_dwordx4 %2, %3, %0 offen lds\n\t"
                 "s_add_u32 m0, m0, 0x1000\n\ts_add_u32 %0, %0, 28672\n\ts_nop %4\n\tbuffer_load_dwordx4 %2, %3, %0 offen lds"
                 : "+s"(soff) : "s"(lds), "v"(voff), "s"(rs), "i"(P) : "memory", "scc"); }
template <int P> DI void dma_w5(rsrc_t rs, const unsigned (&v)[6], unsigned soff, unsigned lds) {
    asm volatile("s_mov_b32 m0, %0\n\ts_nop %8\n\tbuffer_load_dwordx4 %3, %1, %2 offen lds\n\t"
                 "s_add_u32 m0, m0, 0x1000\n\ts_nop %8\n\tbuffer_load_dwordx4 %4, %1, %2 offen lds\n\t"
                 "s_add_u32 m0, m0, 0x1000\n\ts_nop %8\n\tbuffer_load_dwordx4 %5, %1, %2 offen lds\n\t"
                 "s_add_u32 m0, m0, 0x1000\n\ts_nop %8\n\tbuffer_load_dwordx4 %6, %1, %2 offen lds\n\t"
                 "s_add_u32 m0, m0, 0x1000\n\ts_nop %8\n\tbuffer_load_dwordx4 %7, %1, %2 offen lds"
                 :: "s"(lds), "s"(rs), "s"(soff), "v"(v[0]), "v"(v[1]), "v"(v[2]), "v"(v[3]), "v"(v[4]), "i"(P) : "memory", "scc"); }
DI unsigned tr_addr128(unsigned base, int cb, int rowgrp, int lane) {
    const int q = (lane & 15) >> 2, byte = cb + 32 * ((lane >> 4) & 1) + 8 * (lane & 3);
    return base + (rowgrp + q) * 128 + ((((byte >> 4) ^ ((q >> 1) << 2)) & 7) << 4) + (byte & 8);
}
DI void feat(const f32x16& p, f32x16& e, f32x16& g) {
    const float mx = swap_max(max16(p)) * LOG2E, mn = swap_min(min16(p)) * LOG2E;
#pragma unroll
    for (int i = 0; i < 16; i += 2) {
        const f32x2 pp = {p[i], p[i + 1]};
        const f32x2 a = __builtin_elementwise_fma(pp, (f32x2){LOG2E, LOG2E}, (f32x2){-mx, -mx}), c = __builtin_elementwise_fma(pp, (f32x2){-LOG2E, -LOG2E}, (f32x2){mn, mn});
        e[i] = __builtin_amdgcn_exp2f(a[0]); e[i + 1] = __builtin_amdgcn_exp2f(a[1]); g[i] = __builtin_amdgcn_exp2f(c[0]); g[i + 1] = __builtin_amdgcn_exp2f(c[1]);
    }
}

typedef __attribute__((ext_vector_type(16))) unsigned u32x16;
DI rsrc_t mk_rsrc(unsigned lo, unsigned hi, unsigned bytes) {
    return __builtin_amdgcn_make_buffer_rsrc((void*)(((unsigned long long)hi << 32) | lo), 0, bytes, 0x00020000); }
DI void read_wo(bf16x8 (&wf)[16][2], int kh, int rot, int lane) {
    const unsigned ta0 = tr_addr128(L_R0, 0, kh * 224 + 4 * (lane >> 5), lane), ta1 = tr_addr128(L_R0, 64, kh * 224 + 4 * (lane >> 5), lane);
#pragma unroll
    for (int k = 0; k < 8; ++k) {
        const unsigned ro = ((k + rot) & 7) * 28 * 128, b0 = ta0 + ro, b1 = ta1 + ro;
        wf[2 * k][0] = lds_tr2(b0, b0 + 1024); wf[2 * k][1] = lds_tr2(b1, b1 + 1024);
        wf[2 * k + 1][0] = lds_tr2(b0 + 2048, b0 + 3072); wf[2 * k + 1][1] = lds_tr2(b1 + 2048, b1 + 3072);
    }
}
DI void ones_row(u32x4& a, u32x4& b, u32x2 c) {
    u32x2 a0 = {a[0], a[1]}, a1 = {a[2], a[3]}, b0 = {b[0], b[1]}, b1 = {b[2], b[3]};
    asm volatile("s_mov_b64 exec, %4\n\tv_mov_b64 %0, %5\n\tv_mov_b64 %1, %5\n\tv_mov_b64 %2, %5\n\tv_mov_b64 %3, %5\n\ts_mov_b64 exec, -1"
                 : "+v"(a0), "+v"(a1), "+v"(b0), "+v"(b1) : "s"(0xF0000000F0000000ull), "v"(c));
    a = (u32x4){a0[0], a0[1], a1[0], a1[1]}; b = (u32x4){b0[0], b0[1], b1[0], b1[1]}; }
template <int ND>
DI void publish_a(f32x16 (&acc)[6], rsrc_t rws, const unsigned (&so)[3], int nq, int lane, bool hi, u32x2 oc) {
#pragma unroll
    for (int i = 0; i < 3; ++i) {
        if (i < 3 - ND && i < nq) {
            asm volatile("" ::: "memory");
#pragma unroll
            for (int k = 0; k < 16; ++k) { acc[i][k] *= QSCALE; acc[3 + i][k] *= QSCALE; }
            PIN(acc[i]); PIN(acc[3 + i]);
        }
        const unsigned pad = i < nq ? 0u : 0x00003F80u;
#pragma unroll
        for (int t = 0; t < 2; ++t) {
            u32x4 v0 = pk8(acc[3 * t + i], 0), v1 = pk8(acc[3 * t + i], 1);
            if (i >= 3 - ND) ones_row(v0, v1, oc); else { v1[2] = hi ? pad : v1[2]; v1[3] = hi ? 0u : v1[3]; }
            st16(rws, v0, lane * 16, so[i] + t * 6144); st16(rws, v1, lane * 16 + 1024, so[i] + t * 6144);
            SBAR();
        }
    }
}
DI void phi_w(bf16x8& f0, u32x4& f1, int hh, int lane, bool hi) {
    const unsigned pb = L_PHI + hh * 1568 + (4 * (lane >> 5) + ((lane & 15) >> 2)) * 56 + ((lane & 19) == 19 ? 0 : 32 * ((lane >> 4) & 1) + 8 * (lane & 3));
    f0 = lds_tr2(pb, pb + 8 * 56);
    f1 = __builtin_bit_cast(u32x4, lds_tr2(pb + 16 * 56, pb + 24 * 56));
    if (hi) { f1[2] = 0; f1[3] = 0; }
}
DI f32x16 phi_pre(u32x4 k0, u32x4 k1, bf16x8 f0, u32x4 f1) {
    f32x16 p = MFMA(f0, asbf(k0), f32x16{});
    return MFMA(asbf(f1), asbf(k1), p);
}

__global__ __launch_bounds__(512, 2) void fused_fwd(const void*, const void*, const void*, const void*, const void*, const void*, const void*, void*, void*) {
    STAMP_DECL; STAMP(0); STAMPC(30);
    u32x16 ka; u32x2 kb;
    asm volatile("s_load_dwordx16 %0, %2, 0x0\n\ts_load_dwordx2 %1, %2, 0x40" : "=&s"(ka), "=&s"(kb) : "s"(__builtin_amdgcn_kernarg_segment_ptr()));
    const int tid = threadIdx.x, lane = tid & 63, wid = __builtin_amdgcn_readfirstlane(tid >> 6);
    const int b = blockIdx.x, xcd = b & 7, r = b >> 3, w = wid & 3, role = wid >> 2;
    const int isb = r & 1, ml = (r >> 1) & 3, nl = r >> 3;
    const int mt = (xcd & 3) * 4 + ml, nt = (xcd >> 2) * 4 + nl;
    const bool hi = lane >= 32;
    int g0 = ROT == 0 ? 0 : ROT == 1 ? ((ml + nl) & 1) * 4 : ROT == 2 ? 2 * ml + nl : ROT == 3 ? (isb ? ((ml + nl) & 1) * 4 : 0) : ROT == 4 ? (isb ? 2 * ml + nl : ((ml + nl) & 1) * 4) : (isb ? 2 * ml + nl : 0); g0 -= (g0 >= 7) ? 7 : 0;
    const int head = 2 * xcd + (r >> 4), qt = r & 15;
    const int ct = r >> 2, rt = xcd * 4 + (r & 3);
    u32x2 tag;
    bf16x8 wf[16][2];
    u32x2 fl0 = {};
    unsigned pfd = 0;

    unsigned xvo, wvo[6];
    if (role) {
        __builtin_amdgcn_s_setprio(2);
        xvo = (w * 8 + (lane >> 3)) * 896 + ((((lane & 7) ^ (lane >> 4) ^ ((w & 1) << 2)) & 7) << 4);
        PIN(xvo);
#pragma unroll
        for (int i = 0; i < 6; ++i) {
            const int c = 64 * (w + 4 * i) + lane, rho = (c * 3121) >> 16, ch = c - 21 * rho, win = (ch * 37) >> 8;
            const int krow = (rho & ~15) | ((rho & 3) << 2) | ((rho >> 2) & 3);
            wvo[i] = krow * 2688 + ch * 16 + (isb ? win * (896 - 112) : 0);
            PIN(wvo[i]);
        }
    }
    if (wid == 0) {
        unsigned long long pc; unsigned len;
        asm volatile("s_getpc_b64 %0\n.Lpf_begin%=:\n\ts_mov_b32 %1, .Lpf_end-.Lpf_begin%=" : "=s"(pc), "=s"(len));
        const rsrc_t rc = __builtin_amdgcn_make_buffer_rsrc((void*)pc, 0, len, 0x00020000);
        asm volatile("s_nop 4\n\tbuffer_load_dword %0, %1, %2, 0 offen" : "+v"(pfd) : "v"(lane < 12 ? (r * 12 + lane) * 128 : 0x7FFFFFF0), "s"(rc) : "memory");
        *(LAS unsigned*)(uintptr_t)(L_CNT + 4 * lane) = 0u;
    }
    asm volatile("s_waitcnt lgkmcnt(0)" : "+s"(ka), "+s"(kb) :: "memory");
    const rsrc_t rws = mk_rsrc(kb[0], kb[1], WS_BYTES);
    u32x4 pe[2][4];
    if (role) {
        const rsrc_t rx = mk_rsrc(isb ? ka[6] : ka[0], isb ? ka[7] : ka[1], 2048 * 896), rw = mk_rsrc(isb ? ka[8] : ka[2], isb ? ka[9] : ka[3], 448 * 2688);
        const unsigned xs0 = mt * 114688, ws0 = (isb ? 112 : 336) * nt;
        auto feed = [&](int g, auto PX, auto PW) {
            int ga = g + g0; ga -= (ga >= 7) ? 7 : 0;
            const unsigned ws = ws0 + ga * 64 * 2688, l = (g & 3) * SLOT + w * 1024;
            dma_x4<decltype(PX)::value>(rx, xvo, xs0 + ga * 128, l);
            dma_w5<decltype(PW)::value>(rw, wvo, ws, l + 16384);
            if (w == 0) dma(rw, wvo[5], ws, l + 16384 + 5 * 4096); };
        const unsigned lw = L_CNT + 4 * (C_LAND0 + w);
        constexpr std::integral_constant<int, 0> fast{};
        FSTAMP(18);
        dma(mk_rsrc(ka[10], ka[11], 16 * 1568), lane * 16, nt * 3136 + w * 1024, L_PHI + w * 1024);
        feed(0, fast, fast);
        if (LATE1 == 0) feed(1, fast, fast);
        asm volatile("s_barrier" ::: "memory");
        if (LATE1 == 1) { WAIT_VM(4); feed(1, fast, fast); }
        if (LATE1 == 2) { WAIT_VM(0); *(volatile LAS unsigned*)(uintptr_t)lw = 1; feed(1, fast, fast); }
        if (w == 0) WAIT_VM(10); else WAIT_VM(9);
        *(volatile LAS unsigned*)(uintptr_t)lw = 1;
        STAMP(1);
#pragma unroll 1
        for (int g = 1; g < 6; ++g) {
            if (g >= 3) wait4(C_DONE0, g - 2);
            if (g < FASTG) feed(g + 1, fast, fast); else feed(g + 1, std::integral_constant<int, PNX>{}, std::integral_constant<int, PNW>{});
            if (w == 0) WAIT_VM(10); else WAIT_VM(9);
            *(volatile LAS unsigned*)(uintptr_t)lw = g + 1;
        }
        {
            const rsrc_t rt_ = mk_rsrc(w < 2 ? ka[4] : ka[12], w < 2 ? ka[5] : ka[13], 448 * 896);
            asm volatile("s_mov_b32 m0, %0\n\ts_nop 3\n\tbuffer_load_dword %1, %2, 0 offen lds"
                         :: "s"(L_JUNK), "v"(lane < 49 ? (r * 98 + (w & 1) * 49 + lane) * 128 : 0x7FFFFF00), "s"(rt_) : "memory");
        }
        WAIT_VM(1);
        *(volatile LAS unsigned*)(uintptr_t)lw = 8;
        STAMP(2);
        tag = rd_tag();
        if (isb) {
            bf16x8 ph0; u32x4 ph1;
            wait4(C_LAND0, 1);
            phi_w(ph0, ph1, w >> 1, lane, hi);
            const int tk = lane & 31, pi = (((tk >> 1) & 1) << 3) | (tk & 1) | (((tk >> 2) & 3) << 1);
            const unsigned ib = L_R1 + (w >> 1) * 16384 + ((w & 1) * 64 + tk) * 128 + ((pi ^ (lane >> 5)) << 3);
            const float lo = hi ? 0.f : 1.f;
            waitc<1>(C_QB + w, 1);
            f32x16 pp[2];
#pragma unroll
            for (int t = 0; t < 2; ++t) { const unsigned qa = L_QB + ((w * 2 + t) * 4 + 2) * 1024 + lane * 16; pp[t] = phi_pre(lds_rd128(qa), lds_rd128(qa + 1024), ph0, ph1); }
#pragma unroll
            for (int t = 0; t < 2; ++t) {
                f32x16 e, g;
                feat(pp[t], e, g);
                f32x2 s2 = {e[0], e[1]}, g2 = {g[0], g[1]};
#pragma unroll
                for (int i = 2; i < 12; i += 2) { s2 += (f32x2){e[i], e[i + 1]}; g2 += (f32x2){g[i], g[i + 1]}; }
                s2 = __builtin_elementwise_fma((f32x2){e[12], e[13]} + (f32x2){e[14], e[15]}, (f32x2){lo, lo}, s2);
                g2 = __builtin_elementwise_fma((f32x2){g[12], g[13]} + (f32x2){g[14], g[15]}, (f32x2){lo, lo}, g2);
                const float se = __builtin_amdgcn_rcpf(swap_sum(s2[0] + s2[1])), sg_ = __builtin_amdgcn_rcpf(swap_sum(g2[0] + g2[1]));
#pragma unroll
                for (int u = 0; u < 8; ++u) {
                    const int k = 4 * (u & 3);
                    const f32x16& z = (u >> 2) ? g : e; const float sc = (u >> 2) ? sg_ : se;
                    lds_wr64((ib ^ ((8 * (u >> 2) + 2 * (u & 3)) << 3)) + t * 4096, (u32x2){pk2(z[k] * sc, z[k + 1] * sc), pk2(z[k + 2] * sc, z[k + 3] * sc)});
                }
            }
            WAIT_LGKM0(); sig(C_PV, lane);
#pragma unroll
            for (int t = 0; t < 2; ++t) {
                const unsigned qa = L_QB + ((w * 2 + t) * 4) * 1024 + lane * 16;
                f32x16 p = phi_pre(lds_rd128(qa), lds_rd128(qa + 1024), ph0, ph1), e, g;
                feat(p, e, g);
                pe[t][0] = pk8(e, 0); pe[t][1] = pk8(e, 1); pe[t][2] = pk8(g, 0); pe[t][3] = pk8(g, 1);
            }
        }
        STAMP(3);
    } else {
        f32x16 acc[6];
        const int tp = w & 1, sg = w >> 1;
        const int rr = tp * 64 + (lane & 31);
        const unsigned xa0 = rr * 128 + ((((lane >> 5) ^ (rr >> 1)) & 7) << 4);
        const unsigned lb = 16384 + (4 * ((lane & 15) >> 2) + 2 * (lane >> 5)) * 336 + 32 * ((lane >> 4) & 1) + 8 * (lane & 3);
        const unsigned wst = isb ? 112 : 56, wa0 = lb + 56 * (isb ? sg : 3 * sg), wb0 = wa0 + wst, wc0 = wb0 + wst;
        const int G = 2 * nt + sg;
        if (w == 0) {
            const u32x2 ep = ldflag(rws, WS_EPOCH);
            const unsigned long long e = (((unsigned long long)ep[1] << 32) | ep[0]) + 1;
            lds_wr64(L_TAG, (u32x2){(unsigned)e, (unsigned)(e >> 32)});
            WAIT_VM(0);
            asm volatile("" :: "v"(pfd));
        }
        unsigned so[3];
#pragma unroll
        for (int i = 0; i < 3; ++i) { const int sl = 3 * G + i; so[i] = WS_QKV + (((sl & 15) * 64 + mt * 4 + 2 * tp) * 6 + (sl >> 4) * 2) * 1024; asm volatile("" : "+s"(so[i])); }
        const int nq = 16 - 3 * G;
        u32x2 oc; oc[0] = oc[1] = (lane & 31) == 28 ? 0x3F803F80u : 0u; PIN(oc);
        auto start = [&]() { asm volatile("s_waitcnt lgkmcnt(0)\n\ts_barrier" ::: "memory"); tag = rd_tag(); asm volatile("" : "+s"(tag)); wait4(C_LAND0, 1); STAMP(1); STAMPC(23); };
        if (isb || G == 10) { gemm6<1>(xa0, wa0, wb0, wc0, w, acc, start); STAMPC(24); STAMP(2); if (!isb) publish_a<1>(acc, rws, so, nq, lane, hi, oc); }
        else if (G < 10) { gemm6<0>(xa0, wa0, wb0, wc0, w, acc, start); STAMPC(24); STAMP(2); publish_a<0>(acc, rws, so, nq, lane, hi, oc); }
        else { gemm6<3>(xa0, wa0, wb0, wc0, w, acc, start); STAMPC(24); STAMP(2); publish_a<3>(acc, rws, so, nq, lane, hi, oc); }
        if (!isb) {
            FSTAMP(15);
            post(rws, WS_F1 + (G * 32 + mt * 2 + tp) * 128, tag, lane);
            wait4(C_DONE0, 7);
        } else {
            u32x4 fr[2][3][2];
#pragma unroll
            for (int t = 0; t < 2; ++t) {
#pragma unroll
                for (int i = 0; i < 3; ++i) { fr[t][i][0] = pk8(acc[3 * t + i], 0); fr[t][i][1] = pk8(acc[3 * t + i], 1); }
                ones_row(fr[t][2][0], fr[t][2][1], oc);
            }
            wait4(C_DONE0, 7);
#pragma unroll
            for (int t = 0; t < 2; ++t)
#pragma unroll
                for (int s = 0; s < 2; ++s) {
                    lds_wr128(L_QB + (((w * 2 + t) * 2 + 1) * 2 + s) * 1024 + lane * 16, fr[t][1][s]);
                    lds_wr128(L_QB + (((w * 2 + t) * 2) * 2 + s) * 1024 + lane * 16, fr[t][0][s]);
                    lds_wr128(L_R0 + ((sg * 4 + 2 * tp + t) * 2 + s) * 1024 + lane * 16, fr[t][2][s]);
                }
            WAIT_LGKM0(); sig(C_QB + w, lane);
        }
        STAMP(3);
    }
    FSTAMP(4);

    if (role) {
        const int wc = (lane & 7) ^ (((lane >> 4) & 1) << 2);
        const unsigned wovo = (lane >> 3) * 896 + wc * 16;
        if (isb) {
            waitc<0>(C_PV, 4);
            const int hh = w >> 1, ft = w & 1, q = (lane & 15) >> 2, P = 8 * ft + 4 * ((lane >> 4) & 1) + (lane & 3);
            bf16x8 fa[8], fb[8];
#pragma unroll
            for (int st = 0; st < 8; ++st) {
                const int t0 = 16 * (st & 1) + 4 * (lane >> 5) + q, t1 = t0 + 8;
                const int p0 = (((t0 >> 1) & 1) << 3) | (t0 & 1) | (((t0 >> 2) & 3) << 1), p1 = (((t1 >> 1) & 1) << 3) | (t1 & 1) | (((t1 >> 2) & 3) << 1);
                const unsigned ib = L_R1 + hh * 16384 + (st >> 1) * 4096;
                fa[st] = lds_tr2(ib + t0 * 128 + ((P ^ p0) << 3), ib + t1 * 128 + ((P ^ p1) << 3));
                fb[st] = asbf(lds_rd128(L_R0 + ((hh * 4 + (st >> 1)) * 2 + (st & 1)) * 1024 + lane * 16));
            }
            SBAR();
            f32x16 kv = {};
#pragma unroll
            for (int st = 0; st < 8; ++st) kv = MFMA(fa[st], fb[st], kv);
            WAIT_LGKM0(); sig(C_KVDONE, lane);
            const int hk = 2 * nt + hh;
#pragma unroll
            for (int u = 0; u < 4; ++u)
                st16(rws, __builtin_bit_cast(u32x4, (f32x4){kv[4 * u], kv[4 * u + 1], kv[4 * u + 2], kv[4 * u + 3]}), lane * 16 + u * 1024, WS_KVP + ((hk * 16 + mt) * 8 + ft * 4) * 1024);
            FSTAMP(19);
            post(rws, WS_F2 + ((hk * 16 + mt) * 2 + ft) * 128, tag, lane);
            waitc<0>(C_KVDONE, 4);
        }
        STAMP(5);
        if (r < 28) {
            waitc<HSLP>(C_QX, 4);
            if (w == 0) *(LAS u32x2*)(uintptr_t)(L_R0 + 57344 + 8 * lane) = (u32x2){0u, 0u};
            const rsrc_t rwob = mk_rsrc(ka[12], ka[13], 448 * 896);
#pragma unroll
            for (int i = 0; i < 14; ++i) { WAIT_VM(WOWIN); dma(rwob, wovo, ct * 128 + (w + 4 * i) * 8 * 896, L_R0 + (w + 4 * i) * 1024); }
            WAIT_VM(0); WAIT_LGKM0(); sig(C_WO0 + 1, lane); waitc<HSLP>(C_WO0 + 1, 4);
            read_wo(wf, w >> 1, ct, lane);
            WAIT_LGKM0(); sig(C_WOFREE, lane);
        }
        if (w == 3) {
            const int hr = head, i = lane & 15, pg = lane >> 4;
            poll<HSLP>(rws, WS_F2 + ((hr * 16 + i) * 2 + (qt >> 3)) * 128, tag);
            u32x4 pl[8];
#pragma unroll
            for (int p = 0; p < 4; ++p) {
                const unsigned vo = (pg * 4 + p) * 8192 + (16 * (qt & 3) + i) * 16, so = WS_KVP + hr * 131072 + (qt >> 2) * 2048;
                pl[2 * p] = ld16(rws, vo, so); pl[2 * p + 1] = ld16(rws, vo + 1024, so);
            }
            SBAR();
            f32x4 s0 = {}, s1 = {};
#pragma unroll
            for (int p = 0; p < 4; ++p) { s0 += __builtin_bit_cast(f32x4, pl[2 * p]); s1 += __builtin_bit_cast(f32x4, pl[2 * p + 1]); }
            float v[8] = {s0[0], s0[1], s0[2], s0[3], s1[0], s1[1], s1[2], s1[3]};
#pragma unroll
            for (int k = 0; k < 8; ++k) {
                auto t = __builtin_amdgcn_permlane16_swap(__float_as_uint(v[k]), __float_as_uint(v[k]), false, false);
                v[k] = swap_sum(__uint_as_float(t[0]) + __uint_as_float(t[1]));
            }
            u32x4 o = {pk2(v[0], v[1]), pk2(v[2], v[3]), pk2(v[4], v[5]), pk2(v[6], v[7])};
            const int cl = 16 * (qt & 3) + i;
            if ((cl & 31) == 29) o = (u32x4){0x3F803F80u, 0x3F803F80u, 0x3F803F80u, 0x3F803F80u};
            if (((qt >> 2) & 1) && cl >= 32) { o[2] = 0u; o[3] = 0u; }
            st16(rws, o, lane < 16 ? i * 16 : 0x7FFFFF00u, WS_KVR + (hr * 256 + qt * 16) * 16);
            post(rws, WS_F3 + (hr * 16 + qt) * 128, tag, lane);
        }
        STAMP(6);
        if (r < 28) {
            const rsrc_t rwoa = mk_rsrc(ka[4], ka[5], 448 * 896);
            waitc<HSLP>(C_WOFREE, 4);
#pragma unroll
            for (int i = 0; i < 14; ++i) { WAIT_VM(WOWIN); dma(rwoa, wovo, ct * 128 + (w + 4 * i) * 8 * 896, L_R0 + (w + 4 * i) * 1024); }
            WAIT_VM(0); sig(C_WO0, lane);
        }
        if (isb) {
            const int hb_ = 2 * nt + (w >> 1), tb = mt * 4 + 2 * (w & 1);
            poll<HSLP>(rws, WS_F3 + (hb_ * 16 + (lane & 15)) * 128, tag);
            u32x4 kf[4];
#pragma unroll
            for (int f = 0; f < 4; ++f) kf[f] = ld16(rws, lane * 16 + f * 1024, WS_KVR + hb_ * 4096);
            SBAR();
#pragma unroll
            for (int t = 0; t < 2; ++t) {
                f32x16 np = MFMA(asbf(kf[0]), asbf(pe[t][0]), f32x16{}); np = MFMA(asbf(kf[1]), asbf(pe[t][1]), np);
                f32x16 nm = MFMA(asbf(kf[2]), asbf(pe[t][2]), f32x16{}); nm = MFMA(asbf(kf[3]), asbf(pe[t][3]), nm);
                const float rp = __builtin_amdgcn_rcpf(np[13]), rm = __builtin_amdgcn_rcpf(nm[13]);
                const float rd = __builtin_amdgcn_rcpf(__builtin_fmaf(rp, np[12], rm * nm[12]) + 1e-6f);
                const float cp = from_hi(rp * rd), cm = from_hi(rm * rd);
                f32x16 o;
#pragma unroll
                for (int i = 0; i < 16; ++i) o[i] = __builtin_fmaf(cp, np[i], cm * nm[i]);
                u32x4 o0 = pk8(o, 0), o1 = pk8(o, 1);
                if (hi) { o1[2] = 0; o1[3] = 0; }
                const unsigned so = WS_O + (2u << 20) + (((tb + t) * 16 + hb_) * 2) * 1024;
                st16(rws, o0, lane * 16, so); st16(rws, o1, lane * 16 + 1024, so);
            }
            WAIT_VM(0);
            if (lane < 2) __builtin_amdgcn_raw_buffer_store_b64(tag, rws, WS_FO + (1024 + (tb + lane) * 16 + hb_) * 128, 0, 16);
            CBAR();
        }
        STAMP(7);
        if (b == 255 && w == 0) {
#pragma unroll 1
            for (int i = 0; i < 4; ++i) poll<HSLP>(rws, WS_F3 + (i * 64 + lane) * 128, tag);
            if (lane == 0) __builtin_amdgcn_raw_buffer_store_b64(tag, rws, WS_EPOCH, 0, 16);
        }
    } else {
        const int tq = qt * 4 + w;
        const int gq = (head * 43) >> 7, gk = ((16 + head) * 43) >> 7, gv = ((32 + head) * 43) >> 7;
        const int rk0 = qt >> 2;
        const unsigned fall = WS_F1 + (((lane & 8) ? gv : gk) * 32 + 8 * w + (lane & 7)) * 128;
        __builtin_amdgcn_s_setprio(3);
        poll<1>(rws, WS_F1 + (lane >= 4 ? gq * 32 + (tq >> 1) : ((lane & 2) ? gv : gk) * 32 + 8 * w + 2 * rk0 + (lane & 1)) * 128, tag);
        STAMP(5);
        const unsigned hb = WS_QKV + head * 64 * 6144, kvb = hb + 2048;
        const unsigned kb0 = kvb + (16 * w + (qt & 3)) * 6144, kb1 = kvb + (16 * w + ((qt + 1) & 3)) * 6144,
                       kb2 = kvb + (16 * w + ((qt + 2) & 3)) * 6144, kb3 = kvb + (16 * w + ((qt + 3) & 3)) * 6144;
        unsigned vc = lane * 16 + rk0 * 24576;
        u32x4 kA[4], kB[4], kC[4], kD[4];
#define LD(d, f, v, sb) d[f] = ld16(rws, (v) + (f) * 1024, sb)
        u32x4 q0 = ld16(rws, lane * 16, hb + tq * 6144), q1 = ld16(rws, lane * 16 + 1024, hb + tq * 6144);
        LD(kA, 0, vc, kb0); LD(kA, 1, vc, kb0);
        SBAR();
        f32x16 O[4] = {};
        u32x4 pA0 = {}, pA1 = {}, pB0, pB1;
        kD[2] = (u32x4){}; kD[3] = (u32x4){};
        PIN(O[0]); PIN(O[1]); PIN(O[2]); PIN(O[3]); PIN(pA0); PIN(pA1); PIN(kD[2]); PIN(kD[3]);
        SBAR();
        bf16x8 Q[4][2];
        {
            f32x16 s = MFMA(asbf(kA[0]), asbf(q0), f32x16{}); s = MFMA(asbf(kA[1]), asbf(q1), s);
            SBAR(); FSTAMP(13);
            LD(kA, 2, vc, kb0); LD(kA, 3, vc, kb0); LD(kB, 0, vc, kb1); LD(kB, 1, vc, kb1); LD(kB, 2, vc, kb1); LD(kB, 3, vc, kb1);
            LD(kC, 0, vc, kb2); LD(kC, 1, vc, kb2); LD(kC, 2, vc, kb2); LD(kC, 3, vc, kb2);
            SBAR();
            lds_wr128(L_QX + (w * 2) * 1024 + lane * 16, q0);
            const float m = swap_max(max16(s)) + REF_SHIFT;
            u32x4 q1m = q1;
            if (hi) q1m[2] = pk2(-m, 0.f);
            lds_wr128(L_QX + (w * 2 + 1) * 1024 + lane * 16, q1m);
            WAIT_LGKM0(); sig(C_QX, lane); FSTAMP(14); waitc<0>(C_QX, 4);
#pragma unroll
            for (int a = 0; a < 4; ++a) { Q[a][0] = asbf(lds_rd128(L_QX + (a * 2) * 1024 + lane * 16)); Q[a][1] = asbf(lds_rd128(L_QX + (a * 2 + 1) * 1024 + lane * 16)); }
        }
        const u32x2 fl = ldflag(rws, fall);
        STAMP(6); STAMPC(20);
        __builtin_amdgcn_s_setprio(0);
        f32x16 sA, sB;
        sA = MFMA(asbf(kA[0]), Q[0][0], f32x16{}); sA = MFMA(asbf(kA[1]), Q[0][1], sA);
        PIN(Q[1][0]); PIN(Q[1][1]); PIN(Q[2][0]); PIN(Q[2][1]); PIN(Q[3][0]); PIN(Q[3][1]);
#define EX4(S, i) S[i] = __builtin_amdgcn_exp2f(S[i]); S[i + 1] = __builtin_amdgcn_exp2f(S[i + 1]); S[i + 2] = __builtin_amdgcn_exp2f(S[i + 2]); S[i + 3] = __builtin_amdgcn_exp2f(S[i + 3])
#define STAGE(SC, SN, PC0, PC1, PP0, PP1, KN, an, VP, ap, F0, F1, F2, F3) do { \
        SN = MFMA(asbf(KN[0]), Q[an][0], f32x16{}); SBAR(); F0; SBAR(); EX4(SC, 0); PIN(SC); SBAR(); \
        SN = MFMA(asbf(KN[1]), Q[an][1], SN); SBAR(); F1; SBAR(); EX4(SC, 4); PC0[0] = pk2(SC[0], SC[1]); PC0[1] = pk2(SC[2], SC[3]); PIN(SC); PIN(PC0); SBAR(); \
        O[ap] = MFMA(asbf(VP[2]), asbf(PP0), O[ap]); SBAR(); F2; SBAR(); EX4(SC, 8); PC0[2] = pk2(SC[4], SC[5]); PC0[3] = pk2(SC[6], SC[7]); PIN(SC); PIN(PC0); SBAR(); \
        O[ap] = MFMA(asbf(VP[3]), asbf(PP1), O[ap]); SBAR(); F3; SBAR(); EX4(SC, 12); PC1[0] = pk2(SC[8], SC[9]); PC1[1] = pk2(SC[10], SC[11]); \
        PC1[2] = pk2(SC[12], SC[13]); PC1[3] = pk2(SC[14], SC[15]); PIN(PC1); SBAR(); } while (0)
#define NOP (void)0
#define ARRIVE(K) asm volatile("" : "+v"(K[0]), "+v"(K[1]), "+v"(K[2]), "+v"(K[3]))
#define TILE(KP, KC, KN, V, SB, G3) do { \
        STAGE(sA, sB, pB0, pB1, pA0, pA1, KC, 1, KP, 3, NOP, NOP, NOP, NOP); \
        STAGE(sB, sA, pA0, pA1, pB0, pB1, KC, 2, KC, 0, LD(KP, 0, V, SB), LD(KP, 1, V, SB), LD(KP, 2, V, SB), LD(KP, 3, V, SB)); \
        STAGE(sA, sB, pB0, pB1, pA0, pA1, KC, 3, KC, 1, NOP, NOP, NOP, ARRIVE(KN)); \
        STAGE(sB, sA, pA0, pA1, pB0, pB1, KN, 0, KC, 2, G3, NOP, NOP, NOP); } while (0)
#pragma unroll 1
        for (int it = 0; it < 4; ++it) {
            const unsigned vn = it < 3 ? lane * 16 + ((rk0 + it + 1) & 3) * 24576 : 0x7FFF0000u;
            TILE(kD, kA, kB, vc, kb3, NOP);
            if (it == 0) { CBAR(); if (!tagok(fl, tag)) poll<1>(rws, fall, tag); CBAR(); }
            TILE(kA, kB, kC, vn, kb0, NOP);
            TILE(kB, kC, kD, vn, kb1, NOP);
            TILE(kC, kD, kA, vn, kb2, NOP);
            vc = vn;
        }
        O[3] = MFMA(asbf(kD[2]), asbf(pA0), O[3]); O[3] = MFMA(asbf(kD[3]), asbf(pA1), O[3]);
        STAMP(7); STAMPC(21);
        f32x16 o;
        const unsigned cb = L_R2 + lane * 16;
#define SEND(a, sl) _Pragma("unroll") for (int u = 0; u < 4; ++u) lds_wrf4(cb + ((sl) * 4 + u) * 1024, (f32x4){O[a][4 * u], O[a][4 * u + 1], O[a][4 * u + 2], O[a][4 * u + 3]})
#define RECV(a) do { WAIT_LGKM0(); sig(C_CMB, lane); waitc<0>(C_CMB, 4); f32x4 pr[12]; \
            _Pragma("unroll") for (int k = 0; k < 12; ++k) pr[k] = lds_rdf4(cb + ((a) * 12 + k) * 1024); SBAR(); \
            _Pragma("unroll") for (int u = 0; u < 4; ++u) { const f32x4 t = (pr[u] + (f32x4){O[a][4 * u], O[a][4 * u + 1], O[a][4 * u + 2], O[a][4 * u + 3]}) + (pr[4 + u] + pr[8 + u]); \
                o[4 * u] = t[0]; o[4 * u + 1] = t[1]; o[4 * u + 2] = t[2]; o[4 * u + 3] = t[3]; } PIN(o); } while (0)
        if (w == 0) { SEND(1, 3); SEND(2, 6); SEND(3, 9); RECV(0); }
        else if (w == 1) { SEND(0, 0); SEND(2, 7); SEND(3, 10); RECV(1); }
        else if (w == 2) { SEND(0, 1); SEND(1, 4); SEND(3, 11); RECV(2); }
        else { SEND(0, 2); SEND(1, 5); SEND(2, 8); RECV(3); }
        float l = from_hi(o[12]);
        {
            f32x2 chk = {0.f, 0.f};
#pragma unroll
            for (int i = 0; i < 16; i += 2) chk = __builtin_elementwise_fma((f32x2){o[i], o[i + 1]}, (f32x2){0.f, 0.f}, chk);
            bool bad = !(chk[0] + chk[1] == 0.f) || !(l >= 1e-32f && l <= 1e30f);
            if (__any(bad)) {
                float mrun = -3.0e38f;
                o = f32x16{};
#pragma unroll 1
                for (int k2 = 0; k2 < 4; ++k2) {
                    poll(rws, WS_F1 + (((lane & 8) ? gv : gk) * 32 + 8 * k2 + (lane & 7)) * 128, tag);
#pragma unroll 1
                    for (int m2 = 0; m2 < 16; ++m2) {
                        const unsigned so = kvb + (k2 * 16 + m2) * 6144;
                        const u32x4 k0 = ld16(rws, lane * 16, so), k1 = ld16(rws, lane * 16 + 1024, so), v0 = ld16(rws, lane * 16 + 2048, so), v1 = ld16(rws, lane * 16 + 3072, so);
                        f32x16 s = MFMA(asbf(k0), asbf(q0), f32x16{}); s = MFMA(asbf(k1), asbf(q1), s);
                        const float mn = __builtin_fmaxf(mrun, swap_max(max16(s))), al = __builtin_amdgcn_exp2f(mrun - mn);
                        mrun = mn;
#pragma unroll
                        for (int i = 0; i < 16; ++i) { s[i] = __builtin_amdgcn_exp2f(s[i] - mn); o[i] *= al; }
                        o = MFMA(asbf(v0), asbf(pk8(s, 0)), o); o = MFMA(asbf(v1), asbf(pk8(s, 1)), o);
                    }
                }
                l = from_hi(o[12]);
            }
        }
        const float rl = __builtin_amdgcn_rcpf(l);
#pragma unroll
        for (int i = 0; i < 16; ++i) o[i] *= rl;
        u32x4 o0 = pk8(o, 0), o1 = pk8(o, 1);
        if (hi) { o1[2] = 0; o1[3] = 0; }
        const unsigned so = WS_O + ((tq * 16 + head) * 2) * 1024;
        st16(rws, o0, lane * 16, so); st16(rws, o1, lane * 16 + 1024, so);
        sig(C_ATT, lane);
        STAMP(8); FSTAMPC(27);
        fl0 = ldflag(rws, WS_FO + ((rt * 2 + (w & 1)) * 16 + (w >> 1) * 8 + ((lane + ct) & 7)) * 128);
        SBAR();
        if (r < 28) { waitc<0>(C_WO0, 4); read_wo(wf, w >> 1, ct, lane); }
        post(rws, WS_FO + (tq * 16 + head) * 128, tag, lane);
        FSTAMP(9);
    }

    if (r < 28) {
        const int br = role, tp = w & 1, kh = w >> 1, tto = rt * 2 + tp;
        const rsrc_t rout = mk_rsrc(ka[14], ka[15], 2048 * 1792);
        const unsigned ffo = WS_FO + (br * 1024 + tto * 16 + kh * 8 + ((lane + ct) & 7)) * 128;
        const unsigned ob = WS_O + br * (2u << 20) + (tto * 16 + kh * 8) * 2048;
        u32x4 of[16];
        f32x16 c0 = {}, c1 = {};
        if (role) {
            poll<HSLP>(rws, ffo, tag);
#pragma unroll
            for (int k = 0; k < 16; ++k) {
                if ((k & 3) == 0 && k) WAIT_VM(0);
                of[k] = ld16(rws, lane * 16 + (k & 1) * 1024, ob + (((k >> 1) + ct) & 7) * 2048); SBAR();
            }
            waitc<4>(C_ATT, 4);
            __builtin_amdgcn_s_setprio(0);
#pragma unroll
            for (int k = 0; k < 16; ++k) { c0 = MFMA(wf[k][0], asbf(of[k]), c0); c1 = MFMA(wf[k][1], asbf(of[k]), c1); }
        } else {
            unsigned req = 0, fin = 0;
#pragma unroll
            for (int k = 0; k < 16; ++k) asm volatile("" : "=v"(of[k]));
#pragma clang loop unroll(disable)
            for (unsigned it = 0; it < SPIN_CAP && fin != 255u; ++it) {
                unsigned rdy = 255u;
                if (req != 255u) {
                    const u32x2 f = it ? ldflag(rws, ffo) : fl0;
                    rdy = (unsigned)__builtin_amdgcn_ballot_w64(__builtin_bit_cast(unsigned long long, f) == __builtin_bit_cast(unsigned long long, tag)) & 255u;
                }
                CBAR();
                const unsigned todo = (req ^ (req + 1u)) >> 1 & ~fin, nw = rdy & ~req;
#pragma unroll
                for (int k = 0; k < 8; ++k)
                    if (nw & (1u << k)) { const unsigned sb = ob + ((k + ct) & 7) * 2048; of[2 * k] = ld16(rws, lane * 16, sb); of[2 * k + 1] = ld16(rws, lane * 16 + 1024, sb); CBAR(); }
#pragma unroll
                for (int k = 0; k < 8; ++k)
                    if (todo & (1u << k)) {
                        c0 = MFMA(wf[2 * k][0], asbf(of[2 * k]), c0); c1 = MFMA(wf[2 * k][1], asbf(of[2 * k]), c1);
                        c0 = MFMA(wf[2 * k + 1][0], asbf(of[2 * k + 1]), c0); c1 = MFMA(wf[2 * k + 1][1], asbf(of[2 * k + 1]), c1); CBAR();
                    }
                fin |= todo; req |= nw;
            }
            sig(C_GATH, lane);
        }
        STAMP(10);
        const unsigned xb_ = L_R1 + br * 16384;
        const unsigned vo = (tto * 32 + (lane & 31)) * 1792 + (hi ? 16 : 0), so = br * 896 + ct * 128 + kh * 64;
#define FINISH(KEEP, GIVE) do { \
            _Pragma("unroll") for (int u = 0; u < 4; ++u) lds_wrf4(xb_ + (w * 4 + u) * 1024 + lane * 16, (f32x4){GIVE[4 * u], GIVE[4 * u + 1], GIVE[4 * u + 2], GIVE[4 * u + 3]}); \
            WAIT_LGKM0(); sig(C_EX0 + br * 2 + tp, lane); waitc<0>(C_EX0 + br * 2 + tp, 2); \
            _Pragma("unroll") for (int u = 0; u < 4; ++u) { const f32x4 s = lds_rdf4(xb_ + ((w ^ 2) * 4 + u) * 1024 + lane * 16); \
                _Pragma("unroll") for (int i = 0; i < 4; ++i) KEEP[4 * u + i] += s[i]; } \
            if (role) waitc<1>(C_GATH, 4); \
            _Pragma("unroll") for (int gp = 0; gp < 2; ++gp) { \
                auto s0 = __builtin_amdgcn_permlane32_swap(pk2(KEEP[8 * gp], KEEP[8 * gp + 1]), pk2(KEEP[8 * gp + 4], KEEP[8 * gp + 5]), false, false); \
                auto s1 = __builtin_amdgcn_permlane32_swap(pk2(KEEP[8 * gp + 2], KEEP[8 * gp + 3]), pk2(KEEP[8 * gp + 6], KEEP[8 * gp + 7]), false, false); \
                st16a<0>(rout, (u32x4){s0[0], s1[0], s0[1], s1[1]}, vo + gp * 32, so); } } while (0)
        if (kh) FINISH(c1, c0); else FINISH(c0, c1);
        STAMP(12);
    }
    STAMP_FLUSH();
    asm volatile(".Lpf_end:");
}

extern "C" void kernel_launch(void* const* d_in, const int* in_sizes, int n_in, void* d_out, int out_size, void* d_ws, size_t ws_size,
                              hipStream_t stream) {
    (void)in_sizes; (void)n_in; (void)out_size; (void)ws_size;
    (void)hipFuncSetAttribute((const void*)fused_fwd, hipFuncAttributeMaxDynamicSharedMemorySize, LDS_BYTES);
    fused_fwd<<<256, 512, LDS_BYTES, stream>>>(d_in[0], d_in[1], d_in[2], d_in[3], d_in[4], d_in[5], d_in[6], d_out, d_ws);
}
```

```cpp
#define STAMPS 1
#include <hip/hip_runtime.h>
#include <hip/hip_bf16.h>
#include <stdint.h>
#include <type_traits>

typedef __attribute__((ext_vector_type(8))) __bf16 bf16x8;
typedef __attribute__((ext_vector_type(2))) __bf16 bf16x2;
typedef __attribute__((ext_vector_type(2))) float f32x2;
typedef __attribute__((ext_vector_type(4))) float f32x4;
typedef __attribute__((ext_vector_type(16))) float f32x16;
typedef __attribute__((ext_vector_type(4))) unsigned u32x4;
typedef __attribute__((ext_vector_type(2))) unsigned u32x2;
typedef __attribute__((ext_vector_type(4))) short s16x4;
typedef __amdgpu_buffer_rsrc_t rsrc_t;
#define LAS __attribute__((address_space(3)))
#define DI __device__ __forceinline__

constexpr unsigned WS_EPOCH = 0;
constexpr unsigned WS_F1 = 4096;
constexpr unsigned WS_F2 = WS_F1 + 1024 * 128;
constexpr unsigned WS_F3 = WS_F2 + 512 * 128;
constexpr unsigned WS_FO = WS_F3 + 256 * 128;
constexpr unsigned WS_QKV = 1u << 20;
constexpr unsigned WS_KVP = WS_QKV + (6u << 20);
constexpr unsigned WS_KVR = WS_KVP + (2u << 20);
constexpr unsigned WS_O = WS_KVR + (1u << 20);
constexpr unsigned WS_DBG = WS_O + (4u << 20);
constexpr unsigned WS_BYTES = WS_DBG + (1u << 20);

constexpr unsigned SLOT = 37888;
constexpr unsigned L_R0 = 0;
constexpr unsigned L_R1 = 57856;
constexpr unsigned L_R2 = L_R1 + 32768;
constexpr unsigned L_QX = L_R2 + 49152;
constexpr unsigned L_QB = L_R2;
constexpr unsigned L_PHI = 4 * SLOT + 64;
constexpr unsigned L_JUNK = L_PHI + 4096;
constexpr unsigned L_CNT = L_JUNK + 1024;
constexpr unsigned L_TAG = L_CNT + 256;
constexpr unsigned LDS_BYTES = L_TAG + 16;
enum { C_LAND0 = 24, C_DONE0 = 32, C_QB = 4, C_QX = 10, C_PV = 11, C_KVDONE = 12, C_WO0 = 13, C_WOFREE = 15, C_EX0 = 16, C_CMB = 20, C_ATT = 21, C_GATH = 22 };

#define MAP_SEP 0
#define PNX 3
#define PNW 3
#define FASTG 2
#define ROT 1
#define LATE1 0
#define HSLP 16
#define WOWIN 2
constexpr unsigned SPIN_CAP = 1u << 16;
constexpr float QSCALE = 0.18898223650461362f * 1.4426950408889634f;
constexpr float LOG2E = 1.4426950408889634f;
constexpr float REF_SHIFT = 100.f;

#define MFMA(a, b, c) __builtin_amdgcn_mfma_f32_32x32x16_bf16((a), (b), (c), 0, 0, 0)
#define CBAR() asm volatile("" ::: "memory")
#define WAIT_VM(n) asm volatile("s_waitcnt vmcnt(%0)" :: "i"(n) : "memory")
#define WAIT_LGKM0() asm volatile("s_waitcnt lgkmcnt(0)" ::: "memory")
#define SBAR() __builtin_amdgcn_sched_barrier(0)

#define STAMP_DECL unsigned stv = 0
#define STAMP(i) do { unsigned long long t_; asm volatile("s_memrealtime %0\n\ts_waitcnt lgkmcnt(0)" : "=s"(t_) :: "memory"); \
    asm volatile("v_writelane_b32 %0, %1, " #i : "+v"(stv) : "s"((unsigned)t_)); } while (0)
#define STAMPC(i) do { unsigned long long t_; asm volatile("s_memtime %0\n\ts_waitcnt lgkmcnt(0)" : "=s"(t_) :: "memory"); \
    asm volatile("v_writelane_b32 %0, %1, " #i : "+v"(stv) : "s"((unsigned)t_)); } while (0)
#define STAMP_FLUSH() do { STAMPC(31); STAMP(29); if (lane < 32) __builtin_amdgcn_raw_buffer_store_b32(stv, rws, WS_DBG + ((blockIdx.x * 8 + wid) * 32 + lane) * 4, 0, 0); } while (0)
#define FSTAMP(i) do {} while (0)
#define FSTAMPC(i) do {} while (0)
#define PIN(x) asm volatile("" : "+v"(x))

DI unsigned pk2(float a, float b) { f32x2 v = {a, b}; return __builtin_bit_cast(unsigned, __builtin_convertvector(v, bf16x2)); }
DI u32x4 pk8(const f32x16& a, int s) {
    return (u32x4){pk2(a[8 * s], a[8 * s + 1]), pk2(a[8 * s + 2], a[8 * s + 3]), pk2(a[8 * s + 4], a[8 * s + 5]), pk2(a[8 * s + 6], a[8 * s + 7])}; }
DI bf16x8 asbf(u32x4 v) { return __builtin_bit_cast(bf16x8, v); }
DI float swap_max(float m) { auto r = __builtin_amdgcn_permlane32_swap(__float_as_uint(m), __float_as_uint(m), false, false);
    return __builtin_fmaxf(__uint_as_float(r[0]), __uint_as_float(r[1])); }
DI float swap_min(float m) { auto r = __builtin_amdgcn_permlane32_swap(__float_as_uint(m), __float_as_uint(m), false, false);
    return __builtin_fminf(__uint_as_float(r[0]), __uint_as_float(r[1])); }
DI float swap_sum(float m) { auto r = __builtin_amdgcn_permlane32_swap(__float_as_uint(m), __float_as_uint(m), false, false);
    return __uint_as_float(r[0]) + __uint_as_float(r[1]); }
DI float from_hi(float m) { auto r = __builtin_amdgcn_permlane32_swap(__float_as_uint(m), __float_as_uint(m), false, false);
    return __uint_as_float(r[1]); }
DI float max16(const f32x16& s) {
    float a = __builtin_fmaxf(__builtin_fmaxf(s[0], s[1]), s[2]), b = __builtin_fmaxf(__builtin_fmaxf(s[3], s[4]), s[5]);
    a = __builtin_fmaxf(__builtin_fmaxf(a, s[6]), s[7]); b = __builtin_fmaxf(__builtin_fmaxf(b, s[8]), s[9]);
    a = __builtin_fmaxf(__builtin_fmaxf(a, s[10]), s[11]); b = __builtin_fmaxf(__builtin_fmaxf(b, s[12]), s[13]);
    a = __builtin_fmaxf(__builtin_fmaxf(a, s[14]), s[15]); return __builtin_fmaxf(a, b); }
DI float min16(const f32x16& s) {
    float a = __builtin_fminf(__builtin_fminf(s[0], s[1]), s[2]), b = __builtin_fminf(__builtin_fminf(s[3], s[4]), s[5]);
    a = __builtin_fminf(__builtin_fminf(a, s[6]), s[7]); b = __builtin_fminf(__builtin_fminf(b, s[8]), s[9]);
    a = __builtin_fminf(__builtin_fminf(a, s[10]), s[11]); b = __builtin_fminf(__builtin_fminf(b, s[12]), s[13]);
    a = __builtin_fminf(__builtin_fminf(a, s[14]), s[15]); return __builtin_fminf(a, b); }

DI void dma(rsrc_t rs, unsigned voff, unsigned soff, unsigned lds) {
    asm volatile("s_mov_b32 m0, %0\n\ts_nop 0\n\tbuffer_load_dwordx4 %1, %2, %3 offen lds" :: "s"(lds), "v"(voff), "s"(rs), "s"(soff) : "memory"); }
DI u32x4 lds_rd128(unsigned a) { return *(LAS const u32x4*)(uintptr_t)a; }
DI f32x4 lds_rdf4(unsigned a) { return *(LAS const f32x4*)(uintptr_t)a; }
DI void lds_wr128(unsigned a, u32x4 v) { *(LAS u32x4*)(uintptr_t)a = v; }
DI void lds_wrf4(unsigned a, f32x4 v) { *(LAS f32x4*)(uintptr_t)a = v; }
DI void lds_wr64(unsigned a, u32x2 v) { *(LAS u32x2*)(uintptr_t)a = v; }
DI bf16x8 lds_tr2(unsigned a0, unsigned a1) {
    s16x4 lo = __builtin_amdgcn_ds_read_tr16_b64_v4i16((LAS s16x4*)(uintptr_t)a0), hi = __builtin_amdgcn_ds_read_tr16_b64_v4i16((LAS s16x4*)(uintptr_t)a1);
    return __builtin_bit_cast(bf16x8, __builtin_shufflevector(lo, hi, 0, 1, 2, 3, 4, 5, 6, 7)); }
DI void sig(int c, int) {
    asm volatile("s_mov_b64 exec, 1\n\tds_add_u32 %0, %1\n\ts_mov_b64 exec, -1" :: "v"(L_CNT + 4 * c), "v"(1u) : "memory"); }
template <int SLP = 1> DI void waitc(int c, unsigned n) {
    unsigned v, sv, it;
    asm volatile("s_mov_b32 %2, %5\n"
                 "1:\n\tds_read_b32 %0, %3\n\ts_waitcnt lgkmcnt(0)\n\tv_readfirstlane_b32 %1, %0\n\ts_cmp_ge_u32 %1, %4\n\ts_cbranch_scc1 2f\n\t"
                 "s_sleep %6\n\ts_sub_u32 %2, %2, 1\n\ts_cbranch_scc0 1b\n2:"
                 : "=&v"(v), "=&s"(sv), "=&s"(it) : "v"(L_CNT + 4 * c), "i"(n), "i"(SPIN_CAP), "i"(SLP) : "memory", "scc"); }
DI unsigned rd_cnt(int c) { return *(volatile LAS const unsigned*)(uintptr_t)(L_CNT + 4 * c); }
DI u32x2 rd_tag() { const u32x2 t = *(LAS const u32x2*)(uintptr_t)L_TAG;
    return (u32x2){(unsigned)__builtin_amdgcn_readfirstlane(t[0]), (unsigned)__builtin_amdgcn_readfirstlane(t[1])}; }
DI u32x4 ld16(rsrc_t rs, unsigned voff, unsigned soff) { return __builtin_amdgcn_raw_buffer_load_b128(rs, voff, soff, 16); }
template <int AUX> DI void st16a(rsrc_t rs, u32x4 v, unsigned voff, unsigned soff) {
    __builtin_amdgcn_raw_buffer_store_b128(v, rs, voff, soff, AUX); asm volatile("s_nop 1" :: "v"(v) : "memory"); }
DI void st16(rsrc_t rs, u32x4 v, unsigned voff, unsigned soff) { st16a<16>(rs, v, voff, soff); }
DI u32x2 ldflag(rsrc_t rs, unsigned voff) { return __builtin_amdgcn_raw_buffer_load_b64(rs, voff, 0, 16); }
DI bool tagok(u32x2 f, u32x2 tag) {
    return __builtin_amdgcn_ballot_w64(__builtin_bit_cast(unsigned long long, f) == __builtin_bit_cast(unsigned long long, tag)) == ~0ull; }
template <int SLP = 2> DI void poll(rsrc_t rs, unsigned voff, u32x2 tag) {
    CBAR();
#pragma clang loop unroll(disable)
    for (unsigned it = 0; it < SPIN_CAP; ++it) { u32x2 f = ldflag(rs, voff); if (tagok(f, tag)) break; __builtin_amdgcn_s_sleep(SLP); CBAR(); }
    CBAR(); }
DI void post(rsrc_t rs, unsigned off, u32x2 tag, int lane) {
    WAIT_VM(0);
    if (lane == 0) __builtin_amdgcn_raw_buffer_store_b64(tag, rs, off, 0, 16);
    CBAR(); }

#define GB(n, k) "v[176+20*" #n "+4*" #k ":179+20*" #n "+4*" #k "]"
#define GT(A, n, t, i) "v_mfma_f32_32x32x16_bf16 %[" #A "], " GB(n, (2+i)) ", " GB(n, t) ", %[" #A "]\n\t"
#define GD(A, n, t, i) "v_mfma_f32_32x32x16_bf16 %[" #A "], " GB(n, t) ", " GB(n, (2+i)) ", %[" #A "]\n\t"
#define GX(n, t, X) "ds_read_b128 " GB(n, t) ", " X " offset:4096*" #t "\n\t"
#define GF(n, i, W, O) "ds_read_b64_tr_b16 v[176+20*" #n "+8+4*" #i ":177+20*" #n "+8+4*" #i "], " W " offset:" O "\n\t" \
                       "ds_read_b64_tr_b16 v[178+20*" #n "+8+4*" #i ":179+20*" #n "+8+4*" #i "], " W " offset:" O "+336\n\t"
#define GSTEP(M0, M1, M2, c, n, X, O, P0, P0B, P2, P4, P5) \
    M0(a0, c, 0, 0) P0 GX(n, 0, X) P0B M0(a3, c, 1, 0) GF(n, 0, "%[wa]", O) M1(a1, c, 0, 1) GX(n, 1, X) P2 M1(a4, c, 1, 1) GF(n, 1, "%[wb]", O) \
    M2(a2, c, 0, 2) GF(n, 2, "%[wc]", O) P4 M2(a5, c, 1, 2) P5
#define GLOOP(M0, M1, M2) \
    GX(0, 0, "%[xa]") GF(0, 0, "%[wa]", "0") GX(0, 1, "%[xa]") GF(0, 1, "%[wb]", "0") GF(0, 2, "%[wc]", "0") \
    GX(1, 0, "v170") GF(1, 0, "%[wa]", "5376") GX(1, 1, "v170") GF(1, 1, "%[wb]", "5376") GF(1, 2, "%[wc]", "5376") \
    "s_waitcnt lgkmcnt(8)\n" \
    "1:\n\t" \
    GSTEP(M0, M1, M2, 0, 2, "v171", "10752", "", "ds_read_b128 v[166:169], %[la]\n\t", "v_xor_b32 v172, 0x60, %[xa]\n\t", "s_and_b32 %[so], %[g], 3\n\t", \
          "s_mul_i32 %[so], %[so], %[slot]\n\ts_waitcnt lgkmcnt(9)\n\t") \
    GSTEP(M0, M1, M2, 1, 3, "v172", "16128", "", "v_add_u32 %[xa], %[so], %[xa0]\n\ts_waitcnt lgkmcnt(8)\n\t", "v_min3_u32 v173, v166, v167, v168\n\tv_xor_b32 v170, 32, %[xa]\n\t", \
          "v_min_u32 v173, v173, v169\n\tv_add_u32 %[wa], %[so], %[wa0]\n\t", "v_add_u32 %[wb], %[so], %[wb0]\n\tv_readfirstlane_b32 %[rf], v173\n\tv_add_u32 %[wc], %[so], %[wc0]\n\ts_waitcnt lgkmcnt(8)\n\t") \
    GSTEP(M0, M1, M2, 2, 0, "%[xa]", "0", "s_cmp_gt_u32 %[rf], %[g]\n\ts_cbranch_scc0 3f\n2:\n\t", "", "v_xor_b32 v171, 64, %[xa]\n\t", "", \
          "s_waitcnt lgkmcnt(8)\n\tv_mov_b32 v174, %[g]\n\t") \
    GSTEP(M0, M1, M2, 3, 1, "v170", "5376", "", "s_add_u32 %[g], %[g], 1\n\t", "s_cmp_lt_u32 %[g], 8\n\t", "", \
          "s_waitcnt lgkmcnt(8)\n\tds_write_b32 %[da], v174\n\ts_cbranch_scc1 1b\n\t") \
    "s_branch 4f\n" \
    "3:\n\ts_mov_b32 %[so], %[cap]\n" \
    "5:\n\tds_read_b128 v[166:169], %[la]\n\ts_waitcnt lgkmcnt(0)\n\tv_min3_u32 v173, v166, v167, v168\n\tv_min_u32 v173, v173, v169\n\ts_nop 0\n\tv_readfirstlane_b32 %[rf], v173\n\ts_cmp_gt_u32 %[rf], %[g]\n\ts_cbranch_scc1 2b\n\t" \
    "s_sub_u32 %[so], %[so], 1\n\ts_cbranch_scc0 5b\n\ts_branch 2b\n" \
    "4:\n\ts_waitcnt lgkmcnt(0)\n\ts_nop 7\n\ts_nop 3"
template <int ND, class Start>
DI void gemm6(unsigned xa0, unsigned wa0, unsigned wb0, unsigned wc0, int w, f32x16 (&acc)[6], Start start) {
    unsigned xa = xa0, wa = wa0, wb = wb0, wc = wc0, g = 1, so, rf;
    const unsigned la = L_CNT + 4 * C_LAND0, da = L_CNT + 4 * (C_DONE0 + w);
#pragma unroll
    for (int i = 0; i < 6; ++i) { acc[i] = f32x16{}; PIN(acc[i]); }
    PIN(xa); PIN(wa); PIN(wb); PIN(wc);
    start();
#define GASM(M0, M1, M2) asm volatile("v_xor_b32 v170, 32, %[xa]\n\tv_xor_b32 v171, 64, %[xa]\n\t" GLOOP(M0, M1, M2) \
        : [a0] "+v"(acc[0]), [a1] "+v"(acc[1]), [a2] "+v"(acc[2]), [a3] "+v"(acc[3]), [a4] "+v"(acc[4]), [a5] "+v"(acc[5]), \
          [xa] "+v"(xa), [wa] "+v"(wa), [wb] "+v"(wb), [wc] "+v"(wc), [g] "+s"(g), [so] "=&s"(so), [rf] "=&s"(rf) \
        : [xa0] "v"(xa0), [wa0] "v"(wa0), [wb0] "v"(wb0), [wc0] "v"(wc0), [la] "v"(la), [da] "v"(da), [slot] "i"(SLOT), [cap] "i"(SPIN_CAP) \
        : "memory", "scc", "v166", "v167", "v168", "v169", "v170", "v171", "v172", "v173", "v174", "v175", "v176", "v177", "v178", "v179", "v180", "v181", "v182", "v183", "v184", "v185", "v186", "v187", "v188", "v189", "v190", "v191", "v192", "v193", "v194", "v195", "v196", "v197", "v198", "v199", "v200", "v201", "v202", "v203", "v204", "v205", "v206", "v207", "v208", "v209", "v210", "v211", "v212", "v213", "v214", "v215", "v216", "v217", "v218", "v219", "v220", "v221", "v222", "v223", "v224", "v225", "v226", "v227", "v228", "v229", "v230", "v231", "v232", "v233", "v234", "v235", "v236", "v237", "v238", "v239", "v240", "v241", "v242", "v243", "v244", "v245", "v246", "v247", "v248", "v249", "v250", "v251", "v252", "v253", "v254", "v255")
    if constexpr (ND == 0) GASM(GT, GT, GT); else if constexpr (ND == 1) GASM(GT, GT, GD); else GASM(GD, GD, GD);
}
DI unsigned min4(u32x4 d) { return __builtin_amdgcn_readfirstlane(min(min(d[0], d[1]), min(d[2], d[3]))); }
DI u32x4 rd4(int c) { return *(volatile LAS const u32x4*)(uintptr_t)(L_CNT + 4 * c); }
DI void wait4(int c, unsigned n) {
    CBAR();
#pragma clang loop unroll(disable)
    for (unsigned it = 0; it < SPIN_CAP; ++it) if (min4(rd4(c)) >= n) break;
    CBAR(); }
template <int P> DI void dma_x4(rsrc_t rs, unsigned voff, unsigned soff, unsigned lds) {
    asm volatile("s_mov_b32 m0, %1\n\ts_nop %4\n\tbuffer_load_dwordx4 %2, %3, %0 offen lds\n\t"
                 "s_add_u32 m0, m0, 0x1000\n\ts_add_u32 %0, %0, 28672\n\ts_nop %4\n\tbuffer_load_dwordx4 %2, %3, %0 offen lds\n\t"
                 "s_add_u32 m0, m0, 0x1000\n\ts_add_u32 %0, %0, 28672\n\ts_nop %4\n\tbuffer_load_dwordx4 %2, %3, %0 offen lds\n\t"
                 "s_add_u32 m0, m0, 0x1000\n\ts_add_u32 %0, %0, 28672\n\ts_nop %4\n\tbuffer_load_dwordx4 %2, %3, %0 offen lds"
                 : "+s"(soff) : "s"(lds), "v"(voff), "s"(rs), "i"(P) : "memory", "scc"); }
template <int P> DI void dma_w5(rsrc_t rs, const unsigned (&v)[6], unsigned soff, unsigned lds) {
    asm volatile("s_mov_b32 m0, %0\n\ts_nop %8\n\tbuffer_load_dwordx4 %3, %1, %2 offen lds\n\t"
                 "s_add_u32 m0, m0, 0x1000\n\ts_nop %8\n\tbuffer_load_dwordx4 %4, %1, %2 offen lds\n\t"
                 "s_add_u32 m0, m0, 0x1000\n\ts_nop %8\n\tbuffer_load_dwordx4 %5, %1, %2 offen lds\n\t"
                 "s_add_u32 m0, m0, 0x1000\n\ts_nop %8\n\tbuffer_load_dwordx4 %6, %1, %2 offen lds\n\t"
                 "s_add_u32 m0, m0, 0x1000\n\ts_nop %8\n\tbuffer_load_dwordx4 %7, %1, %2 offen lds"
                 :: "s"(lds), "s"(rs), "s"(soff), "v"(v[0]), "v"(v[1]), "v"(v[2]), "v"(v[3]), "v"(v[4]), "i"(P) : "memory", "scc"); }
DI unsigned tr_addr128(unsigned base, int cb, int rowgrp, int lane) {
    const int q = (lane & 15) >> 2, byte = cb + 32 * ((lane >> 4) & 1) + 8 * (lane & 3);
    return base + (rowgrp + q) * 128 + ((((byte >> 4) ^ ((q >> 1) << 2)) & 7) << 4) + (byte & 8);
}
DI void feat(const f32x16& p, f32x16& e, f32x16& g) {
    const float mx = swap_max(max16(p)) * LOG2E, mn = swap_min(min16(p)) * LOG2E;
#pragma unroll
    for (int i = 0; i < 16; i += 2) {
        const f32x2 pp = {p[i], p[i + 1]};
        const f32x2 a = __builtin_elementwise_fma(pp, (f32x2){LOG2E, LOG2E}, (f32x2){-mx, -mx}), c = __builtin_elementwise_fma(pp, (f32x2){-LOG2E, -LOG2E}, (f32x2){mn, mn});
        e[i] = __builtin_amdgcn_exp2f(a[0]); e[i + 1] = __builtin_amdgcn_exp2f(a[1]); g[i] = __builtin_amdgcn_exp2f(c[0]); g[i + 1] = __builtin_amdgcn_exp2f(c[1]);
    }
}

typedef __attribute__((ext_vector_type(16))) unsigned u32x16;
DI rsrc_t mk_rsrc(unsigned lo, unsigned hi, unsigned bytes) {
    return __builtin_amdgcn_make_buffer_rsrc((void*)(((unsigned long long)hi << 32) | lo), 0, bytes, 0x00020000); }
DI void read_wo(bf16x8 (&wf)[16][2], int kh, int rot, int lane) {
    const unsigned ta0 = tr_addr128(L_R0, 0, kh * 224 + 4 * (lane >> 5), lane), ta1 = tr_addr128(L_R0, 64, kh * 224 + 4 * (lane >> 5), lane);
#pragma unroll
    for (int k = 0; k < 8; ++k) {
        const unsigned ro = ((k + rot) & 7) * 28 * 128, b0 = ta0 + ro, b1 = ta1 + ro;
        wf[2 * k][0] = lds_tr2(b0, b0 + 1024); wf[2 * k][1] = lds_tr2(b1, b1 + 1024);
        wf[2 * k + 1][0] = lds_tr2(b0 + 2048, b0 + 3072); wf[2 * k + 1][1] = lds_tr2(b1 + 2048, b1 + 3072);
    }
}
DI void ones_row(u32x4& a, u32x4& b, u32x2 c) {
    u32x2 a0 = {a[0], a[1]}, a1 = {a[2], a[3]}, b0 = {b[0], b[1]}, b1 = {b[2], b[3]};
    asm volatile("s_mov_b64 exec, %4\n\tv_mov_b64 %0, %5\n\tv_mov_b64 %1, %5\n\tv_mov_b64 %2, %5\n\tv_mov_b64 %3, %5\n\ts_mov_b64 exec, -1"
                 : "+v"(a0), "+v"(a1), "+v"(b0), "+v"(b1) : "s"(0xF0000000F0000000ull), "v"(c));
    a = (u32x4){a0[0], a0[1], a1[0], a1[1]}; b = (u32x4){b0[0], b0[1], b1[0], b1[1]}; }
template <int ND>
DI void publish_a(f32x16 (&acc)[6], rsrc_t rws, const unsigned (&so)[3], int nq, int lane, bool hi, u32x2 oc) {
#pragma unroll
    for (int i = 0; i < 3; ++i) {
        if (i < 3 - ND && i < nq) {
            asm volatile("" ::: "memory");
#pragma unroll
            for (int k = 0; k < 16; ++k) { acc[i][k] *= QSCALE; acc[3 + i][k] *= QSCALE; }
            PIN(acc[i]); PIN(acc[3 + i]);
        }
        const unsigned pad = i < nq ? 0u : 0x00003F80u;
#pragma unroll
        for (int t = 0; t < 2; ++t) {
            u32x4 v0 = pk8(acc[3 * t + i], 0), v1 = pk8(acc[3 * t + i], 1);
            if (i >= 3 - ND) ones_row(v0, v1, oc); else { v1[2] = hi ? pad : v1[2]; v1[3] = hi ? 0u : v1[3]; }
            st16(rws, v0, lane * 16, so[i] + t * 6144); st16(rws, v1, lane * 16 + 1024, so[i] + t * 6144);
            SBAR();
        }
    }
}
DI void phi_w(bf16x8& f0, u32x4& f1, int hh, int lane, bool hi) {
    const unsigned pb = L_PHI + hh * 1568 + (4 * (lane >> 5) + ((lane & 15) >> 2)) * 56 + ((lane & 19) == 19 ? 0 : 32 * ((lane >> 4) & 1) + 8 * (lane & 3));
    f0 = lds_tr2(pb, pb + 8 * 56);
    f1 = __builtin_bit_cast(u32x4, lds_tr2(pb + 16 * 56, pb + 24 * 56));
    if (hi) { f1[2] = 0; f1[3] = 0; }
}
DI f32x16 phi_pre(u32x4 k0, u32x4 k1, bf16x8 f0, u32x4 f1) {
    f32x16 p = MFMA(f0, asbf(k0), f32x16{});
    return MFMA(asbf(f1), asbf(k1), p);
}

__global__ __launch_bounds__(512, 2) void fused_fwd(const void*, const void*, const void*, const void*, const void*, const void*, const void*, void*, void*) {
    STAMP_DECL; STAMP(0); STAMPC(30);
    u32x16 ka; u32x2 kb;
    asm volatile("s_load_dwordx16 %0, %2, 0x0\n\ts_load_dwordx2 %1, %2, 0x40" : "=&s"(ka), "=&s"(kb) : "s"(__builtin_amdgcn_kernarg_segment_ptr()));
    const int tid = threadIdx.x, lane = tid & 63, wid = __builtin_amdgcn_readfirstlane(tid >> 6);
    const int b = blockIdx.x, xcd = b & 7, r = b >> 3, w = wid & 3, role = wid >> 2;
    const int isb = r & 1, ml = (r >> 1) & 3, nl = r >> 3;
    const int mt = (xcd & 3) * 4 + ml, nt = (xcd >> 2) * 4 + nl;
    const bool hi = lane >= 32;
    int g0 = ROT == 0 ? 0 : ROT == 1 ? ((ml + nl) & 1) * 4 : ROT == 2 ? 2 * ml + nl : ROT == 3 ? (isb ? ((ml + nl) & 1) * 4 : 0) : ROT == 4 ? (isb ? 2 * ml + nl : ((ml + nl) & 1) * 4) : (isb ? 2 * ml + nl : 0); g0 -= (g0 >= 7) ? 7 : 0;
    const int head = 2 * xcd + (r >> 4), qt = r & 15;
    const int ct = r >> 2, rt = xcd * 4 + (r & 3);
    u32x2 tag;
    bf16x8 wf[16][2];
    u32x2 fl0 = {};
    unsigned pfd = 0;

    unsigned xvo, wvo[6];
    if (role) {
        __builtin_amdgcn_s_setprio(2);
        xvo = (w * 8 + (lane >> 3)) * 896 + ((((lane & 7) ^ (lane >> 4) ^ ((w & 1) << 2)) & 7) << 4);
        PIN(xvo);
#pragma unroll
        for (int i = 0; i < 6; ++i) {
            const int c = 64 * (w + 4 * i) + lane, rho = (c * 3121) >> 16, ch = c - 21 * rho, win = (ch * 37) >> 8;
            const int krow = (rho & ~15) | ((rho & 3) << 2) | ((rho >> 2) & 3);
            wvo[i] = krow * 2688 + ch * 16 + (isb ? win * (896 - 112) : 0);
            PIN(wvo[i]);
        }
    }
    if (wid == 0) {
        unsigned long long pc; unsigned len;
        asm volatile("s_getpc_b64 %0\n.Lpf_begin%=:\n\ts_mov_b32 %1, .Lpf_end-.Lpf_begin%=" : "=s"(pc), "=s"(len));
        const rsrc_t rc = __builtin_amdgcn_make_buffer_rsrc((void*)pc, 0, len, 0x00020000);
        asm volatile("s_nop 4\n\tbuffer_load_dword %0, %1, %2, 0 offen" : "+v"(pfd) : "v"(lane < 12 ? (r * 12 + lane) * 128 : 0x7FFFFFF0), "s"(rc) : "memory");
        *(LAS unsigned*)(uintptr_t)(L_CNT + 4 * lane) = 0u;
    }
    asm volatile("s_waitcnt lgkmcnt(0)" : "+s"(ka), "+s"(kb) :: "memory");
    const rsrc_t rws = mk_rsrc(kb[0], kb[1], WS_BYTES);
    u32x4 pe[2][4];
    if (role) {
        const rsrc_t rx = mk_rsrc(isb ? ka[6] : ka[0], isb ? ka[7] : ka[1], 2048 * 896), rw = mk_rsrc(isb ? ka[8] : ka[2], isb ? ka[9] : ka[3], 448 * 2688);
        const unsigned xs0 = mt * 114688, ws0 = (isb ? 112 : 336) * nt;
        auto feed = [&](int g, auto PX, auto PW) {
            int ga = g + g0; ga -= (ga >= 7) ? 7 : 0;
            const unsigned ws = ws0 + ga * 64 * 2688, l = (g & 3) * SLOT + w * 1024;
            dma_x4<decltype(PX)::value>(rx, xvo, xs0 + ga * 128, l);
            dma_w5<decltype(PW)::value>(rw, wvo, ws, l + 16384);
            if (w == 0) dma(rw, wvo[5], ws, l + 16384 + 5 * 4096); };
        const unsigned lw = L_CNT + 4 * (C_LAND0 + w);
        constexpr std::integral_constant<int, 0> fast{};
        FSTAMP(18);
        dma(mk_rsrc(ka[10], ka[11], 16 * 1568), lane * 16, nt * 3136 + w * 1024, L_PHI + w * 1024);
        feed(0, fast, fast);
        if (LATE1 == 0) feed(1, fast, fast);
        asm volatile("s_barrier" ::: "memory");
        if (LATE1 == 1) { WAIT_VM(4); feed(1, fast, fast); }
        if (LATE1 == 2) { WAIT_VM(0); *(volatile LAS unsigned*)(uintptr_t)lw = 1; feed(1, fast, fast); }
        if (w == 0) WAIT_VM(10); else WAIT_VM(9);
        *(volatile LAS unsigned*)(uintptr_t)lw = 1;
        STAMP(1);
#pragma unroll 1
        for (int g = 1; g < 6; ++g) {
            if (g >= 3) wait4(C_DONE0, g - 2);
            if (g < FASTG) feed(g + 1, fast, fast); else feed(g + 1, std::integral_constant<int, PNX>{}, std::integral_constant<int, PNW>{});
            if (w == 0) WAIT_VM(10); else WAIT_VM(9);
            *(volatile LAS unsigned*)(uintptr_t)lw = g + 1;
        }
        {
            const rsrc_t rt_ = mk_rsrc(w < 2 ? ka[4] : ka[12], w < 2 ? ka[5] : ka[13], 448 * 896);
            asm volatile("s_mov_b32 m0, %0\n\ts_nop 3\n\tbuffer_load_dword %1, %2, 0 offen lds"
                         :: "s"(L_JUNK), "v"(lane < 49 ? (r * 98 + (w & 1) * 49 + lane) * 128 : 0x7FFFFF00), "s"(rt_) : "memory");
        }
        WAIT_VM(1);
        *(volatile LAS unsigned*)(uintptr_t)lw = 8;
        STAMP(2);
        tag = rd_tag();
        if (isb) {
            bf16x8 ph0; u32x4 ph1;
            wait4(C_LAND0, 1);
            phi_w(ph0, ph1, w >> 1, lane, hi);
            const int tk = lane & 31, pi = (((tk >> 1) & 1) << 3) | (tk & 1) | (((tk >> 2) & 3) << 1);
            const unsigned ib = L_R1 + (w >> 1) * 16384 + ((w & 1) * 64 + tk) * 128 + ((pi ^ (lane >> 5)) << 3);
            const float lo = hi ? 0.f : 1.f;
            waitc<1>(C_QB + w, 1);
            f32x16 pp[2];
#pragma unroll
            for (int t = 0; t < 2; ++t) { const unsigned qa = L_QB + ((w * 2 + t) * 4 + 2) * 1024 + lane * 16; pp[t] = phi_pre(lds_rd128(qa), lds_rd128(qa + 1024), ph0, ph1); }
#pragma unroll
            for (int t = 0; t < 2; ++t) {
                f32x16 e, g;
                feat(pp[t], e, g);
                f32x2 s2 = {e[0], e[1]}, g2 = {g[0], g[1]};
#pragma unroll
                for (int i = 2; i < 12; i += 2) { s2 += (f32x2){e[i], e[i + 1]}; g2 += (f32x2){g[i], g[i + 1]}; }
                s2 = __builtin_elementwise_fma((f32x2){e[12], e[13]} + (f32x2){e[14], e[15]}, (f32x2){lo, lo}, s2);
                g2 = __builtin_elementwise_fma((f32x2){g[12], g[13]} + (f32x2){g[14], g[15]}, (f32x2){lo, lo}, g2);
                const float se = __builtin_amdgcn_rcpf(swap_sum(s2[0] + s2[1])), sg_ = __builtin_amdgcn_rcpf(swap_sum(g2[0] + g2[1]));
#pragma unroll
                for (int u = 0; u < 8; ++u) {
                    const int k = 4 * (u & 3);
                    const f32x16& z = (u >> 2) ? g : e; const float sc = (u >> 2) ? sg_ : se;
                    lds_wr64((ib ^ ((8 * (u >> 2) + 2 * (u & 3)) << 3)) + t * 4096, (u32x2){pk2(z[k] * sc, z[k + 1] * sc), pk2(z[k + 2] * sc, z[k + 3] * sc)});
                }
            }
            WAIT_LGKM0(); sig(C_PV, lane);
#pragma unroll
            for (int t = 0; t < 2; ++t) {
                const unsigned qa = L_QB + ((w * 2 + t) * 4) * 1024 + lane * 16;
                f32x16 p = phi_pre(lds_rd128(qa), lds_rd128(qa + 1024), ph0, ph1), e, g;
                feat(p, e, g);
                pe[t][0] = pk8(e, 0); pe[t][1] = pk8(e, 1); pe[t][2] = pk8(g, 0); pe[t][3] = pk8(g, 1);
            }
        }
        STAMP(3);
    } else {
        f32x16 acc[6];
        const int tp = w & 1, sg = w >> 1;
        const int rr = tp * 64 + (lane & 31);
        const unsigned xa0 = rr * 128 + ((((lane >> 5) ^ (rr >> 1)) & 7) << 4);
        const unsigned lb = 16384 + (4 * ((lane & 15) >> 2) + 2 * (lane >> 5)) * 336 + 32 * ((lane >> 4) & 1) + 8 * (lane & 3);
        const unsigned wst = isb ? 112 : 56, wa0 = lb + 56 * (isb ? sg : 3 * sg), wb0 = wa0 + wst, wc0 = wb0 + wst;
        const int G = 2 * nt + sg;
        if (w == 0) {
            const u32x2 ep = ldflag(rws, WS_EPOCH);
            const unsigned long long e = (((unsigned long long)ep[1] << 32) | ep[0]) + 1;
            lds_wr64(L_TAG, (u32x2){(unsigned)e, (unsigned)(e >> 32)});
            WAIT_VM(0);
            asm volatile("" :: "v"(pfd));
        }
        unsigned so[3];
#pragma unroll
        for (int i = 0; i < 3; ++i) { const int sl = 3 * G + i; so[i] = WS_QKV + (((sl & 15) * 64 + mt * 4 + 2 * tp) * 6 + (sl >> 4) * 2) * 1024; asm volatile("" : "+s"(so[i])); }
        const int nq = 16 - 3 * G;
        u32x2 oc; oc[0] = oc[1] = (lane & 31) == 28 ? 0x3F803F80u : 0u; PIN(oc);
        auto start = [&]() { asm volatile("s_waitcnt lgkmcnt(0)\n\ts_barrier" ::: "memory"); tag = rd_tag(); asm volatile("" : "+s"(tag)); wait4(C_LAND0, 1); STAMP(1); STAMPC(23); };
        if (isb || G == 10) { gemm6<1>(xa0, wa0, wb0, wc0, w, acc, start); STAMPC(24); STAMP(2); if (!isb) publish_a<1>(acc, rws, so, nq, lane, hi, oc); }
        else if (G < 10) { gemm6<0>(xa0, wa0, wb0, wc0, w, acc, start); STAMPC(24); STAMP(2); publish_a<0>(acc, rws, so, nq, lane, hi, oc); }
        else { gemm6<3>(xa0, wa0, wb0, wc0, w, acc, start); STAMPC(24); STAMP(2); publish_a<3>(acc, rws, so, nq, lane, hi, oc); }
        if (!isb) {
            FSTAMP(15);
            post(rws, WS_F1 + (G * 32 + mt * 2 + tp) * 128, tag, lane);
            wait4(C_DONE0, 7);
        } else {
            u32x4 fr[2][3][2];
#pragma unroll
            for (int t = 0; t < 2; ++t) {
#pragma unroll
                for (int i = 0; i < 3; ++i) { fr[t][i][0] = pk8(acc[3 * t + i], 0); fr[t][i][1] = pk8(acc[3 * t + i], 1); }
                ones_row(fr[t][2][0], fr[t][2][1], oc);
            }
            wait4(C_DONE0, 7);
#pragma unroll
            for (int t = 0; t < 2; ++t)
#pragma unroll
                for (int s = 0; s < 2; ++s) {
                    lds_wr128(L_QB + (((w * 2 + t) * 2 + 1) * 2 + s) * 1024 + lane * 16, fr[t][1][s]);
                    lds_wr128(L_QB + (((w * 2 + t) * 2) * 2 + s) * 1024 + lane * 16, fr[t][0][s]);
                    lds_wr128(L_R0 + ((sg * 4 + 2 * tp + t) * 2 + s) * 1024 + lane * 16, fr[t][2][s]);
                }
            WAIT_LGKM0(); sig(C_QB + w, lane);
        }
        STAMP(3);
    }
    FSTAMP(4);

    if (role) {
        const int wc = (lane & 7) ^ (((lane >> 4) & 1) << 2);
        const unsigned wovo = (lane >> 3) * 896 + wc * 16;
        if (isb) {
            waitc<0>(C_PV, 4);
            const int hh = w >> 1, ft = w & 1, q = (lane & 15) >> 2, P = 8 * ft + 4 * ((lane >> 4) & 1) + (lane & 3);
            bf16x8 fa[8], fb[8];
#pragma unroll
            for (int st = 0; st < 8; ++st) {
                const int t0 = 16 * (st & 1) + 4 * (lane >> 5) + q, t1 = t0 + 8;
                const int p0 = (((t0 >> 1) & 1) << 3) | (t0 & 1) | (((t0 >> 2) & 3) << 1), p1 = (((t1 >> 1) & 1) << 3) | (t1 & 1) | (((t1 >> 2) & 3) << 1);
                const unsigned ib = L_R1 + hh * 16384 + (st >> 1) * 4096;
                fa[st] = lds_tr2(ib + t0 * 128 + ((P ^ p0) << 3), ib + t1 * 128 + ((P ^ p1) << 3));
                fb[st] = asbf(lds_rd128(L_R0 + ((hh * 4 + (st >> 1)) * 2 + (st & 1)) * 1024 + lane * 16));
            }
            SBAR();
            f32x16 kv = {};
#pragma unroll
            for (int st = 0; st < 8; ++st) kv = MFMA(fa[st], fb[st], kv);
            WAIT_LGKM0(); sig(C_KVDONE, lane);
            const int hk = 2 * nt + hh;
#pragma unroll
            for (int u = 0; u < 4; ++u)
                st16(rws, __builtin_bit_cast(u32x4, (f32x4){kv[4 * u], kv[4 * u + 1], kv[4 * u + 2], kv[4 * u + 3]}), lane * 16 + u * 1024, WS_KVP + ((hk * 16 + mt) * 8 + ft * 4) * 1024);
            FSTAMP(19);
            post(rws, WS_F2 + ((hk * 16 + mt) * 2 + ft) * 128, tag, lane);
            waitc<0>(C_KVDONE, 4);
        }
        STAMP(5);
        if (r < 28) {
            waitc<HSLP>(C_QX, 4);
            if (w == 0) *(LAS u32x2*)(uintptr_t)(L_R0 + 57344 + 8 * lane) = (u32x2){0u, 0u};
            const rsrc_t rwob = mk_rsrc(ka[12], ka[13], 448 * 896);
#pragma unroll
            for (int i = 0; i < 14; ++i) { WAIT_VM(WOWIN); dma(rwob, wovo, ct * 128 + (w + 4 * i) * 8 * 896, L_R0 + (w + 4 * i) * 1024); }
            WAIT_VM(0); WAIT_LGKM0(); sig(C_WO0 + 1, lane); waitc<HSLP>(C_WO0 + 1, 4);
            read_wo(wf, w >> 1, ct, lane);
            WAIT_LGKM0(); sig(C_WOFREE, lane);
        }
        if (w == 3) {
            const int hr = head, i = lane & 15, pg = lane >> 4;
            poll<HSLP>(rws, WS_F2 + ((hr * 16 + i) * 2 + (qt >> 3)) * 128, tag);
            u32x4 pl[8];
#pragma unroll
            for (int p = 0; p < 4; ++p) {
                const unsigned vo = (pg * 4 + p) * 8192 + (16 * (qt & 3) + i) * 16, so = WS_KVP + hr * 131072 + (qt >> 2) * 2048;
                pl[2 * p] = ld16(rws, vo, so); pl[2 * p + 1] = ld16(rws, vo + 1024, so);
            }
            SBAR();
            f32x4 s0 = {}, s1 = {};
#pragma unroll
            for (int p = 0; p < 4; ++p) { s0 += __builtin_bit_cast(f32x4, pl[2 * p]); s1 += __builtin_bit_cast(f32x4, pl[2 * p + 1]); }
            float v[8] = {s0[0], s0[1], s0[2], s0[3], s1[0], s1[1], s1[2], s1[3]};
#pragma unroll
            for (int k = 0; k < 8; ++k) {
                auto t = __builtin_amdgcn_permlane16_swap(__float_as_uint(v[k]), __float_as_uint(v[k]), false, false);
                v[k] = swap_sum(__uint_as_float(t[0]) + __uint_as_float(t[1]));
            }
            u32x4 o = {pk2(v[0], v[1]), pk2(v[2], v[3]), pk2(v[4], v[5]), pk2(v[6], v[7])};
            const int cl = 16 * (qt & 3) + i;
            if ((cl & 31) == 29) o = (u32x4){0x3F803F80u, 0x3F803F80u, 0x3F803F80u, 0x3F803F80u};
            if (((qt >> 2) & 1) && cl >= 32) { o[2] = 0u; o[3] = 0u; }
            st16(rws, o, lane < 16 ? i * 16 : 0x7FFFFF00u, WS_KVR + (hr * 256 + qt * 16) * 16);
            post(rws, WS_F3 + (hr * 16 + qt) * 128, tag, lane);
        }
        STAMP(6);
        if (r < 28) {
            const rsrc_t rwoa = mk_rsrc(ka[4], ka[5], 448 * 896);
            waitc<HSLP>(C_WOFREE, 4);
#pragma unroll
            for (int i = 0; i < 14; ++i) { WAIT_VM(WOWIN); dma(rwoa, wovo, ct * 128 + (w + 4 * i) * 8 * 896, L_R0 + (w + 4 * i) * 1024); }
            WAIT_VM(0); sig(C_WO0, lane);
        }
        if (isb) {
            const int hb_ = 2 * nt + (w >> 1), tb = mt * 4 + 2 * (w & 1);
            poll<HSLP>(rws, WS_F3 + (hb_ * 16 + (lane & 15)) * 128, tag);
            u32x4 kf[4];
#pragma unroll
            for (int f = 0; f < 4; ++f) kf[f] = ld16(rws, lane * 16 + f * 1024, WS_KVR + hb_ * 4096);
            SBAR();
#pragma unroll
            for (int t = 0; t < 2; ++t) {
                f32x16 np = MFMA(asbf(kf[0]), asbf(pe[t][0]), f32x16{}); np = MFMA(asbf(kf[1]), asbf(pe[t][1]), np);
                f32x16 nm = MFMA(asbf(kf[2]), asbf(pe[t][2]), f32x16{}); nm = MFMA(asbf(kf[3]), asbf(pe[t][3]), nm);
                const float rp = __builtin_amdgcn_rcpf(np[13]), rm = __builtin_amdgcn_rcpf(nm[13]);
                const float rd = __builtin_amdgcn_rcpf(__builtin_fmaf(rp, np[12], rm * nm[12]) + 1e-6f);
                const float cp = from_hi(rp * rd), cm = from_hi(rm * rd);
                f32x16 o;
#pragma unroll
                for (int i = 0; i < 16; ++i) o[i] = __builtin_fmaf(cp, np[i], cm * nm[i]);
                u32x4 o0 = pk8(o, 0), o1 = pk8(o, 1);
                if (hi) { o1[2] = 0; o1[3] = 0; }
                const unsigned so = WS_O + (2u << 20) + (((tb + t) * 16 + hb_) * 2) * 1024;
                st16(rws, o0, lane * 16, so); st16(rws, o1, lane * 16 + 1024, so);
            }
            WAIT_VM(0);
            if (lane < 2) __builtin_amdgcn_raw_buffer_store_b64(tag, rws, WS_FO + (1024 + (tb + lane) * 16 + hb_) * 128, 0, 16);
            CBAR();
        }
        STAMP(7);
        if (b == 255 && w == 0) {
#pragma unroll 1
            for (int i = 0; i < 4; ++i) poll<HSLP>(rws, WS_F3 + (i * 64 + lane) * 128, tag);
            if (lane == 0) __builtin_amdgcn_raw_buffer_store_b64(tag, rws, WS_EPOCH, 0, 16);
        }
    } else {
        const int tq = qt * 4 + w;
        const int gq = (head * 43) >> 7, gk = ((16 + head) * 43) >> 7, gv = ((32 + head) * 43) >> 7;
        const int rk0 = qt >> 2;
        const unsigned fall = WS_F1 + (((lane & 8) ? gv : gk) * 32 + 8 * w + (lane & 7)) * 128;
        __builtin_amdgcn_s_setprio(3);
        poll<1>(rws, WS_F1 + (lane >= 4 ? gq * 32 + (tq >> 1) : ((lane & 2) ? gv : gk) * 32 + 8 * w + 2 * rk0 + (lane & 1)) * 128, tag);
        STAMP(5);
        const unsigned hb = WS_QKV + head * 64 * 6144, kvb = hb + 2048;
        const unsigned kb0 = kvb + (16 * w + (qt & 3)) * 6144, kb1 = kvb + (16 * w + ((qt + 1) & 3)) * 6144,
                       kb2 = kvb + (16 * w + ((qt + 2) & 3)) * 6144, kb3 = kvb + (16 * w + ((qt + 3) & 3)) * 6144;
        unsigned vc = lane * 16 + rk0 * 24576;
        u32x4 kA[4], kB[4], kC[4], kD[4];
#define LD(d, f, v, sb) d[f] = ld16(rws, (v) + (f) * 1024, sb)
        u32x4 q0 = ld16(rws, lane * 16, hb + tq * 6144), q1 = ld16(rws, lane * 16 + 1024, hb + tq * 6144);
        LD(kA, 0, vc, kb0); LD(kA, 1, vc, kb0);
        SBAR();
        f32x16 O[4] = {};
        u32x4 pA0 = {}, pA1 = {}, pB0, pB1;
        kD[2] = (u32x4){}; kD[3] = (u32x4){};
        PIN(O[0]); PIN(O[1]); PIN(O[2]); PIN(O[3]); PIN(pA0); PIN(pA1); PIN(kD[2]); PIN(kD[3]);
        SBAR();
        bf16x8 Q[4][2];
        {
            f32x16 s = MFMA(asbf(kA[0]), asbf(q0), f32x16{}); s = MFMA(asbf(kA[1]), asbf(q1), s);
            SBAR(); FSTAMP(13);
            LD(kA, 2, vc, kb0); LD(kA, 3, vc, kb0); LD(kB, 0, vc, kb1); LD(kB, 1, vc, kb1); LD(kB, 2, vc, kb1); LD(kB, 3, vc, kb1);
            LD(kC, 0, vc, kb2); LD(kC, 1, vc, kb2); LD(kC, 2, vc, kb2); LD(kC, 3, vc, kb2);
            SBAR();
            lds_wr128(L_QX + (w * 2) * 1024 + lane * 16, q0);
            const float m = swap_max(max16(s)) + REF_SHIFT;
            u32x4 q1m = q1;
            if (hi) q1m[2] = pk2(-m, 0.f);
            lds_wr128(L_QX + (w * 2 + 1) * 1024 + lane * 16, q1m);
            WAIT_LGKM0(); sig(C_QX, lane); FSTAMP(14); waitc<0>(C_QX, 4);
#pragma unroll
            for (int a = 0; a < 4; ++a) { Q[a][0] = asbf(lds_rd128(L_QX + (a * 2) * 1024 + lane * 16)); Q[a][1] = asbf(lds_rd128(L_QX + (a * 2 + 1) * 1024 + lane * 16)); }
        }
        const u32x2 fl = ldflag(rws, fall);
        STAMP(6); STAMPC(20);
        __builtin_amdgcn_s_setprio(0);
        f32x16 sA, sB;
        sA = MFMA(asbf(kA[0]), Q[0][0], f32x16{}); sA = MFMA(asbf(kA[1]), Q[0][1], sA);
        PIN(Q[1][0]); PIN(Q[1][1]); PIN(Q[2][0]); PIN(Q[2][1]); PIN(Q[3][0]); PIN(Q[3][1]);
#define EX4(S, i) S[i] = __builtin_amdgcn_exp2f(S[i]); S[i + 1] = __builtin_amdgcn_exp2f(S[i + 1]); S[i + 2] = __builtin_amdgcn_exp2f(S[i + 2]); S[i + 3] = __builtin_amdgcn_exp2f(S[i + 3])
#define STAGE(SC, SN, PC0, PC1, PP0, PP1, KN, an, VP, ap, F0, F1, F2, F3) do { \
        SN = MFMA(asbf(KN[0]), Q[an][0], f32x16{}); SBAR(); F0; SBAR(); EX4(SC, 0); PIN(SC); SBAR(); \
        SN = MFMA(asbf(KN[1]), Q[an][1], SN); SBAR(); F1; SBAR(); EX4(SC, 4); PC0[0] = pk2(SC[0], SC[1]); PC0[1] = pk2(SC[2], SC[3]); PIN(SC); PIN(PC0); SBAR(); \
        O[ap] = MFMA(asbf(VP[2]), asbf(PP0), O[ap]); SBAR(); F2; SBAR(); EX4(SC, 8); PC0[2] = pk2(SC[4], SC[5]); PC0[3] = pk2(SC[6], SC[7]); PIN(SC); PIN(PC0); SBAR(); \
        O[ap] = MFMA(asbf(VP[3]), asbf(PP1), O[ap]); SBAR(); F3; SBAR(); EX4(SC, 12); PC1[0] = pk2(SC[8], SC[9]); PC1[1] = pk2(SC[10], SC[11]); \
        PC1[2] = pk2(SC[12], SC[13]); PC1[3] = pk2(SC[14], SC[15]); PIN(PC1); SBAR(); } while (0)
#define NOP (void)0
#define ARRIVE(K) asm volatile("" : "+v"(K[0]), "+v"(K[1]), "+v"(K[2]), "+v"(K[3]))
#define TILE(KP, KC, KN, V, SB, G3) do { \
        STAGE(sA, sB, pB0, pB1, pA0, pA1, KC, 1, KP, 3, NOP, NOP, NOP, NOP); \
        STAGE(sB, sA, pA0, pA1, pB0, pB1, KC, 2, KC, 0, LD(KP, 0, V, SB), LD(KP, 1, V, SB), LD(KP, 2, V, SB), LD(KP, 3, V, SB)); \
        STAGE(sA, sB, pB0, pB1, pA0, pA1, KC, 3, KC, 1, NOP, NOP, NOP, ARRIVE(KN)); \
        STAGE(sB, sA, pA0, pA1, pB0, pB1, KN, 0, KC, 2, G3, NOP, NOP, NOP); } while (0)
#pragma unroll 1
        for (int it = 0; it < 4; ++it) {
            const unsigned vn = it < 3 ? lane * 16 + ((rk0 + it + 1) & 3) * 24576 : 0x7FFF0000u;
            TILE(kD, kA, kB, vc, kb3, NOP);
            if (it == 0) { CBAR(); if (!tagok(fl, tag)) poll<1>(rws, fall, tag); CBAR(); }
            TILE(kA, kB, kC, vn, kb0, NOP);
            TILE(kB, kC, kD, vn, kb1, NOP);
            TILE(kC, kD, kA, vn, kb2, NOP);
            vc = vn;
        }
        O[3] = MFMA(asbf(kD[2]), asbf(pA0), O[3]); O[3] = MFMA(asbf(kD[3]), asbf(pA1), O[3]);
        STAMP(7); STAMPC(21);
        f32x16 o;
        const unsigned cb = L_R2 + lane * 16;
#define SEND(a, sl) _Pragma("unroll") for (int u = 0; u < 4; ++u) lds_wrf4(cb + ((sl) * 4 + u) * 1024, (f32x4){O[a][4 * u], O[a][4 * u + 1], O[a][4 * u + 2], O[a][4 * u + 3]})
#define RECV(a) do { WAIT_LGKM0(); sig(C_CMB, lane); waitc<0>(C_CMB, 4); f32x4 pr[12]; \
            _Pragma("unroll") for (int k = 0; k < 12; ++k) pr[k] = lds_rdf4(cb + ((a) * 12 + k) * 1024); SBAR(); \
            _Pragma("unroll") for (int u = 0; u < 4; ++u) { const f32x4 t = (pr[u] + (f32x4){O[a][4 * u], O[a][4 * u + 1], O[a][4 * u + 2], O[a][4 * u + 3]}) + (pr[4 + u] + pr[8 + u]); \
                o[4 * u] = t[0]; o[4 * u + 1] = t[1]; o[4 * u + 2] = t[2]; o[4 * u + 3] = t[3]; } PIN(o); } while (0)
        if (w == 0) { SEND(1, 3); SEND(2, 6); SEND(3, 9); RECV(0); }
        else if (w == 1) { SEND(0, 0); SEND(2, 7); SEND(3, 10); RECV(1); }
        else if (w == 2) { SEND(0, 1); SEND(1, 4); SEND(3, 11); RECV(2); }
        else { SEND(0, 2); SEND(1, 5); SEND(2, 8); RECV(3); }
        float l = from_hi(o[12]);
        {
            f32x2 chk = {0.f, 0.f};
#pragma unroll
            for (int i = 0; i < 16; i += 2) chk = __builtin_elementwise_fma((f32x2){o[i], o[i + 1]}, (f32x2){0.f, 0.f}, chk);
            bool bad = !(chk[0] + chk[1] == 0.f) || !(l >= 1e-32f && l <= 1e30f);
            if (__any(bad)) {
                float mrun = -3.0e38f;
                o = f32x16{};
#pragma unroll 1
                for (int k2 = 0; k2 < 4; ++k2) {
                    poll(rws, WS_F1 + (((lane & 8) ? gv : gk) * 32 + 8 * k2 + (lane & 7)) * 128, tag);
#pragma unroll 1
                    for (int m2 = 0; m2 < 16; ++m2) {
                        const unsigned so = kvb + (k2 * 16 + m2) * 6144;
                        const u32x4 k0 = ld16(rws, lane * 16, so), k1 = ld16(rws, lane * 16 + 1024, so), v0 = ld16(rws, lane * 16 + 2048, so), v1 = ld16(rws, lane * 16 + 3072, so);
                        f32x16 s = MFMA(asbf(k0), asbf(q0), f32x16{}); s = MFMA(asbf(k1), asbf(q1), s);
                        const float mn = __builtin_fmaxf(mrun, swap_max(max16(s))), al = __builtin_amdgcn_exp2f(mrun - mn);
                        mrun = mn;
#pragma unroll
                        for (int i = 0; i < 16; ++i) { s[i] = __builtin_amdgcn_exp2f(s[i] - mn); o[i] *= al; }
                        o = MFMA(asbf(v0), asbf(pk8(s, 0)), o); o = MFMA(asbf(v1), asbf(pk8(s, 1)), o);
                    }
                }
                l = from_hi(o[12]);
            }
        }
        const float rl = __builtin_amdgcn_rcpf(l);
#pragma unroll
        for (int i = 0; i < 16; ++i) o[i] *= rl;
        u32x4 o0 = pk8(o, 0), o1 = pk8(o, 1);
        if (hi) { o1[2] = 0; o1[3] = 0; }
        const unsigned so = WS_O + ((tq * 16 + head) * 2) * 1024;
        st16(rws, o0, lane * 16, so); st16(rws, o1, lane * 16 + 1024, so);
        sig(C_ATT, lane);
        STAMP(8); FSTAMPC(27);
        fl0 = ldflag(rws, WS_FO + ((rt * 2 + (w & 1)) * 16 + (w >> 1) * 8 + ((lane + ct) & 7)) * 128);
        SBAR();
        if (r < 28) { waitc<0>(C_WO0, 4); read_wo(wf, w >> 1, ct, lane); }
        post(rws, WS_FO + (tq * 16 + head) * 128, tag, lane);
        FSTAMP(9);
    }

    if (r < 28) {
        const int br = role, tp = w & 1, kh = w >> 1, tto = rt * 2 + tp;
        const rsrc_t rout = mk_rsrc(ka[14], ka[15], 2048 * 1792);
        const unsigned ffo = WS_FO + (br * 1024 + tto * 16 + kh * 8 + ((lane + ct) & 7)) * 128;
        const unsigned ob = WS_O + br * (2u << 20) + (tto * 16 + kh * 8) * 2048;
        u32x4 of[16];
        f32x16 c0 = {}, c1 = {};
        if (role) {
            poll<HSLP>(rws, ffo, tag);
#pragma unroll
            for (int k = 0; k < 16; ++k) {
                if ((k & 3) == 0 && k) WAIT_VM(0);
                of[k] = ld16(rws, lane * 16 + (k & 1) * 1024, ob + (((k >> 1) + ct) & 7) * 2048); SBAR();
            }
            waitc<4>(C_ATT, 4);
            __builtin_amdgcn_s_setprio(0);
#pragma unroll
            for (int k = 0; k < 16; ++k) { c0 = MFMA(wf[k][0], asbf(of[k]), c0); c1 = MFMA(wf[k][1], asbf(of[k]), c1); }
        } else {
            unsigned req = 0, fin = 0;
#pragma unroll
            for (int k = 0; k < 16; ++k) asm volatile("" : "=v"(of[k]));
#pragma clang loop unroll(disable)
            for (unsigned it = 0; it < SPIN_CAP && fin != 255u; ++it) {
                unsigned rdy = 255u;
                if (req != 255u) {
                    const u32x2 f = it ? ldflag(rws, ffo) : fl0;
                    rdy = (unsigned)__builtin_amdgcn_ballot_w64(__builtin_bit_cast(unsigned long long, f) == __builtin_bit_cast(unsigned long long, tag)) & 255u;
                }
                CBAR();
                const unsigned todo = (req ^ (req + 1u)) >> 1 & ~fin, nw = rdy & ~req;
#pragma unroll
                for (int k = 0; k < 8; ++k)
                    if (nw & (1u << k)) { const unsigned sb = ob + ((k + ct) & 7) * 2048; of[2 * k] = ld16(rws, lane * 16, sb); of[2 * k + 1] = ld16(rws, lane * 16 + 1024, sb); CBAR(); }
#pragma unroll
                for (int k = 0; k < 8; ++k)
                    if (todo & (1u << k)) {
                        c0 = MFMA(wf[2 * k][0], asbf(of[2 * k]), c0); c1 = MFMA(wf[2 * k][1], asbf(of[2 * k]), c1);
                        c0 = MFMA(wf[2 * k + 1][0], asbf(of[2 * k + 1]), c0); c1 = MFMA(wf[2 * k + 1][1], asbf(of[2 * k + 1]), c1); CBAR();
                    }
                fin |= todo; req |= nw;
            }
            sig(C_GATH, lane);
        }
        STAMP(10);
        const unsigned xb_ = L_R1 + br * 16384;
        const unsigned vo = (tto * 32 + (lane & 31)) * 1792 + (hi ? 16 : 0), so = br * 896 + ct * 128 + kh * 64;
#define FINISH(KEEP, GIVE) do { \
            _Pragma("unroll") for (int u = 0; u < 4; ++u) lds_wrf4(xb_ + (w * 4 + u) * 1024 + lane * 16, (f32x4){GIVE[4 * u], GIVE[4 * u + 1], GIVE[4 * u + 2], GIVE[4 * u + 3]}); \
            WAIT_LGKM0(); sig(C_EX0 + br * 2 + tp, lane); waitc<0>(C_EX0 + br * 2 + tp, 2); \
            _Pragma("unroll") for (int u = 0; u < 4; ++u) { const f32x4 s = lds_rdf4(xb_ + ((w ^ 2) * 4 + u) * 1024 + lane * 16); \
                _Pragma("unroll") for (int i = 0; i < 4; ++i) KEEP[4 * u + i] += s[i]; } \
            if (role) waitc<1>(C_GATH, 4); \
            _Pragma("unroll") for (int gp = 0; gp < 2; ++gp) { \
                auto s0 = __builtin_amdgcn_permlane32_swap(pk2(KEEP[8 * gp], KEEP[8 * gp + 1]), pk2(KEEP[8 * gp + 4], KEEP[8 * gp + 5]), false, false); \
                auto s1 = __builtin_amdgcn_permlane32_swap(pk2(KEEP[8 * gp + 2], KEEP[8 * gp + 3]), pk2(KEEP[8 * gp + 6], KEEP[8 * gp + 7]), false, false); \
                st16a<0>(rout, (u32x4){s0[0], s1[0], s0[1], s1[1]}, vo + gp * 32, so); } } while (0)
        if (kh) FINISH(c1, c0); else FINISH(c0, c1);
        STAMP(12);
    }
    STAMP_FLUSH();
    if (wid == 0 * 4) { const unsigned d_ = ((unsigned)__builtin_amdgcn_readlane(stv, 12) - (unsigned)__builtin_amdgcn_readlane(stv, 0)) / 1;
        unsigned long long r0_ = __builtin_amdgcn_s_memrealtime(), dur_ = (d_ > 20000u ? 20000u : d_) * 0ull;
        if (__builtin_amdgcn_readlane(stv, 12) != 0) while (__builtin_amdgcn_s_memrealtime() - r0_ < dur_) __builtin_amdgcn_s_sleep(1); }
    asm volatile(".Lpf_end:");
}

extern "C" void kernel_launch(void* const* d_in, const int* in_sizes, int n_in, void* d_out, int out_size, void* d_ws, size_t ws_size,
                              hipStream_t stream) {
    (void)in_sizes; (void)n_in; (void)out_size; (void)ws_size;
    (void)hipFuncSetAttribute((const void*)fused_fwd, hipFuncAttributeMaxDynamicSharedMemorySize, LDS_BYTES);
    fused_fwd<<<256, 512, LDS_BYTES, stream>>>(d_in[0], d_in[1], d_in[2], d_in[3], d_in[4], d_in[5], d_in[6], d_out, d_ws);
}
```

```cpp
#define STAMPS 1
#include <hip/hip_runtime.h>
#include <hip/hip_bf16.h>
#include <stdint.h>
#include <type_traits>

typedef __attribute__((ext_vector_type(8))) __bf16 bf16x8;
typedef __attribute__((ext_vector_type(2))) __bf16 bf16x2;
typedef __attribute__((ext_vector_type(2))) float f32x2;
typedef __attribute__((ext_vector_type(4))) float f32x4;
typedef __attribute__((ext_vector_type(16))) float f32x16;
typedef __attribute__((ext_vector_type(4))) unsigned u32x4;
typedef __attribute__((ext_vector_type(2))) unsigned u32x2;
typedef __attribute__((ext_vector_type(4))) short s16x4;
typedef __amdgpu_buffer_rsrc_t rsrc_t;
#define LAS __attribute__((address_space(3)))
#define DI __device__ __forceinline__

constexpr unsigned WS_EPOCH = 0;
constexpr unsigned WS_F1 = 4096;
constexpr unsigned WS_F2 = WS_F1 + 1024 * 128;
constexpr unsigned WS_F3 = WS_F2 + 512 * 128;
constexpr unsigned WS_FO = WS_F3 + 256 * 128;
constexpr unsigned WS_QKV = 1u << 20;
constexpr unsigned WS_KVP = WS_QKV + (6u << 20);
constexpr unsigned WS_KVR = WS_KVP + (2u << 20);
constexpr unsigned WS_O = WS_KVR + (1u << 20);
constexpr unsigned WS_DBG = WS_O + (4u << 20);
constexpr unsigned WS_BYTES = WS_DBG + (1u << 20);

constexpr unsigned SLOT = 37888;
constexpr unsigned L_R0 = 0;
constexpr unsigned L_R1 = 57856;
constexpr unsigned L_R2 = L_R1 + 32768;
constexpr unsigned L_QX = L_R2 + 49152;
constexpr unsigned L_QB = L_R2;
constexpr unsigned L_PHI = 4 * SLOT + 64;
constexpr unsigned L_JUNK = L_PHI + 4096;
constexpr unsigned L_CNT = L_JUNK + 1024;
constexpr unsigned L_TAG = L_CNT + 256;
constexpr unsigned LDS_BYTES = L_TAG + 16;
enum { C_LAND0 = 24, C_DONE0 = 32, C_QB = 4, C_QX = 10, C_PV = 11, C_KVDONE = 12, C_WO0 = 13, C_WOFREE = 15, C_EX0 = 16, C_CMB = 20, C_ATT = 21, C_GATH = 22 };

#define MAP_SEP 0
#define PNX 3
#define PNW 3
#define FASTG 2
#define ROT 1
#define LATE1 0
#define HSLP 16
#define WOWIN 2
constexpr unsigned SPIN_CAP = 1u << 16;
constexpr float QSCALE = 0.18898223650461362f * 1.4426950408889634f;
constexpr float LOG2E = 1.4426950408889634f;
constexpr float REF_SHIFT = 100.f;

#define MFMA(a, b, c) __builtin_amdgcn_mfma_f32_32x32x16_bf16((a), (b), (c), 0, 0, 0)
#define CBAR() asm volatile("" ::: "memory")
#define WAIT_VM(n) asm volatile("s_waitcnt vmcnt(%0)" :: "i"(n) : "memory")
#define WAIT_LGKM0() asm volatile("s_waitcnt lgkmcnt(0)" ::: "memory")
#define SBAR() __builtin_amdgcn_sched_barrier(0)

#define STAMP_DECL unsigned stv = 0
#define STAMP(i) do { unsigned long long t_; asm volatile("s_memrealtime %0\n\ts_waitcnt lgkmcnt(0)" : "=s"(t_) :: "memory"); \
    asm volatile("v_writelane_b32 %0, %1, " #i : "+v"(stv) : "s"((unsigned)t_)); } while (0)
#define STAMPC(i) do { unsigned long long t_; asm volatile("s_memtime %0\n\ts_waitcnt lgkmcnt(0)" : "=s"(t_) :: "memory"); \
    asm volatile("v_writelane_b32 %0, %1, " #i : "+v"(stv) : "s"((unsigned)t_)); } while (0)
#define STAMP_FLUSH() do { STAMPC(31); STAMP(29); if (lane < 32) __builtin_amdgcn_raw_buffer_store_b32(stv, rws, WS_DBG + ((blockIdx.x * 8 + wid) * 32 + lane) * 4, 0, 0); } while (0)
#define FSTAMP(i) do {} while (0)
#define FSTAMPC(i) do {} while (0)
#define PIN(x) asm volatile("" : "+v"(x))

DI unsigned pk2(float a, float b) { f32x2 v = {a, b}; return __builtin_bit_cast(unsigned, __builtin_convertvector(v, bf16x2)); }
DI u32x4 pk8(const f32x16& a, int s) {
    return (u32x4){pk2(a[8 * s], a[8 * s + 1]), pk2(a[8 * s + 2], a[8 * s + 3]), pk2(a[8 * s + 4], a[8 * s + 5]), pk2(a[8 * s + 6], a[8 * s + 7])}; }
DI bf16x8 asbf(u32x4 v) { return __builtin_bit_cast(bf16x8, v); }
DI float swap_max(float m) { auto r = __builtin_amdgcn_permlane32_swap(__float_as_uint(m), __float_as_uint(m), false, false);
    return __builtin_fmaxf(__uint_as_float(r[0]), __uint_as_float(r[1])); }
DI float swap_min(float m) { auto r = __builtin_amdgcn_permlane32_swap(__float_as_uint(m), __float_as_uint(m), false, false);
    return __builtin_fminf(__uint_as_float(r[0]), __uint_as_float(r[1])); }
DI float swap_sum(float m) { auto r = __builtin_amdgcn_permlane32_swap(__float_as_uint(m), __float_as_uint(m), false, false);
    return __uint_as_float(r[0]) + __uint_as_float(r[1]); }
DI float from_hi(float m) { auto r = __builtin_amdgcn_permlane32_swap(__float_as_uint(m), __float_as_uint(m), false, false);
    return __uint_as_float(r[1]); }
DI float max16(const f32x16& s) {
    float a = __builtin_fmaxf(__builtin_fmaxf(s[0], s[1]), s[2]), b = __builtin_fmaxf(__builtin_fmaxf(s[3], s[4]), s[5]);
    a = __builtin_fmaxf(__builtin_fmaxf(a, s[6]), s[7]); b = __builtin_fmaxf(__builtin_fmaxf(b, s[8]), s[9]);
    a = __builtin_fmaxf(__builtin_fmaxf(a, s[10]), s[11]); b = __builtin_fmaxf(__builtin_fmaxf(b, s[12]), s[13]);
    a = __builtin_fmaxf(__builtin_fmaxf(a, s[14]), s[15]); return __builtin_fmaxf(a, b); }
DI float min16(const f32x16& s) {
    float a = __builtin_fminf(__builtin_fminf(s[0], s[1]), s[2]), b = __builtin_fminf(__builtin_fminf(s[3], s[4]), s[5]);
    a = __builtin_fminf(__builtin_fminf(a, s[6]), s[7]); b = __builtin_fminf(__builtin_fminf(b, s[8]), s[9]);
    a = __builtin_fminf(__builtin_fminf(a, s[10]), s[11]); b = __builtin_fminf(__builtin_fminf(b, s[12]), s[13]);
    a = __builtin_fminf(__builtin_fminf(a, s[14]), s[15]); return __builtin_fminf(a, b); }

DI void dma(rsrc_t rs, unsigned voff, unsigned soff, unsigned lds) {
    asm volatile("s_mov_b32 m0, %0\n\ts_nop 0\n\tbuffer_load_dwordx4 %1, %2, %3 offen lds" :: "s"(lds), "v"(voff), "s"(rs), "s"(soff) : "memory"); }
DI u32x4 lds_rd128(unsigned a) { return *(LAS const u32x4*)(uintptr_t)a; }
DI f32x4 lds_rdf4(unsigned a) { return *(LAS const f32x4*)(uintptr_t)a; }
DI void lds_wr128(unsigned a, u32x4 v) { *(LAS u32x4*)(uintptr_t)a = v; }
DI void lds_wrf4(unsigned a, f32x4 v) { *(LAS f32x4*)(uintptr_t)a = v; }
DI void lds_wr64(unsigned a, u32x2 v) { *(LAS u32x2*)(uintptr_t)a = v; }
DI bf16x8 lds_tr2(unsigned a0, unsigned a1) {
    s16x4 lo = __builtin_amdgcn_ds_read_tr16_b64_v4i16((LAS s16x4*)(uintptr_t)a0), hi = __builtin_amdgcn_ds_read_tr16_b64_v4i16((LAS s16x4*)(uintptr_t)a1);
    return __builtin_bit_cast(bf16x8, __builtin_shufflevector(lo, hi, 0, 1, 2, 3, 4, 5, 6, 7)); }
DI void sig(int c, int) {
    asm volatile("s_mov_b64 exec, 1\n\tds_add_u32 %0, %1\n\ts_mov_b64 exec, -1" :: "v"(L_CNT + 4 * c), "v"(1u) : "memory"); }
template <int SLP = 1> DI void waitc(int c, unsigned n) {
    unsigned v, sv, it;
    asm volatile("s_mov_b32 %2, %5\n"
                 "1:\n\tds_read_b32 %0, %3\n\ts_waitcnt lgkmcnt(0)\n\tv_readfirstlane_b32 %1, %0\n\ts_cmp_ge_u32 %1, %4\n\ts_cbranch_scc1 2f\n\t"
                 "s_sleep %6\n\ts_sub_u32 %2, %2, 1\n\ts_cbranch_scc0 1b\n2:"
                 : "=&v"(v), "=&s"(sv), "=&s"(it) : "v"(L_CNT + 4 * c), "i"(n), "i"(SPIN_CAP), "i"(SLP) : "memory", "scc"); }
DI unsigned rd_cnt(int c) { return *(volatile LAS const unsigned*)(uintptr_t)(L_CNT + 4 * c); }
DI u32x2 rd_tag() { const u32x2 t = *(LAS const u32x2*)(uintptr_t)L_TAG;
    return (u32x2){(unsigned)__builtin_amdgcn_readfirstlane(t[0]), (unsigned)__builtin_amdgcn_readfirstlane(t[1])}; }
DI u32x4 ld16(rsrc_t rs, unsigned voff, unsigned soff) { return __builtin_amdgcn_raw_buffer_load_b128(rs, voff, soff, 16); }
template <int AUX> DI void st16a(rsrc_t rs, u32x4 v, unsigned voff, unsigned soff) {
    __builtin_amdgcn_raw_buffer_store_b128(v, rs, voff, soff, AUX); asm volatile("s_nop 1" :: "v"(v) : "memory"); }
DI void st16(rsrc_t rs, u32x4 v, unsigned voff, unsigned soff) { st16a<16>(rs, v, voff, soff); }
DI u32x2 ldflag(rsrc_t rs, unsigned voff) { return __builtin_amdgcn_raw_buffer_load_b64(rs, voff, 0, 16); }
DI bool tagok(u32x2 f, u32x2 tag) {
    return __builtin_amdgcn_ballot_w64(__builtin_bit_cast(unsigned long long, f) == __builtin_bit_cast(unsigned long long, tag)) == ~0ull; }
template <int SLP = 2> DI void poll(rsrc_t rs, unsigned voff, u32x2 tag) {
    CBAR();
#pragma clang loop unroll(disable)
    for (unsigned it = 0; it < SPIN_CAP; ++it) { u32x2 f = ldflag(rs, voff); if (tagok(f, tag)) break; __builtin_amdgcn_s_sleep(SLP); CBAR(); }
    CBAR(); }
DI void post(rsrc_t rs, unsigned off, u32x2 tag, int lane) {
    WAIT_VM(0);
    if (lane == 0) __builtin_amdgcn_raw_buffer_store_b64(tag, rs, off, 0, 16);
    CBAR(); }

#define GB(n, k) "v[176+20*" #n "+4*" #k ":179+20*" #n "+4*" #k "]"
#define GT(A, n, t, i) "v_mfma_f32_32x32x16_bf16 %[" #A "], " GB(n, (2+i)) ", " GB(n, t) ", %[" #A "]\n\t"
#define GD(A, n, t, i) "v_mfma_f32_32x32x16_bf16 %[" #A "], " GB(n, t) ", " GB(n, (2+i)) ", %[" #A "]\n\t"
#define GX(n, t, X) "ds_read_b128 " GB(n, t) ", " X " offset:4096*" #t "\n\t"
#define GF(n, i, W, O) "ds_read_b64_tr_b16 v[176+20*" #n "+8+4*" #i ":177+20*" #n "+8+4*" #i "], " W " offset:" O "\n\t" \
                       "ds_read_b64_tr_b16 v[178+20*" #n "+8+4*" #i ":179+20*" #n "+8+4*" #i "], " W " offset:" O "+336\n\t"
#define GSTEP(M0, M1, M2, c, n, X, O, P0, P0B, P2, P4, P5) \
    M0(a0, c, 0, 0) P0 GX(n, 0, X) P0B M0(a3, c, 1, 0) GF(n, 0, "%[wa]", O) M1(a1, c, 0, 1) GX(n, 1, X) P2 M1(a4, c, 1, 1) GF(n, 1, "%[wb]", O) \
    M2(a2, c, 0, 2) GF(n, 2, "%[wc]", O) P4 M2(a5, c, 1, 2) P5
#define GLOOP(M0, M1, M2) \
    GX(0, 0, "%[xa]") GF(0, 0, "%[wa]", "0") GX(0, 1, "%[xa]") GF(0, 1, "%[wb]", "0") GF(0, 2, "%[wc]", "0") \
    GX(1, 0, "v170") GF(1, 0, "%[wa]", "5376") GX(1, 1, "v170") GF(1, 1, "%[wb]", "5376") GF(1, 2, "%[wc]", "5376") \
    "s_waitcnt lgkmcnt(8)\n" \
    "1:\n\t" \
    GSTEP(M0, M1, M2, 0, 2, "v171", "10752", "", "ds_read_b128 v[166:169], %[la]\n\t", "v_xor_b32 v172, 0x60, %[xa]\n\t", "s_and_b32 %[so], %[g], 3\n\t", \
          "s_mul_i32 %[so], %[so], %[slot]\n\ts_waitcnt lgkmcnt(9)\n\t") \
    GSTEP(M0, M1, M2, 1, 3, "v172", "16128", "", "v_add_u32 %[xa], %[so], %[xa0]\n\ts_waitcnt lgkmcnt(8)\n\t", "v_min3_u32 v173, v166, v167, v168\n\tv_xor_b32 v170, 32, %[xa]\n\t", \
          "v_min_u32 v173, v173, v169\n\tv_add_u32 %[wa], %[so], %[wa0]\n\t", "v_add_u32 %[wb], %[so], %[wb0]\n\tv_readfirstlane_b32 %[rf], v173\n\tv_add_u32 %[wc], %[so], %[wc0]\n\ts_waitcnt lgkmcnt(8)\n\t") \
    GSTEP(M0, M1, M2, 2, 0, "%[xa]", "0", "s_cmp_gt_u32 %[rf], %[g]\n\ts_cbranch_scc0 3f\n2:\n\t", "", "v_xor_b32 v171, 64, %[xa]\n\t", "", \
          "s_waitcnt lgkmcnt(8)\n\tv_mov_b32 v174, %[g]\n\t") \
    GSTEP(M0, M1, M2, 3, 1, "v170", "5376", "", "s_add_u32 %[g], %[g], 1\n\t", "s_cmp_lt_u32 %[g], 8\n\t", "", \
          "s_waitcnt lgkmcnt(8)\n\tds_write_b32 %[da], v174\n\ts_cbranch_scc1 1b\n\t") \
    "s_branch 4f\n" \
    "3:\n\ts_mov_b32 %[so], %[cap]\n" \
    "5:\n\tds_read_b128 v[166:169], %[la]\n\ts_waitcnt lgkmcnt(0)\n\tv_min3_u32 v173, v166, v167, v168\n\tv_min_u32 v173, v173, v169\n\ts_nop 0\n\tv_readfirstlane_b32 %[rf], v173\n\ts_cmp_gt_u32 %[rf], %[g]\n\ts_cbranch_scc1 2b\n\t" \
    "s_sub_u32 %[so], %[so], 1\n\ts_cbranch_scc0 5b\n\ts_branch 2b\n" \
    "4:\n\ts_waitcnt lgkmcnt(0)\n\ts_nop 7\n\ts_nop 3"
template <int ND, class Start>
DI void gemm6(unsigned xa0, unsigned wa0, unsigned wb0, unsigned wc0, int w, f32x16 (&acc)[6], Start start) {
    unsigned xa = xa0, wa = wa0, wb = wb0, wc = wc0, g = 1, so, rf;
    const unsigned la = L_CNT + 4 * C_LAND0, da = L_CNT + 4 * (C_DONE0 + w);
#pragma unroll
    for (int i = 0; i < 6; ++i) { acc[i] = f32x16{}; PIN(acc[i]); }
    PIN(xa); PIN(wa); PIN(wb); PIN(wc);
    start();
#define GASM(M0, M1, M2) asm volatile("v_xor_b32 v170, 32, %[xa]\n\tv_xor_b32 v171, 64, %[xa]\n\t" GLOOP(M0, M1, M2) \
        : [a0] "+v"(acc[0]), [a1] "+v"(acc[1]), [a2] "+v"(acc[2]), [a3] "+v"(acc[3]), [a4] "+v"(acc[4]), [a5] "+v"(acc[5]), \
          [xa] "+v"(xa), [wa] "+v"(wa), [wb] "+v"(wb), [wc] "+v"(wc), [g] "+s"(g), [so] "=&s"(so), [rf] "=&s"(rf) \
        : [xa0] "v"(xa0), [wa0] "v"(wa0), [wb0] "v"(wb0), [wc0] "v"(wc0), [la] "v"(la), [da] "v"(da), [slot] "i"(SLOT), [cap] "i"(SPIN_CAP) \
        : "memory", "scc", "v166", "v167", "v168", "v169", "v170", "v171", "v172", "v173", "v174", "v175", "v176", "v177", "v178", "v179", "v180", "v181", "v182", "v183", "v184", "v185", "v186", "v187", "v188", "v189", "v190", "v191", "v192", "v193", "v194", "v195", "v196", "v197", "v198", "v199", "v200", "v201", "v202", "v203", "v204", "v205", "v206", "v207", "v208", "v209", "v210", "v211", "v212", "v213", "v214", "v215", "v216", "v217", "v218", "v219", "v220", "v221", "v222", "v223", "v224", "v225", "v226", "v227", "v228", "v229", "v230", "v231", "v232", "v233", "v234", "v235", "v236", "v237", "v238", "v239", "v240", "v241", "v242", "v243", "v244", "v245", "v246", "v247", "v248", "v249", "v250", "v251", "v252", "v253", "v254", "v255")
    if constexpr (ND == 0) GASM(GT, GT, GT); else if constexpr (ND == 1) GASM(GT, GT, GD); else GASM(GD, GD, GD);
}
DI unsigned min4(u32x4 d) { return __builtin_amdgcn_readfirstlane(min(min(d[0], d[1]), min(d[2], d[3]))); }
DI u32x4 rd4(int c) { return *(volatile LAS const u32x4*)(uintptr_t)(L_CNT + 4 * c); }
DI void wait4(int c, unsigned n) {
    CBAR();
#pragma clang loop unroll(disable)
    for (unsigned it = 0; it < SPIN_CAP; ++it) if (min4(rd4(c)) >= n) break;
    CBAR(); }
template <int P> DI void dma_x4(rsrc_t rs, unsigned voff, unsigned soff, unsigned lds) {
    asm volatile("s_mov_b32 m0, %1\n\ts_nop %4\n\tbuffer_load_dwordx4 %2, %3, %0 offen lds\n\t"
                 "s_add_u32 m0, m0, 0x1000\n\ts_add_u32 %0, %0, 28672\n\ts_nop %4\n\tbuffer_load_dwordx4 %2, %3, %0 offen lds\n\t"
                 "s_add_u32 m0, m0, 0x1000\n\ts_add_u32 %0, %0, 28672\n\ts_nop %4\n\tbuffer_load_dwordx4 %2, %3, %0 offen lds\n\t"
                 "s_add_u32 m0, m0, 0x1000\n\ts_add_u32 %0, %0, 28672\n\ts_nop %4\n\tbuffer_load_dwordx4 %2, %3, %0 offen lds"
                 : "+s"(soff) : "s"(lds), "v"(voff), "s"(rs), "i"(P) : "memory", "scc"); }
template <int P> DI void dma_w5(rsrc_t rs, const unsigned (&v)[6], unsigned soff, unsigned lds) {
    asm volatile("s_mov_b32 m0, %0\n\ts_nop %8\n\tbuffer_load_dwordx4 %3, %1, %2 offen lds\n\t"
                 "s_add_u32 m0, m0, 0x1000\n\ts_nop %8\n\tbuffer_load_dwordx4 %4, %1, %2 offen lds\n\t"
                 "s_add_u32 m0, m0, 0x1000\n\ts_nop %8\n\tbuffer_load_dwordx4 %5, %1, %2 offen lds\n\t"
                 "s_add_u32 m0, m0, 0x1000\n\ts_nop %8\n\tbuffer_load_dwordx4 %6, %1, %2 offen lds\n\t"
                 "s_add_u32 m0, m0, 0x1000\n\ts_nop %8\n\tbuffer_load_dwordx4 %7, %1, %2 offen lds"
                 :: "s"(lds), "s"(rs), "s"(soff), "v"(v[0]), "v"(v[1]), "v"(v[2]), "v"(v[3]), "v"(v[4]), "i"(P) : "memory", "scc"); }
DI unsigned tr_addr128(unsigned base, int cb, int rowgrp, int lane) {
    const int q = (lane & 15) >> 2, byte = cb + 32 * ((lane >> 4) & 1) + 8 * (lane & 3);
    return base + (rowgrp + q) * 128 + ((((byte >> 4) ^ ((q >> 1) << 2)) & 7) << 4) + (byte & 8);
}
DI void feat(const f32x16& p, f32x16& e, f32x16& g) {
    const float mx = swap_max(max16(p)) * LOG2E, mn = swap_min(min16(p)) * LOG2E;
#pragma unroll
    for (int i = 0; i < 16; i += 2) {
        const f32x2 pp = {p[i], p[i + 1]};
        const f32x2 a = __builtin_elementwise_fma(pp, (f32x2){LOG2E, LOG2E}, (f32x2){-mx, -mx}), c = __builtin_elementwise_fma(pp, (f32x2){-LOG2E, -LOG2E}, (f32x2){mn, mn});
        e[i] = __builtin_amdgcn_exp2f(a[0]); e[i + 1] = __builtin_amdgcn_exp2f(a[1]); g[i] = __builtin_amdgcn_exp2f(c[0]); g[i + 1] = __builtin_amdgcn_exp2f(c[1]);
    }
}

typedef __attribute__((ext_vector_type(16))) unsigned u32x16;
DI rsrc_t mk_rsrc(unsigned lo, unsigned hi, unsigned bytes) {
    return __builtin_amdgcn_make_buffer_rsrc((void*)(((unsigned long long)hi << 32) | lo), 0, bytes, 0x00020000); }
DI void read_wo(bf16x8 (&wf)[16][2], int kh, int rot, int lane) {
    const unsigned ta0 = tr_addr128(L_R0, 0, kh * 224 + 4 * (lane >> 5), lane), ta1 = tr_addr128(L_R0, 64, kh * 224 + 4 * (lane >> 5), lane);
#pragma unroll
    for (int k = 0; k < 8; ++k) {
        const unsigned ro = ((k + rot) & 7) * 28 * 128, b0 = ta0 + ro, b1 = ta1 + ro;
        wf[2 * k][0] = lds_tr2(b0, b0 + 1024); wf[2 * k][1] = lds_tr2(b1, b1 + 1024);
        wf[2 * k + 1][0] = lds_tr2(b0 + 2048, b0 + 3072); wf[2 * k + 1][1] = lds_tr2(b1 + 2048, b1 + 3072);
    }
}
DI void ones_row(u32x4& a, u32x4& b, u32x2 c) {
    u32x2 a0 = {a[0], a[1]}, a1 = {a[2], a[3]}, b0 = {b[0], b[1]}, b1 = {b[2], b[3]};
    asm volatile("s_mov_b64 exec, %4\n\tv_mov_b64 %0, %5\n\tv_mov_b64 %1, %5\n\tv_mov_b64 %2, %5\n\tv_mov_b64 %3, %5\n\ts_mov_b64 exec, -1"
                 : "+v"(a0), "+v"(a1), "+v"(b0), "+v"(b1) : "s"(0xF0000000F0000000ull), "v"(c));
    a = (u32x4){a0[0], a0[1], a1[0], a1[1]}; b = (u32x4){b0[0], b0[1], b1[0], b1[1]}; }
template <int ND>
DI void publish_a(f32x16 (&acc)[6], rsrc_t rws, const unsigned (&so)[3], int nq, int lane, bool hi, u32x2 oc) {
#pragma unroll
    for (int i = 0; i < 3; ++i) {
        if (i < 3 - ND && i < nq) {
            asm volatile("" ::: "memory");
#pragma unroll
            for (int k = 0; k < 16; ++k) { acc[i][k] *= QSCALE; acc[3 + i][k] *= QSCALE; }
            PIN(acc[i]); PIN(acc[3 + i]);
        }
        const unsigned pad = i < nq ? 0u : 0x00003F80u;
#pragma unroll
        for (int t = 0; t < 2; ++t) {
            u32x4 v0 = pk8(acc[3 * t + i], 0), v1 = pk8(acc[3 * t + i], 1);
            if (i >= 3 - ND) ones_row(v0, v1, oc); else { v1[2] = hi ? pad : v1[2]; v1[3] = hi ? 0u : v1[3]; }
            st16(rws, v0, lane * 16, so[i] + t * 6144); st16(rws, v1, lane * 16 + 1024, so[i] + t * 6144);
            SBAR();
        }
    }
}
DI void phi_w(bf16x8& f0, u32x4& f1, int hh, int lane, bool hi) {
    const unsigned pb = L_PHI + hh * 1568 + (4 * (lane >> 5) + ((lane & 15) >> 2)) * 56 + ((lane & 19) == 19 ? 0 : 32 * ((lane >> 4) & 1) + 8 * (lane & 3));
    f0 = lds_tr2(pb, pb + 8 * 56);
    f1 = __builtin_bit_cast(u32x4, lds_tr2(pb + 16 * 56, pb + 24 * 56));
    if (hi) { f1[2] = 0; f1[3] = 0; }
}
DI f32x16 phi_pre(u32x4 k0, u32x4 k1, bf16x8 f0, u32x4 f1) {
    f32x16 p = MFMA(f0, asbf(k0), f32x16{});
    return MFMA(asbf(f1), asbf(k1), p);
}

__global__ __launch_bounds__(512, 2) void fused_fwd(const void*, const void*, const void*, const void*, const void*, const void*, const void*, void*, void*) {
    STAMP_DECL; STAMP(0); STAMPC(30);
    u32x16 ka; u32x2 kb;
    asm volatile("s_load_dwordx16 %0, %2, 0x0\n\ts_load_dwordx2 %1, %2, 0x40" : "=&s"(ka), "=&s"(kb) : "s"(__builtin_amdgcn_kernarg_segment_ptr()));
    const int tid = threadIdx.x, lane = tid & 63, wid = __builtin_amdgcn_readfirstlane(tid >> 6);
    const int b = blockIdx.x, xcd = b & 7, r = b >> 3, w = wid & 3, role = wid >> 2;
    const int isb = r & 1, ml = (r >> 1) & 3, nl = r >> 3;
    const int mt = (xcd & 3) * 4 + ml, nt = (xcd >> 2) * 4 + nl;
    const bool hi = lane >= 32;
    int g0 = ROT == 0 ? 0 : ROT == 1 ? ((ml + nl) & 1) * 4 : ROT == 2 ? 2 * ml + nl : ROT == 3 ? (isb ? ((ml + nl) & 1) * 4 : 0) : ROT == 4 ? (isb ? 2 * ml + nl : ((ml + nl) & 1) * 4) : (isb ? 2 * ml + nl : 0); g0 -= (g0 >= 7) ? 7 : 0;
    const int head = 2 * xcd + (r >> 4), qt = r & 15;
    const int ct = r >> 2, rt = xcd * 4 + (r & 3);
    u32x2 tag;
    bf16x8 wf[16][2];
    u32x2 fl0 = {};
    unsigned pfd = 0;

    unsigned xvo, wvo[6];
    if (role) {
        __builtin_amdgcn_s_setprio(2);
        xvo = (w * 8 + (lane >> 3)) * 896 + ((((lane & 7) ^ (lane >> 4) ^ ((w & 1) << 2)) & 7) << 4);
        PIN(xvo);
#pragma unroll
        for (int i = 0; i < 6; ++i) {
            const int c = 64 * (w + 4 * i) + lane, rho = (c * 3121) >> 16, ch = c - 21 * rho, win = (ch * 37) >> 8;
            const int krow = (rho & ~15) | ((rho & 3) << 2) | ((rho >> 2) & 3);
            wvo[i] = krow * 2688 + ch * 16 + (isb ? win * (896 - 112) : 0);
            PIN(wvo[i]);
        }
    }
    if (wid == 0) {
        unsigned long long pc; unsigned len;
        asm volatile("s_getpc_b64 %0\n.Lpf_begin%=:\n\ts_mov_b32 %1, .Lpf_end-.Lpf_begin%=" : "=s"(pc), "=s"(len));
        const rsrc_t rc = __builtin_amdgcn_make_buffer_rsrc((void*)pc, 0, len, 0x00020000);
        asm volatile("s_nop 4\n\tbuffer_load_dword %0, %1, %2, 0 offen" : "+v"(pfd) : "v"(lane < 12 ? (r * 12 + lane) * 128 : 0x7FFFFFF0), "s"(rc) : "memory");
        *(LAS unsigned*)(uintptr_t)(L_CNT + 4 * lane) = 0u;
    }
    asm volatile("s_waitcnt lgkmcnt(0)" : "+s"(ka), "+s"(kb) :: "memory");
    const rsrc_t rws = mk_rsrc(kb[0], kb[1], WS_BYTES);
    u32x4 pe[2][4];
    if (role) {
        const rsrc_t rx = mk_rsrc(isb ? ka[6] : ka[0], isb ? ka[7] : ka[1], 2048 * 896), rw = mk_rsrc(isb ? ka[8] : ka[2], isb ? ka[9] : ka[3], 448 * 2688);
        const unsigned xs0 = mt * 114688, ws0 = (isb ? 112 : 336) * nt;
        auto feed = [&](int g, auto PX, auto PW) {
            int ga = g + g0; ga -= (ga >= 7) ? 7 : 0;
            const unsigned ws = ws0 + ga * 64 * 2688, l = (g & 3) * SLOT + w * 1024;
            dma_x4<decltype(PX)::value>(rx, xvo, xs0 + ga * 128, l);
            dma_w5<decltype(PW)::value>(rw, wvo, ws, l + 16384);
            if (w == 0) dma(rw, wvo[5], ws, l + 16384 + 5 * 4096); };
        const unsigned lw = L_CNT + 4 * (C_LAND0 + w);
        constexpr std::integral_constant<int, 0> fast{};
        STAMP(18);
        dma(mk_rsrc(ka[10], ka[11], 16 * 1568), lane * 16, nt * 3136 + w * 1024, L_PHI + w * 1024);
        feed(0, fast, fast);
        STAMP(17);
        if (LATE1 == 0) feed(1, fast, fast);
        STAMP(16);
        asm volatile("s_barrier" ::: "memory");
        if (LATE1 == 1) { WAIT_VM(4); feed(1, fast, fast); }
        if (LATE1 == 2) { WAIT_VM(0); *(volatile LAS unsigned*)(uintptr_t)lw = 1; feed(1, fast, fast); }
        if (w == 0) WAIT_VM(10); else WAIT_VM(9);
        *(volatile LAS unsigned*)(uintptr_t)lw = 1;
        STAMP(1);
#pragma unroll 1
        for (int g = 1; g < 6; ++g) {
            if (g >= 3) wait4(C_DONE0, g - 2);
            if (g < FASTG) feed(g + 1, fast, fast); else feed(g + 1, std::integral_constant<int, PNX>{}, std::integral_constant<int, PNW>{});
            if (w == 0) WAIT_VM(10); else WAIT_VM(9);
            *(volatile LAS unsigned*)(uintptr_t)lw = g + 1;
        }
        {
            const rsrc_t rt_ = mk_rsrc(w < 2 ? ka[4] : ka[12], w < 2 ? ka[5] : ka[13], 448 * 896);
            asm volatile("s_mov_b32 m0, %0\n\ts_nop 3\n\tbuffer_load_dword %1, %2, 0 offen lds"
                         :: "s"(L_JUNK), "v"(lane < 49 ? (r * 98 + (w & 1) * 49 + lane) * 128 : 0x7FFFFF00), "s"(rt_) : "memory");
        }
        WAIT_VM(1);
        *(volatile LAS unsigned*)(uintptr_t)lw = 8;
        STAMP(2);
        tag = rd_tag();
        if (isb) {
            bf16x8 ph0; u32x4 ph1;
            wait4(C_LAND0, 1);
            phi_w(ph0, ph1, w >> 1, lane, hi);
            const int tk = lane & 31, pi = (((tk >> 1) & 1) << 3) | (tk & 1) | (((tk >> 2) & 3) << 1);
            const unsigned ib = L_R1 + (w >> 1) * 16384 + ((w & 1) * 64 + tk) * 128 + ((pi ^ (lane >> 5)) << 3);
            const float lo = hi ? 0.f : 1.f;
            waitc<1>(C_QB + w, 1);
            f32x16 pp[2];
#pragma unroll
            for (int t = 0; t < 2; ++t) { const unsigned qa = L_QB + ((w * 2 + t) * 4 + 2) * 1024 + lane * 16; pp[t] = phi_pre(lds_rd128(qa), lds_rd128(qa + 1024), ph0, ph1); }
#pragma unroll
            for (int t = 0; t < 2; ++t) {
                f32x16 e, g;
                feat(pp[t], e, g);
                f32x2 s2 = {e[0], e[1]}, g2 = {g[0], g[1]};
#pragma unroll
                for (int i = 2; i < 12; i += 2) { s2 += (f32x2){e[i], e[i + 1]}; g2 += (f32x2){g[i], g[i + 1]}; }
                s2 = __builtin_elementwise_fma((f32x2){e[12], e[13]} + (f32x2){e[14], e[15]}, (f32x2){lo, lo}, s2);
                g2 = __builtin_elementwise_fma((f32x2){g[12], g[13]} + (f32x2){g[14], g[15]}, (f32x2){lo, lo}, g2);
                const float se = __builtin_amdgcn_rcpf(swap_sum(s2[0] + s2[1])), sg_ = __builtin_amdgcn_rcpf(swap_sum(g2[0] + g2[1]));
#pragma unroll
                for (int u = 0; u < 8; ++u) {
                    const int k = 4 * (u & 3);
                    const f32x16& z = (u >> 2) ? g : e; const float sc = (u >> 2) ? sg_ : se;
                    lds_wr64((ib ^ ((8 * (u >> 2) + 2 * (u & 3)) << 3)) + t * 4096, (u32x2){pk2(z[k] * sc, z[k + 1] * sc), pk2(z[k + 2] * sc, z[k + 3] * sc)});
                }
            }
            WAIT_LGKM0(); sig(C_PV, lane);
#pragma unroll
            for (int t = 0; t < 2; ++t) {
                const unsigned qa = L_QB + ((w * 2 + t) * 4) * 1024 + lane * 16;
                f32x16 p = phi_pre(lds_rd128(qa), lds_rd128(qa + 1024), ph0, ph1), e, g;
                feat(p, e, g);
                pe[t][0] = pk8(e, 0); pe[t][1] = pk8(e, 1); pe[t][2] = pk8(g, 0); pe[t][3] = pk8(g, 1);
            }
        }
        STAMP(3);
    } else {
        f32x16 acc[6];
        const int tp = w & 1, sg = w >> 1;
        const int rr = tp * 64 + (lane & 31);
        const unsigned xa0 = rr * 128 + ((((lane >> 5) ^ (rr >> 1)) & 7) << 4);
        const unsigned lb = 16384 + (4 * ((lane & 15) >> 2) + 2 * (lane >> 5)) * 336 + 32 * ((lane >> 4) & 1) + 8 * (lane & 3);
        const unsigned wst = isb ? 112 : 56, wa0 = lb + 56 * (isb ? sg : 3 * sg), wb0 = wa0 + wst, wc0 = wb0 + wst;
        const int G = 2 * nt + sg;
        if (w == 0) {
            const u32x2 ep = ldflag(rws, WS_EPOCH);
            const unsigned long long e = (((unsigned long long)ep[1] << 32) | ep[0]) + 1;
            lds_wr64(L_TAG, (u32x2){(unsigned)e, (unsigned)(e >> 32)});
            WAIT_VM(0);
            asm volatile("" :: "v"(pfd));
        }
        unsigned so[3];
#pragma unroll
        for (int i = 0; i < 3; ++i) { const int sl = 3 * G + i; so[i] = WS_QKV + (((sl & 15) * 64 + mt * 4 + 2 * tp) * 6 + (sl >> 4) * 2) * 1024; asm volatile("" : "+s"(so[i])); }
        const int nq = 16 - 3 * G;
        u32x2 oc; oc[0] = oc[1] = (lane & 31) == 28 ? 0x3F803F80u : 0u; PIN(oc);
        auto start = [&]() { asm volatile("s_waitcnt lgkmcnt(0)\n\ts_barrier" ::: "memory"); tag = rd_tag(); asm volatile("" : "+s"(tag)); wait4(C_LAND0, 1); STAMP(1); STAMPC(23); };
        if (isb || G == 10) { gemm6<1>(xa0, wa0, wb0, wc0, w, acc, start); STAMPC(24); STAMP(2); if (!isb) publish_a<1>(acc, rws, so, nq, lane, hi, oc); }
        else if (G < 10) { gemm6<0>(xa0, wa0, wb0, wc0, w, acc, start); STAMPC(24); STAMP(2); publish_a<0>(acc, rws, so, nq, lane, hi, oc); }
        else { gemm6<3>(xa0, wa0, wb0, wc0, w, acc, start); STAMPC(24); STAMP(2); publish_a<3>(acc, rws, so, nq, lane, hi, oc); }
        if (!isb) {
            FSTAMP(15);
            post(rws, WS_F1 + (G * 32 + mt * 2 + tp) * 128, tag, lane);
            wait4(C_DONE0, 7);
        } else {
            u32x4 fr[2][3][2];
#pragma unroll
            for (int t = 0; t < 2; ++t) {
#pragma unroll
                for (int i = 0; i < 3; ++i) { fr[t][i][0] = pk8(acc[3 * t + i], 0); fr[t][i][1] = pk8(acc[3 * t + i], 1); }
                ones_row(fr[t][2][0], fr[t][2][1], oc);
            }
            wait4(C_DONE0, 7);
#pragma unroll
            for (int t = 0; t < 2; ++t)
#pragma unroll
                for (int s = 0; s < 2; ++s) {
                    lds_wr128(L_QB + (((w * 2 + t) * 2 + 1) * 2 + s) * 1024 + lane * 16, fr[t][1][s]);
                    lds_wr128(L_QB + (((w * 2 + t) * 2) * 2 + s) * 1024 + lane * 16, fr[t][0][s]);
                    lds_wr128(L_R0 + ((sg * 4 + 2 * tp + t) * 2 + s) * 1024 + lane * 16, fr[t][2][s]);
                }
            WAIT_LGKM0(); sig(C_QB + w, lane);
        }
        STAMP(3);
    }
    FSTAMP(4);

    if (role) {
        const int wc = (lane & 7) ^ (((lane >> 4) & 1) << 2);
        const unsigned wovo = (lane >> 3) * 896 + wc * 16;
        if (isb) {
            waitc<0>(C_PV, 4);
            const int hh = w >> 1, ft = w & 1, q = (lane & 15) >> 2, P = 8 * ft + 4 * ((lane >> 4) & 1) + (lane & 3);
            bf16x8 fa[8], fb[8];
#pragma unroll
            for (int st = 0; st < 8; ++st) {
                const int t0 = 16 * (st & 1) + 4 * (lane >> 5) + q, t1 = t0 + 8;
                const int p0 = (((t0 >> 1) & 1) << 3) | (t0 & 1) | (((t0 >> 2) & 3) << 1), p1 = (((t1 >> 1) & 1) << 3) | (t1 & 1) | (((t1 >> 2) & 3) << 1);
                const unsigned ib = L_R1 + hh * 16384 + (st >> 1) * 4096;
                fa[st] = lds_tr2(ib + t0 * 128 + ((P ^ p0) << 3), ib + t1 * 128 + ((P ^ p1) << 3));
                fb[st] = asbf(lds_rd128(L_R0 + ((hh * 4 + (st >> 1)) * 2 + (st & 1)) * 1024 + lane * 16));
            }
            SBAR();
            f32x16 kv = {};
#pragma unroll
            for (int st = 0; st < 8; ++st) kv = MFMA(fa[st], fb[st], kv);
            WAIT_LGKM0(); sig(C_KVDONE, lane);
            const int hk = 2 * nt + hh;
#pragma unroll
            for (int u = 0; u < 4; ++u)
                st16(rws, __builtin_bit_cast(u32x4, (f32x4){kv[4 * u], kv[4 * u + 1], kv[4 * u + 2], kv[4 * u + 3]}), lane * 16 + u * 1024, WS_KVP + ((hk * 16 + mt) * 8 + ft * 4) * 1024);
            FSTAMP(19);
            post(rws, WS_F2 + ((hk * 16 + mt) * 2 + ft) * 128, tag, lane);
            waitc<0>(C_KVDONE, 4);
        }
        STAMP(5);
        if (r < 28) {
            waitc<HSLP>(C_QX, 4);
            if (w == 0) *(LAS u32x2*)(uintptr_t)(L_R0 + 57344 + 8 * lane) = (u32x2){0u, 0u};
            const rsrc_t rwob = mk_rsrc(ka[12], ka[13], 448 * 896);
#pragma unroll
            for (int i = 0; i < 14; ++i) { WAIT_VM(WOWIN); dma(rwob, wovo, ct * 128 + (w + 4 * i) * 8 * 896, L_R0 + (w + 4 * i) * 1024); }
            WAIT_VM(0); WAIT_LGKM0(); sig(C_WO0 + 1, lane); waitc<HSLP>(C_WO0 + 1, 4);
            read_wo(wf, w >> 1, ct, lane);
            WAIT_LGKM0(); sig(C_WOFREE, lane);
        }
        if (w == 3) {
            const int hr = head, i = lane & 15, pg = lane >> 4;
            poll<HSLP>(rws, WS_F2 + ((hr * 16 + i) * 2 + (qt >> 3)) * 128, tag);
            u32x4 pl[8];
#pragma unroll
            for (int p = 0; p < 4; ++p) {
                const unsigned vo = (pg * 4 + p) * 8192 + (16 * (qt & 3) + i) * 16, so = WS_KVP + hr * 131072 + (qt >> 2) * 2048;
                pl[2 * p] = ld16(rws, vo, so); pl[2 * p + 1] = ld16(rws, vo + 1024, so);
            }
            SBAR();
            f32x4 s0 = {}, s1 = {};
#pragma unroll
            for (int p = 0; p < 4; ++p) { s0 += __builtin_bit_cast(f32x4, pl[2 * p]); s1 += __builtin_bit_cast(f32x4, pl[2 * p + 1]); }
            float v[8] = {s0[0], s0[1], s0[2], s0[3], s1[0], s1[1], s1[2], s1[3]};
#pragma unroll
            for (int k = 0; k < 8; ++k) {
                auto t = __builtin_amdgcn_permlane16_swap(__float_as_uint(v[k]), __float_as_uint(v[k]), false, false);
                v[k] = swap_sum(__uint_as_float(t[0]) + __uint_as_float(t[1]));
            }
            u32x4 o = {pk2(v[0], v[1]), pk2(v[2], v[3]), pk2(v[4], v[5]), pk2(v[6], v[7])};
            const int cl = 16 * (qt & 3) + i;
            if ((cl & 31) == 29) o = (u32x4){0x3F803F80u, 0x3F803F80u, 0x3F803F80u, 0x3F803F80u};
            if (((qt >> 2) & 1) && cl >= 32) { o[2] = 0u; o[3] = 0u; }
            st16(rws, o, lane < 16 ? i * 16 : 0x7FFFFF00u, WS_KVR + (hr * 256 + qt * 16) * 16);
            post(rws, WS_F3 + (hr * 16 + qt) * 128, tag, lane);
        }
        STAMP(6);
        if (r < 28) {
            const rsrc_t rwoa = mk_rsrc(ka[4], ka[5], 448 * 896);
            waitc<HSLP>(C_WOFREE, 4);
#pragma unroll
            for (int i = 0; i < 14; ++i) { WAIT_VM(WOWIN); dma(rwoa, wovo, ct * 128 + (w + 4 * i) * 8 * 896, L_R0 + (w + 4 * i) * 1024); }
            WAIT_VM(0); sig(C_WO0, lane);
        }
        if (isb) {
            const int hb_ = 2 * nt + (w >> 1), tb = mt * 4 + 2 * (w & 1);
            poll<HSLP>(rws, WS_F3 + (hb_ * 16 + (lane & 15)) * 128, tag);
            u32x4 kf[4];
#pragma unroll
            for (int f = 0; f < 4; ++f) kf[f] = ld16(rws, lane * 16 + f * 1024, WS_KVR + hb_ * 4096);
            SBAR();
#pragma unroll
            for (int t = 0; t < 2; ++t) {
                f32x16 np = MFMA(asbf(kf[0]), asbf(pe[t][0]), f32x16{}); np = MFMA(asbf(kf[1]), asbf(pe[t][1]), np);
                f32x16 nm = MFMA(asbf(kf[2]), asbf(pe[t][2]), f32x16{}); nm = MFMA(asbf(kf[3]), asbf(pe[t][3]), nm);
                const float rp = __builtin_amdgcn_rcpf(np[13]), rm = __builtin_amdgcn_rcpf(nm[13]);
                const float rd = __builtin_amdgcn_rcpf(__builtin_fmaf(rp, np[12], rm * nm[12]) + 1e-6f);
                const float cp = from_hi(rp * rd), cm = from_hi(rm * rd);
                f32x16 o;
#pragma unroll
                for (int i = 0; i < 16; ++i) o[i] = __builtin_fmaf(cp, np[i], cm * nm[i]);
                u32x4 o0 = pk8(o, 0), o1 = pk8(o, 1);
                if (hi) { o1[2] = 0; o1[3] = 0; }
                const unsigned so = WS_O + (2u << 20) + (((tb + t) * 16 + hb_) * 2) * 1024;
                st16(rws, o0, lane * 16, so); st16(rws, o1, lane * 16 + 1024, so);
            }
            WAIT_VM(0);
            if (lane < 2) __builtin_amdgcn_raw_buffer_store_b64(tag, rws, WS_FO + (1024 + (tb + lane) * 16 + hb_) * 128, 0, 16);
            CBAR();
        }
        STAMP(7);
        if (b == 255 && w == 0) {
#pragma unroll 1
            for (int i = 0; i < 4; ++i) poll<HSLP>(rws, WS_F3 + (i * 64 + lane) * 128, tag);
            if (lane == 0) __builtin_amdgcn_raw_buffer_store_b64(tag, rws, WS_EPOCH, 0, 16);
        }
    } else {
        const int tq = qt * 4 + w;
        const int gq = (head * 43) >> 7, gk = ((16 + head) * 43) >> 7, gv = ((32 + head) * 43) >> 7;
        const int rk0 = qt >> 2;
        const unsigned fall = WS_F1 + (((lane & 8) ? gv : gk) * 32 + 8 * w + (lane & 7)) * 128;
        __builtin_amdgcn_s_setprio(3);
        poll<1>(rws, WS_F1 + (lane >= 4 ? gq * 32 + (tq >> 1) : ((lane & 2) ? gv : gk) * 32 + 8 * w + 2 * rk0 + (lane & 1)) * 128, tag);
        STAMP(5);
        const unsigned hb = WS_QKV + head * 64 * 6144, kvb = hb + 2048;
        const unsigned kb0 = kvb + (16 * w + (qt & 3)) * 6144, kb1 = kvb + (16 * w + ((qt + 1) & 3)) * 6144,
                       kb2 = kvb + (16 * w + ((qt + 2) & 3)) * 6144, kb3 = kvb + (16 * w + ((qt + 3) & 3)) * 6144;
        unsigned vc = lane * 16 + rk0 * 24576;
        u32x4 kA[4], kB[4], kC[4], kD[4];
#define LD(d, f, v, sb) d[f] = ld16(rws, (v) + (f) * 1024, sb)
        u32x4 q0 = ld16(rws, lane * 16, hb + tq * 6144), q1 = ld16(rws, lane * 16 + 1024, hb + tq * 6144);
        LD(kA, 0, vc, kb0); LD(kA, 1, vc, kb0);
        SBAR();
        f32x16 O[4] = {};
        u32x4 pA0 = {}, pA1 = {}, pB0, pB1;
        kD[2] = (u32x4){}; kD[3] = (u32x4){};
        PIN(O[0]); PIN(O[1]); PIN(O[2]); PIN(O[3]); PIN(pA0); PIN(pA1); PIN(kD[2]); PIN(kD[3]);
        SBAR();
        bf16x8 Q[4][2];
        {
            f32x16 s = MFMA(asbf(kA[0]), asbf(q0), f32x16{}); s = MFMA(asbf(kA[1]), asbf(q1), s);
            SBAR(); FSTAMP(13);
            LD(kA, 2, vc, kb0); LD(kA, 3, vc, kb0); LD(kB, 0, vc, kb1); LD(kB, 1, vc, kb1); LD(kB, 2, vc, kb1); LD(kB, 3, vc, kb1);
            LD(kC, 0, vc, kb2); LD(kC, 1, vc, kb2); LD(kC, 2, vc, kb2); LD(kC, 3, vc, kb2);
            SBAR();
            lds_wr128(L_QX + (w * 2) * 1024 + lane * 16, q0);
            const float m = swap_max(max16(s)) + REF_SHIFT;
            u32x4 q1m = q1;
            if (hi) q1m[2] = pk2(-m, 0.f);
            lds_wr128(L_QX + (w * 2 + 1) * 1024 + lane * 16, q1m);
            WAIT_LGKM0(); sig(C_QX, lane); FSTAMP(14); waitc<0>(C_QX, 4);
#pragma unroll
            for (int a = 0; a < 4; ++a) { Q[a][0] = asbf(lds_rd128(L_QX + (a * 2) * 1024 + lane * 16)); Q[a][1] = asbf(lds_rd128(L_QX + (a * 2 + 1) * 1024 + lane * 16)); }
        }
        const u32x2 fl = ldflag(rws, fall);
        STAMP(6); STAMPC(20);
        __builtin_amdgcn_s_setprio(0);
        f32x16 sA, sB;
        sA = MFMA(asbf(kA[0]), Q[0][0], f32x16{}); sA = MFMA(asbf(kA[1]), Q[0][1], sA);
        PIN(Q[1][0]); PIN(Q[1][1]); PIN(Q[2][0]); PIN(Q[2][1]); PIN(Q[3][0]); PIN(Q[3][1]);
#define EX4(S, i) S[i] = __builtin_amdgcn_exp2f(S[i]); S[i + 1] = __builtin_amdgcn_exp2f(S[i + 1]); S[i + 2] = __builtin_amdgcn_exp2f(S[i + 2]); S[i + 3] = __builtin_amdgcn_exp2f(S[i + 3])
#define STAGE(SC, SN, PC0, PC1, PP0, PP1, KN, an, VP, ap, F0, F1, F2, F3) do { \
        SN = MFMA(asbf(KN[0]), Q[an][0], f32x16{}); SBAR(); F0; SBAR(); EX4(SC, 0); PIN(SC); SBAR(); \
        SN = MFMA(asbf(KN[1]), Q[an][1], SN); SBAR(); F1; SBAR(); EX4(SC, 4); PC0[0] = pk2(SC[0], SC[1]); PC0[1] = pk2(SC[2], SC[3]); PIN(SC); PIN(PC0); SBAR(); \
        O[ap] = MFMA(asbf(VP[2]), asbf(PP0), O[ap]); SBAR(); F2; SBAR(); EX4(SC, 8); PC0[2] = pk2(SC[4], SC[5]); PC0[3] = pk2(SC[6], SC[7]); PIN(SC); PIN(PC0); SBAR(); \
        O[ap] = MFMA(asbf(VP[3]), asbf(PP1), O[ap]); SBAR(); F3; SBAR(); EX4(SC, 12); PC1[0] = pk2(SC[8], SC[9]); PC1[1] = pk2(SC[10], SC[11]); \
        PC1[2] = pk2(SC[12], SC[13]); PC1[3] = pk2(SC[14], SC[15]); PIN(PC1); SBAR(); } while (0)
#define NOP (void)0
#define ARRIVE(K) asm volatile("" : "+v"(K[0]), "+v"(K[1]), "+v"(K[2]), "+v"(K[3]))
#define TILE(KP, KC, KN, V, SB, G3) do { \
        STAGE(sA, sB, pB0, pB1, pA0, pA1, KC, 1, KP, 3, NOP, NOP, NOP, NOP); \
        STAGE(sB, sA, pA0, pA1, pB0, pB1, KC, 2, KC, 0, LD(KP, 0, V, SB), LD(KP, 1, V, SB), LD(KP, 2, V, SB), LD(KP, 3, V, SB)); \
        STAGE(sA, sB, pB0, pB1, pA0, pA1, KC, 3, KC, 1, NOP, NOP, NOP, ARRIVE(KN)); \
        STAGE(sB, sA, pA0, pA1, pB0, pB1, KN, 0, KC, 2, G3, NOP, NOP, NOP); } while (0)
#pragma unroll 1
        for (int it = 0; it < 4; ++it) {
            const unsigned vn = it < 3 ? lane * 16 + ((rk0 + it + 1) & 3) * 24576 : 0x7FFF0000u;
            TILE(kD, kA, kB, vc, kb3, NOP);
            if (it == 0) { CBAR(); if (!tagok(fl, tag)) poll<1>(rws, fall, tag); CBAR(); }
            TILE(kA, kB, kC, vn, kb0, NOP);
            TILE(kB, kC, kD, vn, kb1, NOP);
            TILE(kC, kD, kA, vn, kb2, NOP);
            vc = vn;
        }
        O[3] = MFMA(asbf(kD[2]), asbf(pA0), O[3]); O[3] = MFMA(asbf(kD[3]), asbf(pA1), O[3]);
        STAMP(7); STAMPC(21);
        f32x16 o;
        const unsigned cb = L_R2 + lane * 16;
#define SEND(a, sl) _Pragma("unroll") for (int u = 0; u < 4; ++u) lds_wrf4(cb + ((sl) * 4 + u) * 1024, (f32x4){O[a][4 * u], O[a][4 * u + 1], O[a][4 * u + 2], O[a][4 * u + 3]})
#define RECV(a) do { WAIT_LGKM0(); sig(C_CMB, lane); waitc<0>(C_CMB, 4); f32x4 pr[12]; \
            _Pragma("unroll") for (int k = 0; k < 12; ++k) pr[k] = lds_rdf4(cb + ((a) * 12 + k) * 1024); SBAR(); \
            _Pragma("unroll") for (int u = 0; u < 4; ++u) { const f32x4 t = (pr[u] + (f32x4){O[a][4 * u], O[a][4 * u + 1], O[a][4 * u + 2], O[a][4 * u + 3]}) + (pr[4 + u] + pr[8 + u]); \
                o[4 * u] = t[0]; o[4 * u + 1] = t[1]; o[4 * u + 2] = t[2]; o[4 * u + 3] = t[3]; } PIN(o); } while (0)
        if (w == 0) { SEND(1, 3); SEND(2, 6); SEND(3, 9); RECV(0); }
        else if (w == 1) { SEND(0, 0); SEND(2, 7); SEND(3, 10); RECV(1); }
        else if (w == 2) { SEND(0, 1); SEND(1, 4); SEND(3, 11); RECV(2); }
        else { SEND(0, 2); SEND(1, 5); SEND(2, 8); RECV(3); }
        float l = from_hi(o[12]);
        {
            f32x2 chk = {0.f, 0.f};
#pragma unroll
            for (int i = 0; i < 16; i += 2) chk = __builtin_elementwise_fma((f32x2){o[i], o[i + 1]}, (f32x2){0.f, 0.f}, chk);
            bool bad = !(chk[0] + chk[1] == 0.f) || !(l >= 1e-32f && l <= 1e30f);
            if (__any(bad)) {
                float mrun = -3.0e38f;
                o = f32x16{};
#pragma unroll 1
                for (int k2 = 0; k2 < 4; ++k2) {
                    poll(rws, WS_F1 + (((lane & 8) ? gv : gk) * 32 + 8 * k2 + (lane & 7)) * 128, tag);
#pragma unroll 1
                    for (int m2 = 0; m2 < 16; ++m2) {
                        const unsigned so = kvb + (k2 * 16 + m2) * 6144;
                        const u32x4 k0 = ld16(rws, lane * 16, so), k1 = ld16(rws, lane * 16 + 1024, so), v0 = ld16(rws, lane * 16 + 2048, so), v1 = ld16(rws, lane * 16 + 3072, so);
                        f32x16 s = MFMA(asbf(k0), asbf(q0), f32x16{}); s = MFMA(asbf(k1), asbf(q1), s);
                        const float mn = __builtin_fmaxf(mrun, swap_max(max16(s))), al = __builtin_amdgcn_exp2f(mrun - mn);
                        mrun = mn;
#pragma unroll
                        for (int i = 0; i < 16; ++i) { s[i] = __builtin_amdgcn_exp2f(s[i] - mn); o[i] *= al; }
                        o = MFMA(asbf(v0), asbf(pk8(s, 0)), o); o = MFMA(asbf(v1), asbf(pk8(s, 1)), o);
                    }
                }
                l = from_hi(o[12]);
            }
        }
        const float rl = __builtin_amdgcn_rcpf(l);
#pragma unroll
        for (int i = 0; i < 16; ++i) o[i] *= rl;
        u32x4 o0 = pk8(o, 0), o1 = pk8(o, 1);
        if (hi) { o1[2] = 0; o1[3] = 0; }
        const unsigned so = WS_O + ((tq * 16 + head) * 2) * 1024;
        st16(rws, o0, lane * 16, so); st16(rws, o1, lane * 16 + 1024, so);
        sig(C_ATT, lane);
        STAMP(8); FSTAMPC(27);
        fl0 = ldflag(rws, WS_FO + ((rt * 2 + (w & 1)) * 16 + (w >> 1) * 8 + ((lane + ct) & 7)) * 128);
        SBAR();
        if (r < 28) { waitc<0>(C_WO0, 4); read_wo(wf, w >> 1, ct, lane); }
        post(rws, WS_FO + (tq * 16 + head) * 128, tag, lane);
        FSTAMP(9);
    }

    if (r < 28) {
        const int br = role, tp = w & 1, kh = w >> 1, tto = rt * 2 + tp;
        const rsrc_t rout = mk_rsrc(ka[14], ka[15], 2048 * 1792);
        const unsigned ffo = WS_FO + (br * 1024 + tto * 16 + kh * 8 + ((lane + ct) & 7)) * 128;
        const unsigned ob = WS_O + br * (2u << 20) + (tto * 16 + kh * 8) * 2048;
        u32x4 of[16];
        f32x16 c0 = {}, c1 = {};
        if (role) {
            poll<HSLP>(rws, ffo, tag);
#pragma unroll
            for (int k = 0; k < 16; ++k) {
                if ((k & 3) == 0 && k) WAIT_VM(0);
                of[k] = ld16(rws, lane * 16 + (k & 1) * 1024, ob + (((k >> 1) + ct) & 7) * 2048); SBAR();
            }
            waitc<4>(C_ATT, 4);
            __builtin_amdgcn_s_setprio(0);
#pragma unroll
            for (int k = 0; k < 16; ++k) { c0 = MFMA(wf[k][0], asbf(of[k]), c0); c1 = MFMA(wf[k][1], asbf(of[k]), c1); }
        } else {
            unsigned req = 0, fin = 0;
#pragma unroll
            for (int k = 0; k < 16; ++k) asm volatile("" : "=v"(of[k]));
#pragma clang loop unroll(disable)
            for (unsigned it = 0; it < SPIN_CAP && fin != 255u; ++it) {
                unsigned rdy = 255u;
                if (req != 255u) {
                    const u32x2 f = it ? ldflag(rws, ffo) : fl0;
                    rdy = (unsigned)__builtin_amdgcn_ballot_w64(__builtin_bit_cast(unsigned long long, f) == __builtin_bit_cast(unsigned long long, tag)) & 255u;
                }
                CBAR();
                const unsigned todo = (req ^ (req + 1u)) >> 1 & ~fin, nw = rdy & ~req;
#pragma unroll
                for (int k = 0; k < 8; ++k)
                    if (nw & (1u << k)) { const unsigned sb = ob + ((k + ct) & 7) * 2048; of[2 * k] = ld16(rws, lane * 16, sb); of[2 * k + 1] = ld16(rws, lane * 16 + 1024, sb); CBAR(); }
#pragma unroll
                for (int k = 0; k < 8; ++k)
                    if (todo & (1u << k)) {
                        c0 = MFMA(wf[2 * k][0], asbf(of[2 * k]), c0); c1 = MFMA(wf[2 * k][1], asbf(of[2 * k]), c1);
                        c0 = MFMA(wf[2 * k + 1][0], asbf(of[2 * k + 1]), c0); c1 = MFMA(wf[2 * k + 1][1], asbf(of[2 * k + 1]), c1); CBAR();
                    }
                fin |= todo; req |= nw;
            }
            sig(C_GATH, lane);
        }
        STAMP(10);
        const unsigned xb_ = L_R1 + br * 16384;
        const unsigned vo = (tto * 32 + (lane & 31)) * 1792 + (hi ? 16 : 0), so = br * 896 + ct * 128 + kh * 64;
#define FINISH(KEEP, GIVE) do { \
            _Pragma("unroll") for (int u = 0; u < 4; ++u) lds_wrf4(xb_ + (w * 4 + u) * 1024 + lane * 16, (f32x4){GIVE[4 * u], GIVE[4 * u + 1], GIVE[4 * u + 2], GIVE[4 * u + 3]}); \
            WAIT_LGKM0(); sig(C_EX0 + br * 2 + tp, lane); waitc<0>(C_EX0 + br * 2 + tp, 2); \
            _Pragma("unroll") for (int u = 0; u < 4; ++u) { const f32x4 s = lds_rdf4(xb_ + ((w ^ 2) * 4 + u) * 1024 + lane * 16); \
                _Pragma("unroll") for (int i = 0; i < 4; ++i) KEEP[4 * u + i] += s[i]; } \
            if (role) waitc<1>(C_GATH, 4); \
            _Pragma("unroll") for (int gp = 0; gp < 2; ++gp) { \
                auto s0 = __builtin_amdgcn_permlane32_swap(pk2(KEEP[8 * gp], KEEP[8 * gp + 1]), pk2(KEEP[8 * gp + 4], KEEP[8 * gp + 5]), false, false); \
                auto s1 = __builtin_amdgcn_permlane32_swap(pk2(KEEP[8 * gp + 2], KEEP[8 * gp + 3]), pk2(KEEP[8 * gp + 6], KEEP[8 * gp + 7]), false, false); \
                st16a<0>(rout, (u32x4){s0[0], s1[0], s0[1], s1[1]}, vo + gp * 32, so); } } while (0)
        if (kh) FINISH(c1, c0); else FINISH(c0, c1);
        STAMP(12);
    }
    STAMP_FLUSH();
    if (wid == 4 && blockIdx.x == 100) { const unsigned d_ = ((unsigned)__builtin_amdgcn_readlane(stv, 18) - (unsigned)__builtin_amdgcn_readlane(stv, 0)) / 1;
        unsigned long long r0_ = __builtin_amdgcn_s_memrealtime(), dur_ = (d_ > 20000u ? 20000u : d_) * 100ull;
        if (__builtin_amdgcn_readlane(stv, 18) != 0) while (__builtin_amdgcn_s_memrealtime() - r0_ < dur_) __builtin_amdgcn_s_sleep(64); }
    asm volatile(".Lpf_end:");
}

extern "C" void kernel_launch(void* const* d_in, const int* in_sizes, int n_in, void* d_out, int out_size, void* d_ws, size_t ws_size,
                              hipStream_t stream) {
    (void)in_sizes; (void)n_in; (void)out_size; (void)ws_size;
    (void)hipFuncSetAttribute((const void*)fused_fwd, hipFuncAttributeMaxDynamicSharedMemorySize, LDS_BYTES);
    fused_fwd<<<256, 512, LDS_BYTES, stream>>>(d_in[0], d_in[1], d_in[2], d_in[3], d_in[4], d_in[5], d_in[6], d_out, d_ws);
}
```

```cpp
#include <hip/hip_runtime.h>
#include <hip/hip_bf16.h>
#include <stdint.h>
#include <type_traits>

typedef __attribute__((ext_vector_type(8))) __bf16 bf16x8;
typedef __attribute__((ext_vector_type(2))) __bf16 bf16x2;
typedef __attribute__((ext_vector_type(2))) float f32x2;
typedef __attribute__((ext_vector_type(4))) float f32x4;
typedef __attribute__((ext_vector_type(16))) float f32x16;
typedef __attribute__((ext_vector_type(4))) unsigned u32x4;
typedef __attribute__((ext_vector_type(2))) unsigned u32x2;
typedef __attribute__((ext_vector_type(4))) short s16x4;
typedef __amdgpu_buffer_rsrc_t rsrc_t;
#define LAS __attribute__((address_space(3)))
#define DI __device__ __forceinline__

constexpr unsigned WS_EPOCH = 0;
constexpr unsigned WS_F1 = 4096;
constexpr unsigned WS_F2 = WS_F1 + 1024 * 128;
constexpr unsigned WS_F3 = WS_F2 + 512 * 128;
constexpr unsigned WS_FO = WS_F3 + 256 * 128;
constexpr unsigned WS_QKV = 1u << 20;
constexpr unsigned WS_KVP = WS_QKV + (6u << 20);
constexpr unsigned WS_KVR = WS_KVP + (2u << 20);
constexpr unsigned WS_O = WS_KVR + (1u << 20);
constexpr unsigned WS_DBG = WS_O + (4u << 20);
constexpr unsigned WS_BYTES = WS_DBG + (1u << 20);

constexpr unsigned SLOT = 37888;
constexpr unsigned L_R0 = 0;
constexpr unsigned L_R1 = 57856;
constexpr unsigned L_R2 = L_R1 + 32768;
constexpr unsigned L_QX = L_R2 + 49152;
constexpr unsigned L_QB = L_R2;
constexpr unsigned L_PHI = 4 * SLOT + 64;
constexpr unsigned L_JUNK = L_PHI + 4096;
constexpr unsigned L_CNT = L_JUNK + 1024;
constexpr unsigned L_TAG = L_CNT + 256;
constexpr unsigned LDS_BYTES = L_TAG + 16;
enum { C_LAND0 = 24, C_DONE0 = 32, C_QB = 4, C_QX = 10, C_PV = 11, C_KVDONE = 12, C_WO0 = 13, C_WOFREE = 15, C_EX0 = 16, C_CMB = 20, C_ATT = 21, C_GATH = 22 };

#define MAP_SEP 0
#define PNX 3
#define PNW 3
#define FASTG 2
#define ROT 1
#define LATE1 0
#define HSLP 8
#define WOWIN 3
#define GWIN 0
constexpr unsigned SPIN_CAP = 1u << 16;
constexpr float QSCALE = 0.18898223650461362f * 1.4426950408889634f;
constexpr float LOG2E = 1.4426950408889634f;
constexpr float REF_SHIFT = 100.f;

#define MFMA(a, b, c) __builtin_amdgcn_mfma_f32_32x32x16_bf16((a), (b), (c), 0, 0, 0)
#define CBAR() asm volatile("" ::: "memory")
#define WAIT_VM(n) asm volatile("s_waitcnt vmcnt(%0)" :: "i"(n) : "memory")
#define WAIT_LGKM0() asm volatile("s_waitcnt lgkmcnt(0)" ::: "memory")
#define SBAR() __builtin_amdgcn_sched_barrier(0)

#define STAMP_DECL
#define STAMP(i) do {} while (0)
#define STAMPC(i) do {} while (0)
#define STAMP_FLUSH() do {} while (0)
#define FSTAMP(i) do {} while (0)
#define FSTAMPC(i) do {} while (0)
#define PIN(x) asm volatile("" : "+v"(x))

DI unsigned pk2(float a, float b) { f32x2 v = {a, b}; return __builtin_bit_cast(unsigned, __builtin_convertvector(v, bf16x2)); }
DI u32x4 pk8(const f32x16& a, int s) {
    return (u32x4){pk2(a[8 * s], a[8 * s + 1]), pk2(a[8 * s + 2], a[8 * s + 3]), pk2(a[8 * s + 4], a[8 * s + 5]), pk2(a[8 * s + 6], a[8 * s + 7])}; }
DI bf16x8 asbf(u32x4 v) { return __builtin_bit_cast(bf16x8, v); }
DI float swap_max(float m) { auto r = __builtin_amdgcn_permlane32_swap(__float_as_uint(m), __float_as_uint(m), false, false);
    return __builtin_fmaxf(__uint_as_float(r[0]), __uint_as_float(r[1])); }
DI float swap_min(float m) { auto r = __builtin_amdgcn_permlane32_swap(__float_as_uint(m), __float_as_uint(m), false, false);
    return __builtin_fminf(__uint_as_float(r[0]), __uint_as_float(r[1])); }
DI float swap_sum(float m) { auto r = __builtin_amdgcn_permlane32_swap(__float_as_uint(m), __float_as_uint(m), false, false);
    return __uint_as_float(r[0]) + __uint_as_float(r[1]); }
DI float from_hi(float m) { auto r = __builtin_amdgcn_permlane32_swap(__float_as_uint(m), __float_as_uint(m), false, false);
    return __uint_as_float(r[1]); }
DI float max16(const f32x16& s) {
    float a = __builtin_fmaxf(__builtin_fmaxf(s[0], s[1]), s[2]), b = __builtin_fmaxf(__builtin_fmaxf(s[3], s[4]), s[5]);
    a = __builtin_fmaxf(__builtin_fmaxf(a, s[6]), s[7]); b = __builtin_fmaxf(__builtin_fmaxf(b, s[8]), s[9]);
    a = __builtin_fmaxf(__builtin_fmaxf(a, s[10]), s[11]); b = __builtin_fmaxf(__builtin_fmaxf(b, s[12]), s[13]);
    a = __builtin_fmaxf(__builtin_fmaxf(a, s[14]), s[15]); return __builtin_fmaxf(a, b); }
DI float min16(const f32x16& s) {
    float a = __builtin_fminf(__builtin_fminf(s[0], s[1]), s[2]), b = __builtin_fminf(__builtin_fminf(s[3], s[4]), s[5]);
    a = __builtin_fminf(__builtin_fminf(a, s[6]), s[7]); b = __builtin_fminf(__builtin_fminf(b, s[8]), s[9]);
    a = __builtin_fminf(__builtin_fminf(a, s[10]), s[11]); b = __builtin_fminf(__builtin_fminf(b, s[12]), s[13]);
    a = __builtin_fminf(__builtin_fminf(a, s[14]), s[15]); return __builtin_fminf(a, b); }

DI void dma(rsrc_t rs, unsigned voff, unsigned soff, unsigned lds) {
    asm volatile("s_mov_b32 m0, %0\n\ts_nop 0\n\tbuffer_load_dwordx4 %1, %2, %3 offen lds" :: "s"(lds), "v"(voff), "s"(rs), "s"(soff) : "memory"); }
DI u32x4 lds_rd128(unsigned a) { return *(LAS const u32x4*)(uintptr_t)a; }
DI f32x4 lds_rdf4(unsigned a) { return *(LAS const f32x4*)(uintptr_t)a; }
DI void lds_wr128(unsigned a, u32x4 v) { *(LAS u32x4*)(uintptr_t)a = v; }
DI void lds_wrf4(unsigned a, f32x4 v) { *(LAS f32x4*)(uintptr_t)a = v; }
DI void lds_wr64(unsigned a, u32x2 v) { *(LAS u32x2*)(uintptr_t)a = v; }
DI bf16x8 lds_tr2(unsigned a0, unsigned a1) {
    s16x4 lo = __builtin_amdgcn_ds_read_tr16_b64_v4i16((LAS s16x4*)(uintptr_t)a0), hi = __builtin_amdgcn_ds_read_tr16_b64_v4i16((LAS s16x4*)(uintptr_t)a1);
    return __builtin_bit_cast(bf16x8, __builtin_shufflevector(lo, hi, 0, 1, 2, 3, 4, 5, 6, 7)); }
DI void sig(int c, int) {
    asm volatile("s_mov_b64 exec, 1\n\tds_add_u32 %0, %1\n\ts_mov_b64 exec, -1" :: "v"(L_CNT + 4 * c), "v"(1u) : "memory"); }
template <int SLP = 1> DI void waitc(int c, unsigned n) {
    unsigned v, sv, it;
    asm volatile("s_mov_b32 %2, %5\n"
                 "1:\n\tds_read_b32 %0, %3\n\ts_waitcnt lgkmcnt(0)\n\tv_readfirstlane_b32 %1, %0\n\ts_cmp_ge_u32 %1, %4\n\ts_cbranch_scc1 2f\n\t"
                 "s_sleep %6\n\ts_sub_u32 %2, %2, 1\n\ts_cbranch_scc0 1b\n2:"
                 : "=&v"(v), "=&s"(sv), "=&s"(it) : "v"(L_CNT + 4 * c), "i"(n), "i"(SPIN_CAP), "i"(SLP) : "memory", "scc"); }
DI unsigned rd_cnt(int c) { return *(volatile LAS const unsigned*)(uintptr_t)(L_CNT + 4 * c); }
DI u32x2 rd_tag() { const u32x2 t = *(LAS const u32x2*)(uintptr_t)L_TAG;
    return (u32x2){(unsigned)__builtin_amdgcn_readfirstlane(t[0]), (unsigned)__builtin_amdgcn_readfirstlane(t[1])}; }
DI u32x4 ld16(rsrc_t rs, unsigned voff, unsigned soff) { return __builtin_amdgcn_raw_buffer_load_b128(rs, voff, soff, 16); }
template <int AUX> DI void st16a(rsrc_t rs, u32x4 v, unsigned voff, unsigned soff) {
    __builtin_amdgcn_raw_buffer_store_b128(v, rs, voff, soff, AUX); asm volatile("s_nop 1" :: "v"(v) : "memory"); }
DI void st16(rsrc_t rs, u32x4 v, unsigned voff, unsigned soff) { st16a<16>(rs, v, voff, soff); }
DI u32x2 ldflag(rsrc_t rs, unsigned voff) { return __builtin_amdgcn_raw_buffer_load_b64(rs, voff, 0, 16); }
DI bool tagok(u32x2 f, u32x2 tag) {
    return __builtin_amdgcn_ballot_w64(__builtin_bit_cast(unsigned long long, f) == __builtin_bit_cast(unsigned long long, tag)) == ~0ull; }
DI unsigned ready8(u32x2 f, u32x2 tag) {
    return (unsigned)__builtin_amdgcn_ballot_w64(__builtin_bit_cast(unsigned long long, f) == __builtin_bit_cast(unsigned long long, tag)) & 255u; }
template <int SLP = 2> DI void poll(rsrc_t rs, unsigned voff, u32x2 tag) {
    CBAR();
#pragma clang loop unroll(disable)
    for (unsigned it = 0; it < SPIN_CAP; ++it) { u32x2 f = ldflag(rs, voff); if (tagok(f, tag)) break; __builtin_amdgcn_s_sleep(SLP); CBAR(); }
    CBAR(); }
DI void poll_fast(rsrc_t rs, unsigned voff, u32x2 tag, u32x2& a, u32x2& b, u32x2& c) {
    unsigned it;
    asm volatile("s_mov_b32 %3, %7\n\tbuffer_load_dwordx2 %0, %4, %5, 0 offen sc1\n\ts_nop 7\n\ts_nop 7\n\tbuffer_load_dwordx2 %1, %4, %5, 0 offen sc1\n\ts_nop 7\n\ts_nop 7\n"
                 "1:\n\tbuffer_load_dwordx2 %2, %4, %5, 0 offen sc1\n\ts_waitcnt vmcnt(2)\n\tv_cmp_eq_u64 vcc, %6, %0\n\ts_cmp_eq_u64 vcc, -1\n\ts_cbranch_scc1 2f\n\t"
                 "buffer_load_dwordx2 %0, %4, %5, 0 offen sc1\n\ts_waitcnt vmcnt(2)\n\tv_cmp_eq_u64 vcc, %6, %1\n\ts_cmp_eq_u64 vcc, -1\n\ts_cbranch_scc1 2f\n\t"
                 "buffer_load_dwordx2 %1, %4, %5, 0 offen sc1\n\ts_waitcnt vmcnt(2)\n\tv_cmp_eq_u64 vcc, %6, %2\n\ts_cmp_eq_u64 vcc, -1\n\ts_cbranch_scc1 2f\n\t"
                 "s_sub_u32 %3, %3, 1\n\ts_cbranch_scc0 1b\n2:"
                 : "=&v"(a), "=&v"(b), "=&v"(c), "=&s"(it) : "v"(voff), "s"(rs), "s"(tag), "i"(SPIN_CAP) : "memory", "vcc", "scc");
}
DI void post(rsrc_t rs, unsigned off, u32x2 tag, int lane) {
    WAIT_VM(0);
    if (lane == 0) __builtin_amdgcn_raw_buffer_store_b64(tag, rs, off, 0, 16);
    CBAR(); }

#define GB(n, k) "v[176+20*" #n "+4*" #k ":179+20*" #n "+4*" #k "]"
#define GT(A, n, t, i) "v_mfma_f32_32x32x16_bf16 %[" #A "], " GB(n, (2+i)) ", " GB(n, t) ", %[" #A "]\n\t"
#define GD(A, n, t, i) "v_mfma_f32_32x32x16_bf16 %[" #A "], " GB(n, t) ", " GB(n, (2+i)) ", %[" #A "]\n\t"
#define GX(n, t, X) "ds_read_b128 " GB(n, t) ", " X " offset:4096*" #t "\n\t"
#define GF(n, i, W, O) "ds_read_b64_tr_b16 v[176+20*" #n "+8+4*" #i ":177+20*" #n "+8+4*" #i "], " W " offset:" O "\n\t" \
                       "ds_read_b64_tr_b16 v[178+20*" #n "+8+4*" #i ":179+20*" #n "+8+4*" #i "], " W " offset:" O "+336\n\t"
#define GSTEP(M0, M1, M2, c, n, X, O, P0, P0B, P2, P4, P5) \
    M0(a0, c, 0, 0) P0 GX(n, 0, X) P0B M0(a3, c, 1, 0) GF(n, 0, "%[wa]", O) M1(a1, c, 0, 1) GX(n, 1, X) P2 M1(a4, c, 1, 1) GF(n, 1, "%[wb]", O) \
    M2(a2, c, 0, 2) GF(n, 2, "%[wc]", O) P4 M2(a5, c, 1, 2) P5
#define GLOOP(M0, M1, M2) \
    GX(0, 0, "%[xa]") GF(0, 0, "%[wa]", "0") GX(0, 1, "%[xa]") GF(0, 1, "%[wb]", "0") GF(0, 2, "%[wc]", "0") \
    GX(1, 0, "v170") GF(1, 0, "%[wa]", "5376") GX(1, 1, "v170") GF(1, 1, "%[wb]", "5376") GF(1, 2, "%[wc]", "5376") \
    "s_waitcnt lgkmcnt(8)\n" \
    "1:\n\t" \
    GSTEP(M0, M1, M2, 0, 2, "v171", "10752", "", "ds_read_b128 v[166:169], %[la]\n\t", "v_xor_b32 v172, 0x60, %[xa]\n\t", "s_and_b32 %[so], %[g], 3\n\t", \
          "s_mul_i32 %[so], %[so], %[slot]\n\ts_waitcnt lgkmcnt(9)\n\t") \
    GSTEP(M0, M1, M2, 1, 3, "v172", "16128", "", "v_add_u32 %[xa], %[so], %[xa0]\n\ts_waitcnt lgkmcnt(8)\n\t", "v_min3_u32 v173, v166, v167, v168\n\tv_xor_b32 v170, 32, %[xa]\n\t", \
          "v_min_u32 v173, v173, v169\n\tv_add_u32 %[wa], %[so], %[wa0]\n\t", "v_add_u32 %[wb], %[so], %[wb0]\n\tv_readfirstlane_b32 %[rf], v173\n\tv_add_u32 %[wc], %[so], %[wc0]\n\ts_waitcnt lgkmcnt(8)\n\t") \
    GSTEP(M0, M1, M2, 2, 0, "%[xa]", "0", "s_cmp_gt_u32 %[rf], %[g]\n\ts_cbranch_scc0 3f\n2:\n\t", "", "v_xor_b32 v171, 64, %[xa]\n\t", "", \
          "s_waitcnt lgkmcnt(8)\n\tv_mov_b32 v174, %[g]\n\t") \
    GSTEP(M0, M1, M2, 3, 1, "v170", "5376", "", "s_add_u32 %[g], %[g], 1\n\t", "s_cmp_lt_u32 %[g], 8\n\t", "", \
          "s_waitcnt lgkmcnt(8)\n\tds_write_b32 %[da], v174\n\ts_cbranch_scc1 1b\n\t") \
    "s_branch 4f\n" \
    "3:\n\ts_mov_b32 %[so], %[cap]\n" \
    "5:\n\tds_read_b128 v[166:169], %[la]\n\ts_waitcnt lgkmcnt(0)\n\tv_min3_u32 v173, v166, v167, v168\n\tv_min_u32 v173, v173, v169\n\ts_nop 0\n\tv_readfirstlane_b32 %[rf], v173\n\ts_cmp_gt_u32 %[rf], %[g]\n\ts_cbranch_scc1 2b\n\t" \
    "s_sub_u32 %[so], %[so], 1\n\ts_cbranch_scc0 5b\n\ts_branch 2b\n" \
    "4:\n\ts_waitcnt lgkmcnt(0)\n\ts_nop 7\n\ts_nop 3"
template <int ND, class Start>
DI void gemm6(unsigned xa0, unsigned wa0, unsigned wb0, unsigned wc0, int w, f32x16 (&acc)[6], Start start) {
    unsigned xa = xa0, wa = wa0, wb = wb0, wc = wc0, g = 1, so, rf;
    const unsigned la = L_CNT + 4 * C_LAND0, da = L_CNT + 4 * (C_DONE0 + w);
#pragma unroll
    for (int i = 0; i < 6; ++i) { acc[i] = f32x16{}; PIN(acc[i]); }
    PIN(xa); PIN(wa); PIN(wb); PIN(wc);
    start();
#define GASM(M0, M1, M2) asm volatile("v_xor_b32 v170, 32, %[xa]\n\tv_xor_b32 v171, 64, %[xa]\n\t" GLOOP(M0, M1, M2) \
        : [a0] "+v"(acc[0]), [a1] "+v"(acc[1]), [a2] "+v"(acc[2]), [a3] "+v"(acc[3]), [a4] "+v"(acc[4]), [a5] "+v"(acc[5]), \
          [xa] "+v"(xa), [wa] "+v"(wa), [wb] "+v"(wb), [wc] "+v"(wc), [g] "+s"(g), [so] "=&s"(so), [rf] "=&s"(rf) \
        : [xa0] "v"(xa0), [wa0] "v"(wa0), [wb0] "v"(wb0), [wc0] "v"(wc0), [la] "v"(la), [da] "v"(da), [slot] "i"(SLOT), [cap] "i"(SPIN_CAP) \
        : "memory", "scc", "v166", "v167", "v168", "v169", "v170", "v171", "v172", "v173", "v174", "v175", "v176", "v177", "v178", "v179", "v180", "v181", "v182", "v183", "v184", "v185", "v186", "v187", "v188", "v189", "v190", "v191", "v192", "v193", "v194", "v195", "v196", "v197", "v198", "v199", "v200", "v201", "v202", "v203", "v204", "v205", "v206", "v207", "v208", "v209", "v210", "v211", "v212", "v213", "v214", "v215", "v216", "v217", "v218", "v219", "v220", "v221", "v222", "v223", "v224", "v225", "v226", "v227", "v228", "v229", "v230", "v231", "v232", "v233", "v234", "v235", "v236", "v237", "v238", "v239", "v240", "v241", "v242", "v243", "v244", "v245", "v246", "v247", "v248", "v249", "v250", "v251", "v252", "v253", "v254", "v255")
    if constexpr (ND == 0) GASM(GT, GT, GT); else if constexpr (ND == 1) GASM(GT, GT, GD); else GASM(GD, GD, GD);
}
DI unsigned min4(u32x4 d) { return __builtin_amdgcn_readfirstlane(min(min(d[0], d[1]), min(d[2], d[3]))); }
DI u32x4 rd4(int c) { return *(volatile LAS const u32x4*)(uintptr_t)(L_CNT + 4 * c); }
DI void wait4(int c, unsigned n) {
    CBAR();
#pragma clang loop unroll(disable)
    for (unsigned it = 0; it < SPIN_CAP; ++it) if (min4(rd4(c)) >= n) break;
    CBAR(); }
template <int P> DI void dma_x4(rsrc_t rs, unsigned voff, unsigned soff, unsigned lds) {
    asm volatile("s_mov_b32 m0, %1\n\ts_nop %4\n\tbuffer_load_dwordx4 %2, %3, %0 offen lds\n\t"
                 "s_add_u32 m0, m0, 0x1000\n\ts_add_u32 %0, %0, 28672\n\ts_nop %4\n\tbuffer_load_dwordx4 %2, %3, %0 offen lds\n\t"
                 "s_add_u32 m0, m0, 0x1000\n\ts_add_u32 %0, %0, 28672\n\ts_nop %4\n\tbuffer_load_dwordx4 %2, %3, %0 offen lds\n\t"
                 "s_add_u32 m0, m0, 0x1000\n\ts_add_u32 %0, %0, 28672\n\ts_nop %4\n\tbuffer_load_dwordx4 %2, %3, %0 offen lds"
                 : "+s"(soff) : "s"(lds), "v"(voff), "s"(rs), "i"(P) : "memory", "scc"); }
template <int P> DI void dma_w5(rsrc_t rs, const unsigned (&v)[6], unsigned soff, unsigned lds) {
    asm volatile("s_mov_b32 m0, %0\n\ts_nop %8\n\tbuffer_load_dwordx4 %3, %1, %2 offen lds\n\t"
                 "s_add_u32 m0, m0, 0x1000\n\ts_nop %8\n\tbuffer_load_dwordx4 %4, %1, %2 offen lds\n\t"
                 "s_add_u32 m0, m0, 0x1000\n\ts_nop %8\n\tbuffer_load_dwordx4 %5, %1, %2 offen lds\n\t"
                 "s_add_u32 m0, m0, 0x1000\n\ts_nop %8\n\tbuffer_load_dwordx4 %6, %1, %2 offen lds\n\t"
                 "s_add_u32 m0, m0, 0x1000\n\ts_nop %8\n\tbuffer_load_dwordx4 %7, %1, %2 offen lds"
                 :: "s"(lds), "s"(rs), "s"(soff), "v"(v[0]), "v"(v[1]), "v"(v[2]), "v"(v[3]), "v"(v[4]), "i"(P) : "memory", "scc"); }
DI unsigned tr_addr128(unsigned base, int cb, int rowgrp, int lane) {
    const int q = (lane & 15) >> 2, byte = cb + 32 * ((lane >> 4) & 1) + 8 * (lane & 3);
    return base + (rowgrp + q) * 128 + ((((byte >> 4) ^ ((q >> 1) << 2)) & 7) << 4) + (byte & 8);
}
DI void feat(const f32x16& p, f32x16& e, f32x16& g) {
    const float mx = swap_max(max16(p)) * LOG2E, mn = swap_min(min16(p)) * LOG2E;
#pragma unroll
    for (int i = 0; i < 16; i += 2) {
        const f32x2 pp = {p[i], p[i + 1]};
        const f32x2 a = __builtin_elementwise_fma(pp, (f32x2){LOG2E, LOG2E}, (f32x2){-mx, -mx}), c = __builtin_elementwise_fma(pp, (f32x2){-LOG2E, -LOG2E}, (f32x2){mn, mn});
        e[i] = __builtin_amdgcn_exp2f(a[0]); e[i + 1] = __builtin_amdgcn_exp2f(a[1]); g[i] = __builtin_amdgcn_exp2f(c[0]); g[i + 1] = __builtin_amdgcn_exp2f(c[1]);
    }
}

typedef __attribute__((ext_vector_type(16))) unsigned u32x16;
DI rsrc_t mk_rsrc(unsigned lo, unsigned hi, unsigned bytes) {
    return __builtin_amdgcn_make_buffer_rsrc((void*)(((unsigned long long)hi << 32) | lo), 0, bytes, 0x00020000); }
DI void read_wo(bf16x8 (&wf)[16][2], unsigned ta0, unsigned ta1, int rot) {
#pragma unroll
    for (int k = 0; k < 8; ++k) {
        const unsigned ro = ((k + rot) & 7) * 28 * 128, b0 = ta0 + ro, b1 = ta1 + ro;
        wf[2 * k][0] = lds_tr2(b0, b0 + 1024); wf[2 * k][1] = lds_tr2(b1, b1 + 1024);
        wf[2 * k + 1][0] = lds_tr2(b0 + 2048, b0 + 3072); wf[2 * k + 1][1] = lds_tr2(b1 + 2048, b1 + 3072);
    }
}
DI void ones_row(u32x4& a, u32x4& b, u32x2 c) {
    u32x2 a0 = {a[0], a[1]}, a1 = {a[2], a[3]}, b0 = {b[0], b[1]}, b1 = {b[2], b[3]};
    asm volatile("s_mov_b64 exec, %4\n\tv_mov_b64 %0, %5\n\tv_mov_b64 %1, %5\n\tv_mov_b64 %2, %5\n\tv_mov_b64 %3, %5\n\ts_mov_b64 exec, -1"
                 : "+v"(a0), "+v"(a1), "+v"(b0), "+v"(b1) : "s"(0xF0000000F0000000ull), "v"(c));
    a = (u32x4){a0[0], a0[1], a1[0], a1[1]}; b = (u32x4){b0[0], b0[1], b1[0], b1[1]}; }
template <int ND>
DI void publish_a(f32x16 (&acc)[6], rsrc_t rws, const unsigned (&so)[3], int nq, int lane, bool hi, u32x2 oc) {
#pragma unroll
    for (int i = 0; i < 3; ++i) {
        if (i < 3 - ND && i < nq) {
            asm volatile("" ::: "memory");
#pragma unroll
            for (int k = 0; k < 16; ++k) { acc[i][k] *= QSCALE; acc[3 + i][k] *= QSCALE; }
            PIN(acc[i]); PIN(acc[3 + i]);
        }
        const unsigned pad = i < nq ? 0u : 0x00003F80u;
#pragma unroll
        for (int t = 0; t < 2; ++t) {
            u32x4 v0 = pk8(acc[3 * t + i], 0), v1 = pk8(acc[3 * t + i], 1);
            if (i >= 3 - ND) ones_row(v0, v1, oc); else { v1[2] = hi ? pad : v1[2]; v1[3] = hi ? 0u : v1[3]; }
            st16(rws, v0, lane * 16, so[i] + t * 6144); st16(rws, v1, lane * 16 + 1024, so[i] + t * 6144);
            SBAR();
        }
    }
}
DI void phi_w(bf16x8& f0, u32x4& f1, int hh, int lane, bool hi) {
    const unsigned pb = L_PHI + hh * 1568 + (4 * (lane >> 5) + ((lane & 15) >> 2)) * 56 + ((lane & 19) == 19 ? 0 : 32 * ((lane >> 4) & 1) + 8 * (lane & 3));
    f0 = lds_tr2(pb, pb + 8 * 56);
    f1 = __builtin_bit_cast(u32x4, lds_tr2(pb + 16 * 56, pb + 24 * 56));
    if (hi) { f1[2] = 0; f1[3] = 0; }
}
DI f32x16 phi_pre(u32x4 k0, u32x4 k1, bf16x8 f0, u32x4 f1) {
    f32x16 p = MFMA(f0, asbf(k0), f32x16{});
    return MFMA(asbf(f1), asbf(k1), p);
}

DI void phik_store(const f32x16& p, unsigned ib, float lo) {
    f32x16 e, g;
    feat(p, e, g);
    f32x2 s2 = {e[0], e[1]}, g2 = {g[0], g[1]};
#pragma unroll
    for (int i = 2; i < 12; i += 2) { s2 += (f32x2){e[i], e[i + 1]}; g2 += (f32x2){g[i], g[i + 1]}; }
    s2 = __builtin_elementwise_fma((f32x2){e[12], e[13]} + (f32x2){e[14], e[15]}, (f32x2){lo, lo}, s2);
    g2 = __builtin_elementwise_fma((f32x2){g[12], g[13]} + (f32x2){g[14], g[15]}, (f32x2){lo, lo}, g2);
    const float se = __builtin_amdgcn_rcpf(swap_sum(s2[0] + s2[1])), sg = __builtin_amdgcn_rcpf(swap_sum(g2[0] + g2[1]));
#pragma unroll
    for (int u = 0; u < 8; ++u) {
        const int k = 4 * (u & 3);
        const f32x16& z = (u >> 2) ? g : e; const float sc = (u >> 2) ? sg : se;
        const f32x2 a = (f32x2){z[k], z[k + 1]} * (f32x2){sc, sc}, b = (f32x2){z[k + 2], z[k + 3]} * (f32x2){sc, sc};
        lds_wr64(ib ^ ((8 * (u >> 2) + 2 * (u & 3)) << 3), (u32x2){pk2(a[0], a[1]), pk2(b[0], b[1])});
    }
}
DI unsigned phik_base(int w, int lane) {
    const int tk = lane & 31, pi = (((tk >> 1) & 1) << 3) | (tk & 1) | (((tk >> 2) & 3) << 1);
    return L_R1 + (w >> 1) * 16384 + ((w & 1) * 64 + tk) * 128 + ((pi ^ (lane >> 5)) << 3); }

__global__ __launch_bounds__(512, 2) void fused_fwd(const void*, const void*, const void*, const void*, const void*, const void*, const void*, void*, void*) {
    STAMP_DECL; STAMP(0); STAMPC(30);
    u32x16 ka; u32x2 kb;
    asm volatile("s_load_dwordx16 %0, %2, 0x0\n\ts_load_dwordx2 %1, %2, 0x40" : "=&s"(ka), "=&s"(kb) : "s"(__builtin_amdgcn_kernarg_segment_ptr()));
    const int tid = threadIdx.x, lane = tid & 63, wid = __builtin_amdgcn_readfirstlane(tid >> 6);
    const int b = blockIdx.x, xcd = b & 7, r = b >> 3, w = wid & 3, role = wid >> 2;
    const int isb = r & 1, ml = (r >> 1) & 3, nl = r >> 3;
    const int mt = (xcd & 3) * 4 + ml, nt = (xcd >> 2) * 4 + nl;
    const bool hi = lane >= 32;
    int g0 = ROT == 0 ? 0 : ROT == 1 ? ((ml + nl) & 1) * 4 : ROT == 2 ? 2 * ml + nl : ROT == 3 ? (isb ? ((ml + nl) & 1) * 4 : 0) : ROT == 4 ? (isb ? 2 * ml + nl : ((ml + nl) & 1) * 4) : (isb ? 2 * ml + nl : 0); g0 -= (g0 >= 7) ? 7 : 0;
    const int head = 2 * xcd + (r >> 4), qt = r & 15;
    const int ct = r >> 2, tto = (2 * xcd + (w & 1)) * 4 + (r & 3);
    u32x2 tag;
    bf16x8 wf[16][2];
    unsigned rdy0 = 0;
    unsigned pfd = 0;

    unsigned xvo, wvo[6];
    if (role) {
        __builtin_amdgcn_s_setprio(2);
        xvo = (w * 8 + (lane >> 3)) * 896 + ((((lane & 7) ^ (lane >> 4) ^ ((w & 1) << 2)) & 7) << 4);
        PIN(xvo);
#pragma unroll
        for (int i = 0; i < 6; ++i) {
            const int c = 64 * (w + 4 * i) + lane, rho = (c * 3121) >> 16, ch = c - 21 * rho, win = (ch * 37) >> 8;
            const int krow = (rho & ~15) | ((rho & 3) << 2) | ((rho >> 2) & 3);
            wvo[i] = krow * 2688 + ch * 16 + (isb ? win * (896 - 112) : 0);
            PIN(wvo[i]);
        }
    }
    if (wid == 0) {
        unsigned long long pc; unsigned len;
        asm volatile("s_getpc_b64 %0\n.Lpf_begin%=:\n\ts_mov_b32 %1, .Lpf_end-.Lpf_begin%=" : "=s"(pc), "=s"(len));
        const rsrc_t rc = __builtin_amdgcn_make_buffer_rsrc((void*)pc, 0, len, 0x00020000);
        asm volatile("s_nop 4\n\tbuffer_load_dword %0, %1, %2, 0 offen" : "+v"(pfd) : "v"(lane < 12 ? (r * 12 + lane) * 128 : 0x7FFFFFF0), "s"(rc) : "memory");
        *(LAS unsigned*)(uintptr_t)(L_CNT + 4 * lane) = 0u;
    }
    asm volatile("s_waitcnt lgkmcnt(0)" : "+s"(ka), "+s"(kb) :: "memory");
    const rsrc_t rws = mk_rsrc(kb[0], kb[1], WS_BYTES);
    u32x4 pe[2][4];
    unsigned kva[5], wovo, wt0, wt1, rvo, rfo;
    if (role) {
        const rsrc_t rx = mk_rsrc(isb ? ka[6] : ka[0], isb ? ka[7] : ka[1], 2048 * 896), rw = mk_rsrc(isb ? ka[8] : ka[2], isb ? ka[9] : ka[3], 448 * 2688);
        const unsigned xs0 = mt * 114688, ws0 = (isb ? 112 : 336) * nt;
        auto feed = [&](int g, auto PX, auto PW) {
            int ga = g + g0; ga -= (ga >= 7) ? 7 : 0;
            const unsigned ws = ws0 + ga * 64 * 2688, l = (g & 3) * SLOT + w * 1024;
            dma_x4<decltype(PX)::value>(rx, xvo, xs0 + ga * 128, l);
            dma_w5<decltype(PW)::value>(rw, wvo, ws, l + 16384);
            if (w == 0) dma(rw, wvo[5], ws, l + 16384 + 5 * 4096); };
        const unsigned lw = L_CNT + 4 * (C_LAND0 + w);
        constexpr std::integral_constant<int, 0> fast{};
        FSTAMP(18);
        dma(mk_rsrc(ka[10], ka[11], 16 * 1568), lane * 16, nt * 3136 + w * 1024, L_PHI + w * 1024);
        feed(0, fast, fast);
        if (LATE1 == 0) feed(1, fast, fast);
        asm volatile("s_barrier" ::: "memory");
        if (LATE1 == 1) { WAIT_VM(4); feed(1, fast, fast); }
        if (LATE1 == 2) { WAIT_VM(0); *(volatile LAS unsigned*)(uintptr_t)lw = 1; feed(1, fast, fast); }
        if (w == 0) WAIT_VM(10); else WAIT_VM(9);
        *(volatile LAS unsigned*)(uintptr_t)lw = 1;
        STAMP(1);
#pragma unroll 1
        for (int g = 1; g < 6; ++g) {
            if (g >= 3) wait4(C_DONE0, g - 2);
            if (g < FASTG) feed(g + 1, fast, fast); else feed(g + 1, std::integral_constant<int, PNX>{}, std::integral_constant<int, PNW>{});
            if (w == 0) WAIT_VM(10); else WAIT_VM(9);
            *(volatile LAS unsigned*)(uintptr_t)lw = g + 1;
        }
        {
            const rsrc_t rt_ = mk_rsrc(w < 2 ? ka[4] : ka[12], w < 2 ? ka[5] : ka[13], 448 * 896);
            asm volatile("s_mov_b32 m0, %0\n\ts_nop 3\n\tbuffer_load_dword %1, %2, 0 offen lds"
                         :: "s"(L_JUNK), "v"(lane < 49 ? (r * 98 + (w & 1) * 49 + lane) * 128 : 0x7FFFFF00), "s"(rt_) : "memory");
        }
        WAIT_VM(1);
        *(volatile LAS unsigned*)(uintptr_t)lw = 8;
        STAMP(2);
        tag = rd_tag();
        wovo = (lane >> 3) * 896 + (((lane & 7) ^ (((lane >> 4) & 1) << 2)) << 4);
        wt0 = tr_addr128(L_R0, 0, (w >> 1) * 224 + 4 * (lane >> 5), lane); wt1 = tr_addr128(L_R0, 64, (w >> 1) * 224 + 4 * (lane >> 5), lane);
        rvo = (lane >> 4) * 32768 + (16 * (qt & 3) + (lane & 15)) * 16; rfo = WS_F2 + ((head * 16 + (lane & 15)) * 2 + (qt >> 3)) * 128;
        PIN(wovo); PIN(wt0); PIN(wt1); PIN(rvo); PIN(rfo);
        if (isb) {
            bf16x8 ph0; u32x4 ph1;
            wait4(C_LAND0, 1);
            phi_w(ph0, ph1, w >> 1, lane, hi);
            unsigned ib = phik_base(w, lane), qa = L_QB + w * 8192 + lane * 16;
            float lo = hi ? 0.f : 1.f;
            {
                const int q = (lane & 15) >> 2, P = 8 * (w & 1) + 4 * ((lane >> 4) & 1) + (lane & 3);
#pragma unroll
                for (int i = 0; i < 4; ++i) {
                    const int t = 8 * i + 4 * (lane >> 5) + q, pt = (((t >> 1) & 1) << 3) | (t & 1) | (((t >> 2) & 3) << 1);
                    kva[i] = L_R1 + (w >> 1) * 16384 + t * 128 + ((P ^ pt) << 3); PIN(kva[i]);
                }
                kva[4] = L_R0 + (w >> 1) * 8192 + lane * 16; PIN(kva[4]);
            }
            PIN(ib); PIN(qa); PIN(lo); PIN(ph0); PIN(ph1);
            waitc<1>(C_QB + w, 1);
            f32x16 pp[4];
#pragma unroll
            for (int t = 0; t < 4; ++t) pp[t] = phi_pre(lds_rd128(qa + t * 2048), lds_rd128(qa + t * 2048 + 1024), ph0, ph1);
            phik_store(pp[0], ib, lo); phik_store(pp[1], ib + 4096, lo);
            WAIT_LGKM0(); sig(C_PV, lane);
#pragma unroll
            for (int t = 0; t < 2; ++t) {
                f32x16 e, g;
                feat(pp[2 + t], e, g);
                pe[t][0] = pk8(e, 0); pe[t][1] = pk8(e, 1); pe[t][2] = pk8(g, 0); pe[t][3] = pk8(g, 1);
            }
        }
        STAMP(3);
    } else {
        f32x16 acc[6];
        const int tp = w & 1, sg = w >> 1;
        const int rr = tp * 64 + (lane & 31);
        const unsigned xa0 = rr * 128 + ((((lane >> 5) ^ (rr >> 1)) & 7) << 4);
        const unsigned lb = 16384 + (4 * ((lane & 15) >> 2) + 2 * (lane >> 5)) * 336 + 32 * ((lane >> 4) & 1) + 8 * (lane & 3);
        const unsigned wst = isb ? 112 : 56, wa0 = lb + 56 * (isb ? sg : 3 * sg), wb0 = wa0 + wst, wc0 = wb0 + wst;
        const int G = 2 * nt + sg;
        if (w == 0) {
            const u32x2 ep = ldflag(rws, WS_EPOCH);
            const unsigned long long e = (((unsigned long long)ep[1] << 32) | ep[0]) + 1;
            lds_wr64(L_TAG, (u32x2){(unsigned)e, (unsigned)(e >> 32)});
            WAIT_VM(0);
            asm volatile("" :: "v"(pfd));
        }
        unsigned so[3];
#pragma unroll
        for (int i = 0; i < 3; ++i) { const int sl = 3 * G + i; so[i] = WS_QKV + (((sl & 15) * 64 + mt * 4 + 2 * tp) * 6 + (sl >> 4) * 2) * 1024; asm volatile("" : "+s"(so[i])); }
        const int nq = 16 - 3 * G;
        u32x2 oc; oc[0] = oc[1] = (lane & 31) == 28 ? 0x3F803F80u : 0u; PIN(oc);
        auto start = [&]() { asm volatile("s_waitcnt lgkmcnt(0)\n\ts_barrier" ::: "memory"); tag = rd_tag(); asm volatile("" : "+s"(tag)); wait4(C_LAND0, 1); STAMP(1); STAMPC(23); };
        if (isb || G == 10) { gemm6<1>(xa0, wa0, wb0, wc0, w, acc, start); STAMPC(24); STAMP(2); if (!isb) publish_a<1>(acc, rws, so, nq, lane, hi, oc); }
        else if (G < 10) { gemm6<0>(xa0, wa0, wb0, wc0, w, acc, start); STAMPC(24); STAMP(2); publish_a<0>(acc, rws, so, nq, lane, hi, oc); }
        else { gemm6<3>(xa0, wa0, wb0, wc0, w, acc, start); STAMPC(24); STAMP(2); publish_a<3>(acc, rws, so, nq, lane, hi, oc); }
        if (!isb) {
            FSTAMP(15);
            post(rws, WS_F1 + (G * 32 + mt * 2 + tp) * 128, tag, lane);
            wait4(C_DONE0, 7);
        } else {
            u32x4 fr[2][3][2];
#pragma unroll
            for (int t = 0; t < 2; ++t) {
#pragma unroll
                for (int i = 0; i < 3; ++i) { fr[t][i][0] = pk8(acc[3 * t + i], 0); fr[t][i][1] = pk8(acc[3 * t + i], 1); }
                ones_row(fr[t][2][0], fr[t][2][1], oc);
            }
            wait4(C_DONE0, 7);
#pragma unroll
            for (int t = 0; t < 2; ++t)
#pragma unroll
                for (int s = 0; s < 2; ++s) {
                    lds_wr128(L_QB + w * 8192 + t * 2048 + s * 1024 + lane * 16, fr[t][1][s]);
                    lds_wr128(L_QB + w * 8192 + 4096 + t * 2048 + s * 1024 + lane * 16, fr[t][0][s]);
                    lds_wr128(L_R0 + ((sg * 4 + 2 * tp + t) * 2 + s) * 1024 + lane * 16, fr[t][2][s]);
                }
            WAIT_LGKM0(); sig(C_QB + w, lane);
        }
        STAMP(3);
    }
    FSTAMP(4);

    if (role) {

        if (isb) {
            waitc<0>(C_PV, 4);
            const int hh = w >> 1, ft = w & 1;
            bf16x8 fa[8], fb[8];
#pragma unroll
            for (int st = 0; st < 8; ++st) {
                fa[st] = lds_tr2(kva[2 * (st & 1)] + (st >> 1) * 4096, kva[2 * (st & 1) + 1] + (st >> 1) * 4096);
                fb[st] = asbf(lds_rd128(kva[4] + st * 1024));
            }
            SBAR();
            f32x16 kv = {};
#pragma unroll
            for (int st = 0; st < 8; ++st) kv = MFMA(fa[st], fb[st], kv);
            WAIT_LGKM0(); sig(C_KVDONE, lane);
            const int hk = 2 * nt + hh;
#pragma unroll
            for (int u = 0; u < 4; ++u)
                st16(rws, __builtin_bit_cast(u32x4, (f32x4){kv[4 * u], kv[4 * u + 1], kv[4 * u + 2], kv[4 * u + 3]}), lane * 16 + u * 1024, WS_KVP + ((hk * 16 + mt) * 8 + ft * 4) * 1024);
            FSTAMP(19);
            post(rws, WS_F2 + ((hk * 16 + mt) * 2 + ft) * 128, tag, lane);
            waitc<0>(C_KVDONE, 4);
        }
        STAMP(5);
        if (r < 28) {
            waitc<HSLP>(C_QX, 4);
            if (w == 0) *(LAS u32x2*)(uintptr_t)(L_R0 + 57344 + 8 * lane) = (u32x2){0u, 0u};
            const rsrc_t rwob = mk_rsrc(ka[12], ka[13], 448 * 896);
#pragma unroll
            for (int i = 0; i < 14; ++i) { WAIT_VM(WOWIN); dma(rwob, wovo, ct * 128 + (w + 4 * i) * 8 * 896, L_R0 + (w + 4 * i) * 1024); }
            WAIT_VM(0); WAIT_LGKM0(); sig(C_WO0 + 1, lane);
        }
        if (w == 3) {
            const int hr = head, i = lane & 15;
            poll<HSLP>(rws, rfo, tag);
            u32x4 pl[8];
#pragma unroll
            for (int p = 0; p < 4; ++p) {
                const unsigned so = WS_KVP + hr * 131072 + (qt >> 2) * 2048 + p * 8192;
                pl[2 * p] = ld16(rws, rvo, so); pl[2 * p + 1] = ld16(rws, rvo + 1024, so);
            }
            SBAR();
            f32x4 s0 = {}, s1 = {};
#pragma unroll
            for (int p = 0; p < 4; ++p) { s0 += __builtin_bit_cast(f32x4, pl[2 * p]); s1 += __builtin_bit_cast(f32x4, pl[2 * p + 1]); }
            float v[8] = {s0[0], s0[1], s0[2], s0[3], s1[0], s1[1], s1[2], s1[3]};
#pragma unroll
            for (int k = 0; k < 8; ++k) {
                auto t = __builtin_amdgcn_permlane16_swap(__float_as_uint(v[k]), __float_as_uint(v[k]), false, false);
                v[k] = swap_sum(__uint_as_float(t[0]) + __uint_as_float(t[1]));
            }
            u32x4 o = {pk2(v[0], v[1]), pk2(v[2], v[3]), pk2(v[4], v[5]), pk2(v[6], v[7])};
            const int cl = 16 * (qt & 3) + i;
            if ((cl & 31) == 29) o = (u32x4){0x3F803F80u, 0x3F803F80u, 0x3F803F80u, 0x3F803F80u};
            if (((qt >> 2) & 1) && cl >= 32) { o[2] = 0u; o[3] = 0u; }
            st16(rws, o, lane < 16 ? i * 16 : 0x7FFFFF00u, WS_KVR + (hr * 256 + qt * 16) * 16);
            post(rws, WS_F3 + (hr * 16 + qt) * 128, tag, lane);
        }
        STAMP(6);
        if (r < 28) {
            waitc<4>(C_WO0 + 1, 4);
            read_wo(wf, wt0, wt1, 0);
            WAIT_LGKM0(); sig(C_WOFREE, lane);
        }
        if (isb) {
            const int hb_ = 2 * nt + (w >> 1), tb = mt * 4 + 2 * (w & 1);
            poll<HSLP>(rws, WS_F3 + (hb_ * 16 + (lane & 15)) * 128, tag);
            u32x4 kf[4];
#pragma unroll
            for (int f = 0; f < 4; ++f) kf[f] = ld16(rws, lane * 16 + f * 1024, WS_KVR + hb_ * 4096);
            SBAR();
#pragma unroll
            for (int t = 0; t < 2; ++t) {
                f32x16 np = MFMA(asbf(kf[0]), asbf(pe[t][0]), f32x16{}); np = MFMA(asbf(kf[1]), asbf(pe[t][1]), np);
                f32x16 nm = MFMA(asbf(kf[2]), asbf(pe[t][2]), f32x16{}); nm = MFMA(asbf(kf[3]), asbf(pe[t][3]), nm);
                const float rp = __builtin_amdgcn_rcpf(np[13]), rm = __builtin_amdgcn_rcpf(nm[13]);
                const float rd = __builtin_amdgcn_rcpf(__builtin_fmaf(rp, np[12], rm * nm[12]) + 1e-6f);
                const float cp = from_hi(rp * rd), cm = from_hi(rm * rd);
                f32x16 o;
#pragma unroll
                for (int i = 0; i < 16; ++i) o[i] = __builtin_fmaf(cp, np[i], cm * nm[i]);
                u32x4 o0 = pk8(o, 0), o1 = pk8(o, 1);
                if (hi) { o1[2] = 0; o1[3] = 0; }
                const unsigned so = WS_O + (2u << 20) + (((tb + t) * 16 + hb_) * 2) * 1024;
                st16(rws, o0, lane * 16, so); st16(rws, o1, lane * 16 + 1024, so);
            }
            WAIT_VM(0);
            if (lane < 2) __builtin_amdgcn_raw_buffer_store_b64(tag, rws, WS_FO + (1024 + (tb + lane) * 16 + hb_) * 128, 0, 16);
            CBAR();
        }
        STAMP(7);
        if (r < 28) {
            const rsrc_t rwoa = mk_rsrc(ka[4], ka[5], 448 * 896);
            waitc<HSLP>(C_WOFREE, 4);
#pragma unroll
            for (int i = 0; i < 14; ++i) { WAIT_VM(WOWIN); dma(rwoa, wovo, ct * 128 + (w + 4 * i) * 8 * 896, L_R0 + (w + 4 * i) * 1024); }
            WAIT_VM(0); sig(C_WO0, lane);
        }
        if (b == 255 && w == 0) {
#pragma unroll 1
            for (int i = 0; i < 4; ++i) poll<HSLP>(rws, WS_F3 + (i * 64 + lane) * 128, tag);
            if (lane == 0) __builtin_amdgcn_raw_buffer_store_b64(tag, rws, WS_EPOCH, 0, 16);
        }
    } else {
        const int tq = qt * 4 + w;
        const int gq = (head * 43) >> 7, gk = ((16 + head) * 43) >> 7, gv = ((32 + head) * 43) >> 7;
        const int rk0 = qt >> 2;
        const unsigned frk = WS_F1 + (((lane & 2) ? gv : gk) * 32 + 8 * w + (lane & 1)) * 128;
        __builtin_amdgcn_s_setprio(3);
        f32x16 O[4] = {};
        u32x4 pA0 = {}, pA1 = {}, pB0, pB1;
        u32x4 kA[4], kB[4], kC[4], kD[4];
        kD[2] = (u32x4){}; kD[3] = (u32x4){};
        PIN(O[0]); PIN(O[1]); PIN(O[2]); PIN(O[3]); PIN(pA0); PIN(pA1); PIN(kD[2]); PIN(kD[3]);
        const unsigned hb = WS_QKV + head * 64 * 6144, kvb = hb + 2048;
        unsigned kb0 = kvb + (16 * w + (qt & 3)) * 6144, kb1 = kvb + (16 * w + ((qt + 1) & 3)) * 6144,
                 kb2 = kvb + (16 * w + ((qt + 2) & 3)) * 6144, kb3 = kvb + (16 * w + ((qt + 3) & 3)) * 6144, qb = hb + tq * 6144;
        unsigned vc = lane * 16 + rk0 * 24576;
        asm volatile("" : "+s"(kb0), "+s"(kb1), "+s"(kb2), "+s"(kb3), "+s"(qb), "+v"(vc));
        u32x2 pf0, pf1, pf2;
        poll_fast(rws, WS_F1 + (lane >= 4 ? gq * 32 + (tq >> 1) : ((lane & 2) ? gv : gk) * 32 + 8 * w + 2 * rk0 + (lane & 1)) * 128, tag, pf0, pf1, pf2);
        STAMP(5);
#define LD(d, f, v, sb) d[f] = ld16(rws, (v) + (f) * 1024, sb)
        u32x4 q0 = ld16(rws, lane * 16, qb), q1 = ld16(rws, lane * 16 + 1024, qb);
        LD(kA, 0, vc, kb0); LD(kA, 1, vc, kb0);
        SBAR();
        bf16x8 Q[4][2];
        {
            f32x16 s = MFMA(asbf(kA[0]), asbf(q0), f32x16{}); s = MFMA(asbf(kA[1]), asbf(q1), s);
            SBAR(); FSTAMP(13);
            asm volatile("" :: "v"(pf0), "v"(pf1), "v"(pf2));
            LD(kA, 2, vc, kb0); LD(kA, 3, vc, kb0); LD(kB, 0, vc, kb1); LD(kB, 1, vc, kb1); LD(kB, 2, vc, kb1); LD(kB, 3, vc, kb1);
            LD(kC, 0, vc, kb2); LD(kC, 1, vc, kb2); LD(kC, 2, vc, kb2); LD(kC, 3, vc, kb2);
            SBAR();
            lds_wr128(L_QX + (w * 2) * 1024 + lane * 16, q0);
            const float m = swap_max(max16(s)) + REF_SHIFT;
            u32x4 q1m = q1;
            if (hi) q1m[2] = pk2(-m, 0.f);
            lds_wr128(L_QX + (w * 2 + 1) * 1024 + lane * 16, q1m);
            WAIT_LGKM0(); sig(C_QX, lane); FSTAMP(14); waitc<0>(C_QX, 4);
#pragma unroll
            for (int a = 0; a < 3; ++a) { const unsigned qa = L_QX + ((w + 1 + a) & 3) * 2048 + lane * 16; Q[a][0] = asbf(lds_rd128(qa)); Q[a][1] = asbf(lds_rd128(qa + 1024)); }
            Q[3][0] = asbf(q0); Q[3][1] = asbf(q1m);
        }
        u32x2 fl = ldflag(rws, frk + ((rk0 + 1) & 3) * 256);
        STAMP(6); STAMPC(20);
        __builtin_amdgcn_s_setprio(0);
        f32x16 sA, sB;
        sA = MFMA(asbf(kA[0]), Q[0][0], f32x16{}); sA = MFMA(asbf(kA[1]), Q[0][1], sA);
        PIN(Q[1][0]); PIN(Q[1][1]); PIN(Q[2][0]); PIN(Q[2][1]); PIN(Q[3][0]); PIN(Q[3][1]);
#define EX4(S, i) S[i] = __builtin_amdgcn_exp2f(S[i]); S[i + 1] = __builtin_amdgcn_exp2f(S[i + 1]); S[i + 2] = __builtin_amdgcn_exp2f(S[i + 2]); S[i + 3] = __builtin_amdgcn_exp2f(S[i + 3])
#define STAGE(SC, SN, PC0, PC1, PP0, PP1, KN, an, VP, ap, F0, F1, F2, F3) do { \
        SN = MFMA(asbf(KN[0]), Q[an][0], f32x16{}); SBAR(); F0; SBAR(); EX4(SC, 0); PIN(SC); SBAR(); \
        SN = MFMA(asbf(KN[1]), Q[an][1], SN); SBAR(); F1; SBAR(); EX4(SC, 4); PC0[0] = pk2(SC[0], SC[1]); PC0[1] = pk2(SC[2], SC[3]); PIN(SC); PIN(PC0); SBAR(); \
        O[ap] = MFMA(asbf(VP[2]), asbf(PP0), O[ap]); SBAR(); F2; SBAR(); EX4(SC, 8); PC0[2] = pk2(SC[4], SC[5]); PC0[3] = pk2(SC[6], SC[7]); PIN(SC); PIN(PC0); SBAR(); \
        O[ap] = MFMA(asbf(VP[3]), asbf(PP1), O[ap]); SBAR(); F3; SBAR(); EX4(SC, 12); PC1[0] = pk2(SC[8], SC[9]); PC1[1] = pk2(SC[10], SC[11]); \
        PC1[2] = pk2(SC[12], SC[13]); PC1[3] = pk2(SC[14], SC[15]); PIN(PC1); SBAR(); } while (0)
#define NOP (void)0
#define ARRIVE(K) asm volatile("" : "+v"(K[0]), "+v"(K[1]), "+v"(K[2]), "+v"(K[3]))
#define TILE(KP, KC, KN, V, SB, G0) do { \
        STAGE(sA, sB, pB0, pB1, pA0, pA1, KC, 1, KP, 3, G0, NOP, NOP, NOP); \
        STAGE(sB, sA, pA0, pA1, pB0, pB1, KC, 2, KC, 0, LD(KP, 0, V, SB), LD(KP, 1, V, SB), LD(KP, 2, V, SB), LD(KP, 3, V, SB)); \
        STAGE(sA, sB, pB0, pB1, pA0, pA1, KC, 3, KC, 1, NOP, NOP, NOP, ARRIVE(KN)); \
        STAGE(sB, sA, pA0, pA1, pB0, pB1, KN, 0, KC, 2, NOP, NOP, NOP, NOP); } while (0)
#define TILN(KP, KC, KN) do { \
        STAGE(sA, sB, pB0, pB1, pA0, pA1, KC, 1, KP, 3, NOP, NOP, NOP, NOP); \
        STAGE(sB, sA, pA0, pA1, pB0, pB1, KC, 2, KC, 0, NOP, NOP, NOP, NOP); \
        STAGE(sA, sB, pB0, pB1, pA0, pA1, KC, 3, KC, 1, NOP, NOP, NOP, ARRIVE(KN)); \
        STAGE(sB, sA, pA0, pA1, pB0, pB1, KN, 0, KC, 2, NOP, NOP, NOP, NOP); } while (0)
#pragma unroll 1
        for (int it = 0; it < 3; ++it) {
            const unsigned vn = lane * 16 + ((rk0 + it + 1) & 3) * 24576;
            const unsigned fn = frk + ((rk0 + it + 1) & 3) * 256, f2 = frk + ((rk0 + it + 2) & 3) * 256;
            TILE(kD, kA, kB, vc, kb3, NOP);
            if (!tagok(fl, tag)) poll<1>(rws, fn, tag);
            CBAR();
            TILE(kA, kB, kC, vn, kb0, NOP);
            TILE(kB, kC, kD, vn, kb1, NOP);
            TILE(kC, kD, kA, vn, kb2, fl = ldflag(rws, f2));
            vc = vn;
        }
        unsigned cw[3];
#pragma unroll
        for (int a = 0; a < 3; ++a) { cw[a] = L_R2 + (((w + 1 + a) & 3) * 3 + 2 - a) * 4096 + lane * 16; PIN(cw[a]); }
#define SND(a, u) lds_wrf4(cw[a] + (u) * 1024, (f32x4){O[a][4 * (u)], O[a][4 * (u) + 1], O[a][4 * (u) + 2], O[a][4 * (u) + 3]})
        TILE(kD, kA, kB, vc, kb3, NOP);
        TILN(kA, kB, kC);
        TILN(kB, kC, kD);
        STAGE(sA, sB, pB0, pB1, pA0, pA1, kD, 1, kC, 3, NOP, NOP, NOP, NOP);
        STAGE(sB, sA, pA0, pA1, pB0, pB1, kD, 2, kD, 0, NOP, NOP, NOP, NOP);
        STAGE(sA, sB, pB0, pB1, pA0, pA1, kD, 3, kD, 1, NOP, SND(0, 0), SND(0, 1), SND(0, 2));
        O[2] = MFMA(asbf(kD[2]), asbf(pB0), O[2]); SBAR(); SND(0, 3); SBAR(); EX4(sB, 0); EX4(sB, 4); PIN(sB); SBAR();
        O[2] = MFMA(asbf(kD[3]), asbf(pB1), O[2]); SBAR(); SND(1, 0); SBAR();
        pA0 = pk8(sB, 0); PIN(pA0); SBAR(); SND(1, 1); SBAR(); EX4(sB, 8); PIN(sB); SBAR();
        O[3] = MFMA(asbf(kD[2]), asbf(pA0), O[3]); SBAR(); SND(1, 2); SBAR(); EX4(sB, 12); PIN(sB); SBAR(); SND(1, 3); SBAR();
        pA1 = pk8(sB, 1); PIN(pA1); SBAR(); SND(2, 0); SBAR();
        O[3] = MFMA(asbf(kD[3]), asbf(pA1), O[3]); SBAR();
        SND(2, 1); SND(2, 2); SND(2, 3);
        STAMP(7); STAMPC(21);
        f32x16 o;
        {
            WAIT_LGKM0(); sig(C_CMB, lane); waitc<0>(C_CMB, 4);
            f32x4 pr[12];
#pragma unroll
            for (int k = 0; k < 12; ++k) pr[k] = lds_rdf4(L_R2 + (w * 12 + k) * 1024 + lane * 16);
            SBAR();
#pragma unroll
            for (int u = 0; u < 4; ++u) {
                const f32x4 t = (pr[u] + (f32x4){O[3][4 * u], O[3][4 * u + 1], O[3][4 * u + 2], O[3][4 * u + 3]}) + (pr[4 + u] + pr[8 + u]);
                o[4 * u] = t[0]; o[4 * u + 1] = t[1]; o[4 * u + 2] = t[2]; o[4 * u + 3] = t[3];
            }
        }
        float l = from_hi(o[12]);
        {
            f32x2 chk = {0.f, 0.f};
#pragma unroll
            for (int i = 0; i < 16; i += 2) chk = __builtin_elementwise_fma((f32x2){o[i], o[i + 1]}, (f32x2){0.f, 0.f}, chk);
            bool bad = !(chk[0] + chk[1] == 0.f) || !(l >= 1e-32f && l <= 1e30f);
            if (__any(bad)) {
                float mrun = -3.0e38f;
                o = f32x16{};
#pragma unroll 1
                for (int k2 = 0; k2 < 4; ++k2) {
                    poll(rws, WS_F1 + (((lane & 8) ? gv : gk) * 32 + 8 * k2 + (lane & 7)) * 128, tag);
#pragma unroll 1
                    for (int m2 = 0; m2 < 16; ++m2) {
                        const unsigned so = kvb + (k2 * 16 + m2) * 6144;
                        const u32x4 k0 = ld16(rws, lane * 16, so), k1 = ld16(rws, lane * 16 + 1024, so), v0 = ld16(rws, lane * 16 + 2048, so), v1 = ld16(rws, lane * 16 + 3072, so);
                        f32x16 s = MFMA(asbf(k0), asbf(q0), f32x16{}); s = MFMA(asbf(k1), asbf(q1), s);
                        const float mn = __builtin_fmaxf(mrun, swap_max(max16(s))), al = __builtin_amdgcn_exp2f(mrun - mn);
                        mrun = mn;
#pragma unroll
                        for (int i = 0; i < 16; ++i) { s[i] = __builtin_amdgcn_exp2f(s[i] - mn); o[i] *= al; }
                        o = MFMA(asbf(v0), asbf(pk8(s, 0)), o); o = MFMA(asbf(v1), asbf(pk8(s, 1)), o);
                    }
                }
                l = from_hi(o[12]);
            }
        }
        const float rl = __builtin_amdgcn_rcpf(l);
#pragma unroll
        for (int i = 0; i < 16; ++i) o[i] *= rl;
        u32x4 o0 = pk8(o, 0), o1 = pk8(o, 1);
        if (hi) { o1[2] = 0; o1[3] = 0; }
        const unsigned so = WS_O + ((tq * 16 + head) * 2) * 1024;
        st16(rws, o0, lane * 16, so); st16(rws, o1, lane * 16 + 1024, so);
        sig(C_ATT, lane);
        STAMP(8); FSTAMPC(27);
        const u32x2 fl0 = ldflag(rws, WS_FO + (tto * 16 + (w >> 1) * 8 + ((lane + ct) & 7)) * 128);
        SBAR();
        WAIT_VM(0);
        rdy0 = ready8(fl0, tag);
        asm volatile("" : "+s"(rdy0) :: "memory");
        if (lane == 0) __builtin_amdgcn_raw_buffer_store_b64(tag, rws, WS_FO + (tq * 16 + head) * 128, 0, 16);
        CBAR();
        if (r < 28) { waitc<0>(C_WO0, 4); read_wo(wf, tr_addr128(L_R0, 0, (w >> 1) * 224 + 4 * (lane >> 5), lane), tr_addr128(L_R0, 64, (w >> 1) * 224 + 4 * (lane >> 5), lane), ct); }
        FSTAMP(9);
    }

    if (r < 28) {
        const int br = role, tp = w & 1, kh = w >> 1;
        const rsrc_t rout = mk_rsrc(ka[14], ka[15], 2048 * 1792);
        const unsigned ffo = WS_FO + (br * 1024 + tto * 16 + kh * 8 + ((lane + ct) & 7)) * 128;
        const unsigned ob = WS_O + br * (2u << 20) + (tto * 16 + kh * 8) * 2048;
        u32x4 of[16];
        f32x16 c0 = {}, c1 = {};
        if (role) {
            poll<HSLP>(rws, ffo, tag);
#pragma unroll
            for (int k = 0; k < 16; ++k) {
                if (k == 8) WAIT_VM(GWIN);
                of[k] = ld16(rws, lane * 16 + (k & 3) * 1024, ob + (k >> 2) * 4096); SBAR();
            }
            waitc<4>(C_ATT, 4);
            __builtin_amdgcn_s_setprio(0);
#pragma unroll
            for (int k = 0; k < 16; ++k) { c0 = MFMA(wf[k][0], asbf(of[k]), c0); c1 = MFMA(wf[k][1], asbf(of[k]), c1); }
        } else {
            unsigned req = 0, fin = 0, rdy = rdy0;
#pragma unroll
            for (int k = 0; k < 16; ++k) asm volatile("" : "=v"(of[k]));
#pragma clang loop unroll(disable)
            for (unsigned it = 0; it < SPIN_CAP; ++it) {
                const unsigned nw = rdy & ~req;
#pragma unroll
                for (int k = 0; k < 8; ++k)
                    if (nw & (1u << k)) { const unsigned sb = ob + ((k + ct) & 7) * 2048; of[2 * k] = ld16(rws, lane * 16, sb); of[2 * k + 1] = ld16(rws, lane * 16 + 1024, sb); CBAR(); }
                req |= nw;
                const u32x2 f = ldflag(rws, ffo);
                CBAR();
                const unsigned todo = (req ^ (req + 1u)) >> 1 & ~fin;
#pragma unroll
                for (int k = 0; k < 8; ++k)
                    if (todo & (1u << k)) {
                        c0 = MFMA(wf[2 * k][0], asbf(of[2 * k]), c0); c1 = MFMA(wf[2 * k][1], asbf(of[2 * k]), c1);
                        c0 = MFMA(wf[2 * k + 1][0], asbf(of[2 * k + 1]), c0); c1 = MFMA(wf[2 * k + 1][1], asbf(of[2 * k + 1]), c1); CBAR();
                    }
                fin |= todo;
                if (fin == 255u) break;
                rdy = ready8(f, tag);
                CBAR();
            }
            sig(C_GATH, lane);
        }
        STAMP(10);
        const unsigned xb_ = L_R1 + br * 16384;
        const unsigned vo = (tto * 32 + (lane & 31)) * 1792 + (hi ? 16 : 0), so = br * 896 + ct * 128 + kh * 64;
#define FINISH(KEEP, GIVE) do { \
            _Pragma("unroll") for (int u = 0; u < 4; ++u) lds_wrf4(xb_ + (w * 4 + u) * 1024 + lane * 16, (f32x4){GIVE[4 * u], GIVE[4 * u + 1], GIVE[4 * u + 2], GIVE[4 * u + 3]}); \
            WAIT_LGKM0(); sig(C_EX0 + br * 2 + tp, lane); waitc<0>(C_EX0 + br * 2 + tp, 2); \
            _Pragma("unroll") for (int u = 0; u < 4; ++u) { const f32x4 s = lds_rdf4(xb_ + ((w ^ 2) * 4 + u) * 1024 + lane * 16); \
                _Pragma("unroll") for (int i = 0; i < 4; ++i) KEEP[4 * u + i] += s[i]; } \
            if (role) waitc<1>(C_GATH, 4); \
            _Pragma("unroll") for (int gp = 0; gp < 2; ++gp) { \
                auto s0 = __builtin_amdgcn_permlane32_swap(pk2(KEEP[8 * gp], KEEP[8 * gp + 1]), pk2(KEEP[8 * gp + 4], KEEP[8 * gp + 5]), false, false); \
                auto s1 = __builtin_amdgcn_permlane32_swap(pk2(KEEP[8 * gp + 2], KEEP[8 * gp + 3]), pk2(KEEP[8 * gp + 6], KEEP[8 * gp + 7]), false, false); \
                st16a<0>(rout, (u32x4){s0[0], s1[0], s0[1], s1[1]}, vo + gp * 32, so); } } while (0)
        if (kh) FINISH(c1, c0); else FINISH(c0, c1);
        STAMP(12);
    }
    STAMP_FLUSH();
    asm volatile(".Lpf_end:");
}

extern "C" void kernel_launch(void* const* d_in, const int* in_sizes, int n_in, void* d_out, int out_size, void* d_ws, size_t ws_size,
                              hipStream_t stream) {
    (void)in_sizes; (void)n_in; (void)out_size; (void)ws_size;
    (void)hipFuncSetAttribute((const void*)fused_fwd, hipFuncAttributeMaxDynamicSharedMemorySize, LDS_BYTES);
    fused_fwd<<<256, 512, LDS_BYTES, stream>>>(d_in[0], d_in[1], d_in[2], d_in[3], d_in[4], d_in[5], d_in[6], d_out, d_ws);
}
```

```cpp
#define STAMPS 1
#include <hip/hip_runtime.h>
#include <hip/hip_bf16.h>
#include <stdint.h>
#include <type_traits>

typedef __attribute__((ext_vector_type(8))) __bf16 bf16x8;
typedef __attribute__((ext_vector_type(2))) __bf16 bf16x2;
typedef __attribute__((ext_vector_type(2))) float f32x2;
typedef __attribute__((ext_vector_type(4))) float f32x4;
typedef __attribute__((ext_vector_type(16))) float f32x16;
typedef __attribute__((ext_vector_type(4))) unsigned u32x4;
typedef __attribute__((ext_vector_type(2))) unsigned u32x2;
typedef __attribute__((ext_vector_type(4))) short s16x4;
typedef __amdgpu_buffer_rsrc_t rsrc_t;
#define LAS __attribute__((address_space(3)))
#define DI __device__ __forceinline__

constexpr unsigned WS_EPOCH = 0;
constexpr unsigned WS_F1 = 4096;
constexpr unsigned WS_F2 = WS_F1 + 1024 * 128;
constexpr unsigned WS_F3 = WS_F2 + 512 * 128;
constexpr unsigned WS_FO = WS_F3 + 256 * 128;
constexpr unsigned WS_QKV = 1u << 20;
constexpr unsigned WS_KVP = WS_QKV + (6u << 20);
constexpr unsigned WS_KVR = WS_KVP + (2u << 20);
constexpr unsigned WS_O = WS_KVR + (1u << 20);
constexpr unsigned WS_DBG = WS_O + (4u << 20);
constexpr unsigned WS_BYTES = WS_DBG + (1u << 20);

constexpr unsigned SLOT = 37888;
constexpr unsigned L_R0 = 0;
constexpr unsigned L_R1 = 57856;
constexpr unsigned L_R2 = L_R1 + 32768;
constexpr unsigned L_QX = L_R2 + 49152;
constexpr unsigned L_QB = L_R2;
constexpr unsigned L_PHI = 4 * SLOT + 64;
constexpr unsigned L_JUNK = L_PHI + 4096;
constexpr unsigned L_CNT = L_JUNK + 1024;
constexpr unsigned L_TAG = L_CNT + 256;
constexpr unsigned LDS_BYTES = L_TAG + 16;
enum { C_LAND0 = 24, C_DONE0 = 32, C_QB = 4, C_QX = 10, C_PV = 11, C_KVDONE = 12, C_WO0 = 13, C_WOFREE = 15, C_EX0 = 16, C_CMB = 20, C_ATT = 21, C_GATH = 22 };

#define MAP_SEP 0
#define PNX 3
#define PNW 3
#define FASTG 2
#define ROT 1
#define LATE1 0
#define HSLP 8
#define WOWIN 3
#define GWIN 0
constexpr unsigned SPIN_CAP = 1u << 16;
constexpr float QSCALE = 0.18898223650461362f * 1.4426950408889634f;
constexpr float LOG2E = 1.4426950408889634f;
constexpr float REF_SHIFT = 100.f;

#define MFMA(a, b, c) __builtin_amdgcn_mfma_f32_32x32x16_bf16((a), (b), (c), 0, 0, 0)
#define CBAR() asm volatile("" ::: "memory")
#define WAIT_VM(n) asm volatile("s_waitcnt vmcnt(%0)" :: "i"(n) : "memory")
#define WAIT_LGKM0() asm volatile("s_waitcnt lgkmcnt(0)" ::: "memory")
#define SBAR() __builtin_amdgcn_sched_barrier(0)

#define STAMP_DECL unsigned stv = 0
#define STAMP(i) do { unsigned long long t_; asm volatile("s_memrealtime %0\n\ts_waitcnt lgkmcnt(0)" : "=s"(t_) :: "memory"); \
    asm volatile("v_writelane_b32 %0, %1, " #i : "+v"(stv) : "s"((unsigned)t_)); } while (0)
#define STAMPC(i) do { unsigned long long t_; asm volatile("s_memtime %0\n\ts_waitcnt lgkmcnt(0)" : "=s"(t_) :: "memory"); \
    asm volatile("v_writelane_b32 %0, %1, " #i : "+v"(stv) : "s"((unsigned)t_)); } while (0)
#define STAMP_FLUSH() do { STAMPC(31); STAMP(29); if (lane < 32) __builtin_amdgcn_raw_buffer_store_b32(stv, rws, WS_DBG + ((blockIdx.x * 8 + wid) * 32 + lane) * 4, 0, 0); } while (0)
#define FSTAMP(i) do {} while (0)
#define FSTAMPC(i) do {} while (0)
#define PIN(x) asm volatile("" : "+v"(x))

DI unsigned pk2(float a, float b) { f32x2 v = {a, b}; return __builtin_bit_cast(unsigned, __builtin_convertvector(v, bf16x2)); }
DI u32x4 pk8(const f32x16& a, int s) {
    return (u32x4){pk2(a[8 * s], a[8 * s + 1]), pk2(a[8 * s + 2], a[8 * s + 3]), pk2(a[8 * s + 4], a[8 * s + 5]), pk2(a[8 * s + 6], a[8 * s + 7])}; }
DI bf16x8 asbf(u32x4 v) { return __builtin_bit_cast(bf16x8, v); }
DI float swap_max(float m) { auto r = __builtin_amdgcn_permlane32_swap(__float_as_uint(m), __float_as_uint(m), false, false);
    return __builtin_fmaxf(__uint_as_float(r[0]), __uint_as_float(r[1])); }
DI float swap_min(float m) { auto r = __builtin_amdgcn_permlane32_swap(__float_as_uint(m), __float_as_uint(m), false, false);
    return __builtin_fminf(__uint_as_float(r[0]), __uint_as_float(r[1])); }
DI float swap_sum(float m) { auto r = __builtin_amdgcn_permlane32_swap(__float_as_uint(m), __float_as_uint(m), false, false);
    return __uint_as_float(r[0]) + __uint_as_float(r[1]); }
DI float from_hi(float m) { auto r = __builtin_amdgcn_permlane32_swap(__float_as_uint(m), __float_as_uint(m), false, false);
    return __uint_as_float(r[1]); }
DI float max16(const f32x16& s) {
    float a = __builtin_fmaxf(__builtin_fmaxf(s[0], s[1]), s[2]), b = __builtin_fmaxf(__builtin_fmaxf(s[3], s[4]), s[5]);
    a = __builtin_fmaxf(__builtin_fmaxf(a, s[6]), s[7]); b = __builtin_fmaxf(__builtin_fmaxf(b, s[8]), s[9]);
    a = __builtin_fmaxf(__builtin_fmaxf(a, s[10]), s[11]); b = __builtin_fmaxf(__builtin_fmaxf(b, s[12]), s[13]);
    a = __builtin_fmaxf(__builtin_fmaxf(a, s[14]), s[15]); return __builtin_fmaxf(a, b); }
DI float min16(const f32x16& s) {
    float a = __builtin_fminf(__builtin_fminf(s[0], s[1]), s[2]), b = __builtin_fminf(__builtin_fminf(s[3], s[4]), s[5]);
    a = __builtin_fminf(__builtin_fminf(a, s[6]), s[7]); b = __builtin_fminf(__builtin_fminf(b, s[8]), s[9]);
    a = __builtin_fminf(__builtin_fminf(a, s[10]), s[11]); b = __builtin_fminf(__builtin_fminf(b, s[12]), s[13]);
    a = __builtin_fminf(__builtin_fminf(a, s[14]), s[15]); return __builtin_fminf(a, b); }

DI void dma(rsrc_t rs, unsigned voff, unsigned soff, unsigned lds) {
    asm volatile("s_mov_b32 m0, %0\n\ts_nop 0\n\tbuffer_load_dwordx4 %1, %2, %3 offen lds" :: "s"(lds), "v"(voff), "s"(rs), "s"(soff) : "memory"); }
DI u32x4 lds_rd128(unsigned a) { return *(LAS const u32x4*)(uintptr_t)a; }
DI f32x4 lds_rdf4(unsigned a) { return *(LAS const f32x4*)(uintptr_t)a; }
DI void lds_wr128(unsigned a, u32x4 v) { *(LAS u32x4*)(uintptr_t)a = v; }
DI void lds_wrf4(unsigned a, f32x4 v) { *(LAS f32x4*)(uintptr_t)a = v; }
DI void lds_wr64(unsigned a, u32x2 v) { *(LAS u32x2*)(uintptr_t)a = v; }
DI bf16x8 lds_tr2(unsigned a0, unsigned a1) {
    s16x4 lo = __builtin_amdgcn_ds_read_tr16_b64_v4i16((LAS s16x4*)(uintptr_t)a0), hi = __builtin_amdgcn_ds_read_tr16_b64_v4i16((LAS s16x4*)(uintptr_t)a1);
    return __builtin_bit_cast(bf16x8, __builtin_shufflevector(lo, hi, 0, 1, 2, 3, 4, 5, 6, 7)); }
DI void sig(int c, int) {
    asm volatile("s_mov_b64 exec, 1\n\tds_add_u32 %0, %1\n\ts_mov_b64 exec, -1" :: "v"(L_CNT + 4 * c), "v"(1u) : "memory"); }
template <int SLP = 1> DI void waitc(int c, unsigned n) {
    unsigned v, sv, it;
    asm volatile("s_mov_b32 %2, %5\n"
                 "1:\n\tds_read_b32 %0, %3\n\ts_waitcnt lgkmcnt(0)\n\tv_readfirstlane_b32 %1, %0\n\ts_cmp_ge_u32 %1, %4\n\ts_cbranch_scc1 2f\n\t"
                 "s_sleep %6\n\ts_sub_u32 %2, %2, 1\n\ts_cbranch_scc0 1b\n2:"
                 : "=&v"(v), "=&s"(sv), "=&s"(it) : "v"(L_CNT + 4 * c), "i"(n), "i"(SPIN_CAP), "i"(SLP) : "memory", "scc"); }
DI unsigned rd_cnt(int c) { return *(volatile LAS const unsigned*)(uintptr_t)(L_CNT + 4 * c); }
DI u32x2 rd_tag() { const u32x2 t = *(LAS const u32x2*)(uintptr_t)L_TAG;
    return (u32x2){(unsigned)__builtin_amdgcn_readfirstlane(t[0]), (unsigned)__builtin_amdgcn_readfirstlane(t[1])}; }
DI u32x4 ld16(rsrc_t rs, unsigned voff, unsigned soff) { return __builtin_amdgcn_raw_buffer_load_b128(rs, voff, soff, 16); }
template <int AUX> DI void st16a(rsrc_t rs, u32x4 v, unsigned voff, unsigned soff) {
    __builtin_amdgcn_raw_buffer_store_b128(v, rs, voff, soff, AUX); asm volatile("s_nop 1" :: "v"(v) : "memory"); }
DI void st16(rsrc_t rs, u32x4 v, unsigned voff, unsigned soff) { st16a<16>(rs, v, voff, soff); }
DI u32x2 ldflag(rsrc_t rs, unsigned voff) { return __builtin_amdgcn_raw_buffer_load_b64(rs, voff, 0, 16); }
DI bool tagok(u32x2 f, u32x2 tag) {
    return __builtin_amdgcn_ballot_w64(__builtin_bit_cast(unsigned long long, f) == __builtin_bit_cast(unsigned long long, tag)) == ~0ull; }
DI unsigned ready8(u32x2 f, u32x2 tag) {
    return (unsigned)__builtin_amdgcn_ballot_w64(__builtin_bit_cast(unsigned long long, f) == __builtin_bit_cast(unsigned long long, tag)) & 255u; }
template <int SLP = 2> DI void poll(rsrc_t rs, unsigned voff, u32x2 tag) {
    CBAR();
#pragma clang loop unroll(disable)
    for (unsigned it = 0; it < SPIN_CAP; ++it) { u32x2 f = ldflag(rs, voff); if (tagok(f, tag)) break; __builtin_amdgcn_s_sleep(SLP); CBAR(); }
    CBAR(); }
DI void poll_fast(rsrc_t rs, unsigned voff, u32x2 tag, u32x2& a, u32x2& b, u32x2& c) {
    unsigned it;
    asm volatile("s_mov_b32 %3, %7\n\tbuffer_load_dwordx2 %0, %4, %5, 0 offen sc1\n\ts_nop 7\n\ts_nop 7\n\tbuffer_load_dwordx2 %1, %4, %5, 0 offen sc1\n\ts_nop 7\n\ts_nop 7\n"
                 "1:\n\tbuffer_load_dwordx2 %2, %4, %5, 0 offen sc1\n\ts_waitcnt vmcnt(2)\n\tv_cmp_eq_u64 vcc, %6, %0\n\ts_cmp_eq_u64 vcc, -1\n\ts_cbranch_scc1 2f\n\t"
                 "buffer_load_dwordx2 %0, %4, %5, 0 offen sc1\n\ts_waitcnt vmcnt(2)\n\tv_cmp_eq_u64 vcc, %6, %1\n\ts_cmp_eq_u64 vcc, -1\n\ts_cbranch_scc1 2f\n\t"
                 "buffer_load_dwordx2 %1, %4, %5, 0 offen sc1\n\ts_waitcnt vmcnt(2)\n\tv_cmp_eq_u64 vcc, %6, %2\n\ts_cmp_eq_u64 vcc, -1\n\ts_cbranch_scc1 2f\n\t"
                 "s_sub_u32 %3, %3, 1\n\ts_cbranch_scc0 1b\n2:"
                 : "=&v"(a), "=&v"(b), "=&v"(c), "=&s"(it) : "v"(voff), "s"(rs), "s"(tag), "i"(SPIN_CAP) : "memory", "vcc", "scc");
}
DI void post(rsrc_t rs, unsigned off, u32x2 tag, int lane) {
    WAIT_VM(0);
    if (lane == 0) __builtin_amdgcn_raw_buffer_store_b64(tag, rs, off, 0, 16);
    CBAR(); }

#define GB(n, k) "v[176+20*" #n "+4*" #k ":179+20*" #n "+4*" #k "]"
#define GT(A, n, t, i) "v_mfma_f32_32x32x16_bf16 %[" #A "], " GB(n, (2+i)) ", " GB(n, t) ", %[" #A "]\n\t"
#define GD(A, n, t, i) "v_mfma_f32_32x32x16_bf16 %[" #A "], " GB(n, t) ", " GB(n, (2+i)) ", %[" #A "]\n\t"
#define GX(n, t, X) "ds_read_b128 " GB(n, t) ", " X " offset:4096*" #t "\n\t"
#define GF(n, i, W, O) "ds_read_b64_tr_b16 v[176+20*" #n "+8+4*" #i ":177+20*" #n "+8+4*" #i "], " W " offset:" O "\n\t" \
                       "ds_read_b64_tr_b16 v[178+20*" #n "+8+4*" #i ":179+20*" #n "+8+4*" #i "], " W " offset:" O "+336\n\t"
#define GSTEP(M0, M1, M2, c, n, X, O, P0, P0B, P2, P4, P5) \
    M0(a0, c, 0, 0) P0 GX(n, 0, X) P0B M0(a3, c, 1, 0) GF(n, 0, "%[wa]", O) M1(a1, c, 0, 1) GX(n, 1, X) P2 M1(a4, c, 1, 1) GF(n, 1, "%[wb]", O) \
    M2(a2, c, 0, 2) GF(n, 2, "%[wc]", O) P4 M2(a5, c, 1, 2) P5
#define GLOOP(M0, M1, M2) \
    GX(0, 0, "%[xa]") GF(0, 0, "%[wa]", "0") GX(0, 1, "%[xa]") GF(0, 1, "%[wb]", "0") GF(0, 2, "%[wc]", "0") \
    GX(1, 0, "v170") GF(1, 0, "%[wa]", "5376") GX(1, 1, "v170") GF(1, 1, "%[wb]", "5376") GF(1, 2, "%[wc]", "5376") \
    "s_waitcnt lgkmcnt(8)\n" \
    "1:\n\t" \
    GSTEP(M0, M1, M2, 0, 2, "v171", "10752", "", "ds_read_b128 v[166:169], %[la]\n\t", "v_xor_b32 v172, 0x60, %[xa]\n\t", "s_and_b32 %[so], %[g], 3\n\t", \
          "s_mul_i32 %[so], %[so], %[slot]\n\ts_waitcnt lgkmcnt(9)\n\t") \
    GSTEP(M0, M1, M2, 1, 3, "v172", "16128", "", "v_add_u32 %[xa], %[so], %[xa0]\n\ts_waitcnt lgkmcnt(8)\n\t", "v_min3_u32 v173, v166, v167, v168\n\tv_xor_b32 v170, 32, %[xa]\n\t", \
          "v_min_u32 v173, v173, v169\n\tv_add_u32 %[wa], %[so], %[wa0]\n\t", "v_add_u32 %[wb], %[so], %[wb0]\n\tv_readfirstlane_b32 %[rf], v173\n\tv_add_u32 %[wc], %[so], %[wc0]\n\ts_waitcnt lgkmcnt(8)\n\t") \
    GSTEP(M0, M1, M2, 2, 0, "%[xa]", "0", "s_cmp_gt_u32 %[rf], %[g]\n\ts_cbranch_scc0 3f\n2:\n\t", "", "v_xor_b32 v171, 64, %[xa]\n\t", "", \
          "s_waitcnt lgkmcnt(8)\n\tv_mov_b32 v174, %[g]\n\t") \
    GSTEP(M0, M1, M2, 3, 1, "v170", "5376", "", "s_add_u32 %[g], %[g], 1\n\t", "s_cmp_lt_u32 %[g], 8\n\t", "", \
          "s_waitcnt lgkmcnt(8)\n\tds_write_b32 %[da], v174\n\ts_cbranch_scc1 1b\n\t") \
    "s_branch 4f\n" \
    "3:\n\ts_mov_b32 %[so], %[cap]\n" \
    "5:\n\tds_read_b128 v[166:169], %[la]\n\ts_waitcnt lgkmcnt(0)\n\tv_min3_u32 v173, v166, v167, v168\n\tv_min_u32 v173, v173, v169\n\ts_nop 0\n\tv_readfirstlane_b32 %[rf], v173\n\ts_cmp_gt_u32 %[rf], %[g]\n\ts_cbranch_scc1 2b\n\t" \
    "s_sub_u32 %[so], %[so], 1\n\ts_cbranch_scc0 5b\n\ts_branch 2b\n" \
    "4:\n\ts_waitcnt lgkmcnt(0)\n\ts_nop 7\n\ts_nop 3"
template <int ND, class Start>
DI void gemm6(unsigned xa0, unsigned wa0, unsigned wb0, unsigned wc0, int w, f32x16 (&acc)[6], Start start) {
    unsigned xa = xa0, wa = wa0, wb = wb0, wc = wc0, g = 1, so, rf;
    const unsigned la = L_CNT + 4 * C_LAND0, da = L_CNT + 4 * (C_DONE0 + w);
#pragma unroll
    for (int i = 0; i < 6; ++i) { acc[i] = f32x16{}; PIN(acc[i]); }
    PIN(xa); PIN(wa); PIN(wb); PIN(wc);
    start();
#define GASM(M0, M1, M2) asm volatile("v_xor_b32 v170, 32, %[xa]\n\tv_xor_b32 v171, 64, %[xa]\n\t" GLOOP(M0, M1, M2) \
        : [a0] "+v"(acc[0]), [a1] "+v"(acc[1]), [a2] "+v"(acc[2]), [a3] "+v"(acc[3]), [a4] "+v"(acc[4]), [a5] "+v"(acc[5]), \
          [xa] "+v"(xa), [wa] "+v"(wa), [wb] "+v"(wb), [wc] "+v"(wc), [g] "+s"(g), [so] "=&s"(so), [rf] "=&s"(rf) \
        : [xa0] "v"(xa0), [wa0] "v"(wa0), [wb0] "v"(wb0), [wc0] "v"(wc0), [la] "v"(la), [da] "v"(da), [slot] "i"(SLOT), [cap] "i"(SPIN_CAP) \
        : "memory", "scc", "v166", "v167", "v168", "v169", "v170", "v171", "v172", "v173", "v174", "v175", "v176", "v177", "v178", "v179", "v180", "v181", "v182", "v183", "v184", "v185", "v186", "v187", "v188", "v189", "v190", "v191", "v192", "v193", "v194", "v195", "v196", "v197", "v198", "v199", "v200", "v201", "v202", "v203", "v204", "v205", "v206", "v207", "v208", "v209", "v210", "v211", "v212", "v213", "v214", "v215", "v216", "v217", "v218", "v219", "v220", "v221", "v222", "v223", "v224", "v225", "v226", "v227", "v228", "v229", "v230", "v231", "v232", "v233", "v234", "v235", "v236", "v237", "v238", "v239", "v240", "v241", "v242", "v243", "v244", "v245", "v246", "v247", "v248", "v249", "v250", "v251", "v252", "v253", "v254", "v255")
    if constexpr (ND == 0) GASM(GT, GT, GT); else if constexpr (ND == 1) GASM(GT, GT, GD); else GASM(GD, GD, GD);
}
DI unsigned min4(u32x4 d) { return __builtin_amdgcn_readfirstlane(min(min(d[0], d[1]), min(d[2], d[3]))); }
DI u32x4 rd4(int c) { return *(volatile LAS const u32x4*)(uintptr_t)(L_CNT + 4 * c); }
DI void wait4(int c, unsigned n) {
    CBAR();
#pragma clang loop unroll(disable)
    for (unsigned it = 0; it < SPIN_CAP; ++it) if (min4(rd4(c)) >= n) break;
    CBAR(); }
template <int P> DI void dma_x4(rsrc_t rs, unsigned voff, unsigned soff, unsigned lds) {
    asm volatile("s_mov_b32 m0, %1\n\ts_nop %4\n\tbuffer_load_dwordx4 %2, %3, %0 offen lds\n\t"
                 "s_add_u32 m0, m0, 0x1000\n\ts_add_u32 %0, %0, 28672\n\ts_nop %4\n\tbuffer_load_dwordx4 %2, %3, %0 offen lds\n\t"
                 "s_add_u32 m0, m0, 0x1000\n\ts_add_u32 %0, %0, 28672\n\ts_nop %4\n\tbuffer_load_dwordx4 %2, %3, %0 offen lds\n\t"
                 "s_add_u32 m0, m0, 0x1000\n\ts_add_u32 %0, %0, 28672\n\ts_nop %4\n\tbuffer_load_dwordx4 %2, %3, %0 offen lds"
                 : "+s"(soff) : "s"(lds), "v"(voff), "s"(rs), "i"(P) : "memory", "scc"); }
template <int P> DI void dma_w5(rsrc_t rs, const unsigned (&v)[6], unsigned soff, unsigned lds) {
    asm volatile("s_mov_b32 m0, %0\n\ts_nop %8\n\tbuffer_load_dwordx4 %3, %1, %2 offen lds\n\t"
                 "s_add_u32 m0, m0, 0x1000\n\ts_nop %8\n\tbuffer_load_dwordx4 %4, %1, %2 offen lds\n\t"
                 "s_add_u32 m0, m0, 0x1000\n\ts_nop %8\n\tbuffer_load_dwordx4 %5, %1, %2 offen lds\n\t"
                 "s_add_u32 m0, m0, 0x1000\n\ts_nop %8\n\tbuffer_load_dwordx4 %6, %1, %2 offen lds\n\t"
                 "s_add_u32 m0, m0, 0x1000\n\ts_nop %8\n\tbuffer_load_dwordx4 %7, %1, %2 offen lds"
                 :: "s"(lds), "s"(rs), "s"(soff), "v"(v[0]), "v"(v[1]), "v"(v[2]), "v"(v[3]), "v"(v[4]), "i"(P) : "memory", "scc"); }
DI unsigned tr_addr128(unsigned base, int cb, int rowgrp, int lane) {
    const int q = (lane & 15) >> 2, byte = cb + 32 * ((lane >> 4) & 1) + 8 * (lane & 3);
    return base + (rowgrp + q) * 128 + ((((byte >> 4) ^ ((q >> 1) << 2)) & 7) << 4) + (byte & 8);
}
DI void feat(const f32x16& p, f32x16& e, f32x16& g) {
    const float mx = swap_max(max16(p)) * LOG2E, mn = swap_min(min16(p)) * LOG2E;
#pragma unroll
    for (int i = 0; i < 16; i += 2) {
        const f32x2 pp = {p[i], p[i + 1]};
        const f32x2 a = __builtin_elementwise_fma(pp, (f32x2){LOG2E, LOG2E}, (f32x2){-mx, -mx}), c = __builtin_elementwise_fma(pp, (f32x2){-LOG2E, -LOG2E}, (f32x2){mn, mn});
        e[i] = __builtin_amdgcn_exp2f(a[0]); e[i + 1] = __builtin_amdgcn_exp2f(a[1]); g[i] = __builtin_amdgcn_exp2f(c[0]); g[i + 1] = __builtin_amdgcn_exp2f(c[1]);
    }
}

typedef __attribute__((ext_vector_type(16))) unsigned u32x16;
DI rsrc_t mk_rsrc(unsigned lo, unsigned hi, unsigned bytes) {
    return __builtin_amdgcn_make_buffer_rsrc((void*)(((unsigned long long)hi << 32) | lo), 0, bytes, 0x00020000); }
DI void read_wo(bf16x8 (&wf)[16][2], unsigned ta0, unsigned ta1, int rot) {
#pragma unroll
    for (int k = 0; k < 8; ++k) {
        const unsigned ro = ((k + rot) & 7) * 28 * 128, b0 = ta0 + ro, b1 = ta1 + ro;
        wf[2 * k][0] = lds_tr2(b0, b0 + 1024); wf[2 * k][1] = lds_tr2(b1, b1 + 1024);
        wf[2 * k + 1][0] = lds_tr2(b0 + 2048, b0 + 3072); wf[2 * k + 1][1] = lds_tr2(b1 + 2048, b1 + 3072);
    }
}
DI void ones_row(u32x4& a, u32x4& b, u32x2 c) {
    u32x2 a0 = {a[0], a[1]}, a1 = {a[2], a[3]}, b0 = {b[0], b[1]}, b1 = {b[2], b[3]};
    asm volatile("s_mov_b64 exec, %4\n\tv_mov_b64 %0, %5\n\tv_mov_b64 %1, %5\n\tv_mov_b64 %2, %5\n\tv_mov_b64 %3, %5\n\ts_mov_b64 exec, -1"
                 : "+v"(a0), "+v"(a1), "+v"(b0), "+v"(b1) : "s"(0xF0000000F0000000ull), "v"(c));
    a = (u32x4){a0[0], a0[1], a1[0], a1[1]}; b = (u32x4){b0[0], b0[1], b1[0], b1[1]}; }
template <int ND>
DI void publish_a(f32x16 (&acc)[6], rsrc_t rws, const unsigned (&so)[3], int nq, int lane, bool hi, u32x2 oc) {
#pragma unroll
    for (int i = 0; i < 3; ++i) {
        if (i < 3 - ND && i < nq) {
            asm volatile("" ::: "memory");
#pragma unroll
            for (int k = 0; k < 16; ++k) { acc[i][k] *= QSCALE; acc[3 + i][k] *= QSCALE; }
            PIN(acc[i]); PIN(acc[3 + i]);
        }
        const unsigned pad = i < nq ? 0u : 0x00003F80u;
#pragma unroll
        for (int t = 0; t < 2; ++t) {
            u32x4 v0 = pk8(acc[3 * t + i], 0), v1 = pk8(acc[3 * t + i], 1);
            if (i >= 3 - ND) ones_row(v0, v1, oc); else { v1[2] = hi ? pad : v1[2]; v1[3] = hi ? 0u : v1[3]; }
            st16(rws, v0, lane * 16, so[i] + t * 6144); st16(rws, v1, lane * 16 + 1024, so[i] + t * 6144);
            SBAR();
        }
    }
}
DI void phi_w(bf16x8& f0, u32x4& f1, int hh, int lane, bool hi) {
    const unsigned pb = L_PHI + hh * 1568 + (4 * (lane >> 5) + ((lane & 15) >> 2)) * 56 + ((lane & 19) == 19 ? 0 : 32 * ((lane >> 4) & 1) + 8 * (lane & 3));
    f0 = lds_tr2(pb, pb + 8 * 56);
    f1 = __builtin_bit_cast(u32x4, lds_tr2(pb + 16 * 56, pb + 24 * 56));
    if (hi) { f1[2] = 0; f1[3] = 0; }
}
DI f32x16 phi_pre(u32x4 k0, u32x4 k1, bf16x8 f0, u32x4 f1) {
    f32x16 p = MFMA(f0, asbf(k0), f32x16{});
    return MFMA(asbf(f1), asbf(k1), p);
}

DI void phik_store(const f32x16& p, unsigned ib, float lo) {
    f32x16 e, g;
    feat(p, e, g);
    f32x2 s2 = {e[0], e[1]}, g2 = {g[0], g[1]};
#pragma unroll
    for (int i = 2; i < 12; i += 2) { s2 += (f32x2){e[i], e[i + 1]}; g2 += (f32x2){g[i], g[i + 1]}; }
    s2 = __builtin_elementwise_fma((f32x2){e[12], e[13]} + (f32x2){e[14], e[15]}, (f32x2){lo, lo}, s2);
    g2 = __builtin_elementwise_fma((f32x2){g[12], g[13]} + (f32x2){g[14], g[15]}, (f32x2){lo, lo}, g2);
    const float se = __builtin_amdgcn_rcpf(swap_sum(s2[0] + s2[1])), sg = __builtin_amdgcn_rcpf(swap_sum(g2[0] + g2[1]));
#pragma unroll
    for (int u = 0; u < 8; ++u) {
        const int k = 4 * (u & 3);
        const f32x16& z = (u >> 2) ? g : e; const float sc = (u >> 2) ? sg : se;
        const f32x2 a = (f32x2){z[k], z[k + 1]} * (f32x2){sc, sc}, b = (f32x2){z[k + 2], z[k + 3]} * (f32x2){sc, sc};
        lds_wr64(ib ^ ((8 * (u >> 2) + 2 * (u & 3)) << 3), (u32x2){pk2(a[0], a[1]), pk2(b[0], b[1])});
    }
}
DI unsigned phik_base(int w, int lane) {
    const int tk = lane & 31, pi = (((tk >> 1) & 1) << 3) | (tk & 1) | (((tk >> 2) & 3) << 1);
    return L_R1 + (w >> 1) * 16384 + ((w & 1) * 64 + tk) * 128 + ((pi ^ (lane >> 5)) << 3); }

__global__ __launch_bounds__(512, 2) void fused_fwd(const void*, const void*, const void*, const void*, const void*, const void*, const void*, void*, void*) {
    STAMP_DECL; STAMP(0); STAMPC(30);
    u32x16 ka; u32x2 kb;
    asm volatile("s_load_dwordx16 %0, %2, 0x0\n\ts_load_dwordx2 %1, %2, 0x40" : "=&s"(ka), "=&s"(kb) : "s"(__builtin_amdgcn_kernarg_segment_ptr()));
    const int tid = threadIdx.x, lane = tid & 63, wid = __builtin_amdgcn_readfirstlane(tid >> 6);
    const int b = blockIdx.x, xcd = b & 7, r = b >> 3, w = wid & 3, role = wid >> 2;
    const int isb = r & 1, ml = (r >> 1) & 3, nl = r >> 3;
    const int mt = (xcd & 3) * 4 + ml, nt = (xcd >> 2) * 4 + nl;
    const bool hi = lane >= 32;
    int g0 = ROT == 0 ? 0 : ROT == 1 ? ((ml + nl) & 1) * 4 : ROT == 2 ? 2 * ml + nl : ROT == 3 ? (isb ? ((ml + nl) & 1) * 4 : 0) : ROT == 4 ? (isb ? 2 * ml + nl : ((ml + nl) & 1) * 4) : (isb ? 2 * ml + nl : 0); g0 -= (g0 >= 7) ? 7 : 0;
    const int head = 2 * xcd + (r >> 4), qt = r & 15;
    const int ct = r >> 2, tto = (2 * xcd + (w & 1)) * 4 + (r & 3);
    u32x2 tag;
    bf16x8 wf[16][2];
    unsigned rdy0 = 0;
    unsigned pfd = 0;

    unsigned xvo, wvo[6];
    if (role) {
        __builtin_amdgcn_s_setprio(2);
        xvo = (w * 8 + (lane >> 3)) * 896 + ((((lane & 7) ^ (lane >> 4) ^ ((w & 1) << 2)) & 7) << 4);
        PIN(xvo);
#pragma unroll
        for (int i = 0; i < 6; ++i) {
            const int c = 64 * (w + 4 * i) + lane, rho = (c * 3121) >> 16, ch = c - 21 * rho, win = (ch * 37) >> 8;
            const int krow = (rho & ~15) | ((rho & 3) << 2) | ((rho >> 2) & 3);
            wvo[i] = krow * 2688 + ch * 16 + (isb ? win * (896 - 112) : 0);
            PIN(wvo[i]);
        }
    }
    if (wid == 0) {
        unsigned long long pc; unsigned len;
        asm volatile("s_getpc_b64 %0\n.Lpf_begin%=:\n\ts_mov_b32 %1, .Lpf_end-.Lpf_begin%=" : "=s"(pc), "=s"(len));
        const rsrc_t rc = __builtin_amdgcn_make_buffer_rsrc((void*)pc, 0, len, 0x00020000);
        asm volatile("s_nop 4\n\tbuffer_load_dword %0, %1, %2, 0 offen" : "+v"(pfd) : "v"(lane < 12 ? (r * 12 + lane) * 128 : 0x7FFFFFF0), "s"(rc) : "memory");
        *(LAS unsigned*)(uintptr_t)(L_CNT + 4 * lane) = 0u;
    }
    asm volatile("s_waitcnt lgkmcnt(0)" : "+s"(ka), "+s"(kb) :: "memory");
    const rsrc_t rws = mk_rsrc(kb[0], kb[1], WS_BYTES);
    u32x4 pe[2][4];
    unsigned kva[5], wovo, wt0, wt1, rvo, rfo;
    if (role) {
        const rsrc_t rx = mk_rsrc(isb ? ka[6] : ka[0], isb ? ka[7] : ka[1], 2048 * 896), rw = mk_rsrc(isb ? ka[8] : ka[2], isb ? ka[9] : ka[3], 448 * 2688);
        const unsigned xs0 = mt * 114688, ws0 = (isb ? 112 : 336) * nt;
        auto feed = [&](int g, auto PX, auto PW) {
            int ga = g + g0; ga -= (ga >= 7) ? 7 : 0;
            const unsigned ws = ws0 + ga * 64 * 2688, l = (g & 3) * SLOT + w * 1024;
            dma_x4<decltype(PX)::value>(rx, xvo, xs0 + ga * 128, l);
            dma_w5<decltype(PW)::value>(rw, wvo, ws, l + 16384);
            if (w == 0) dma(rw, wvo[5], ws, l + 16384 + 5 * 4096); };
        const unsigned lw = L_CNT + 4 * (C_LAND0 + w);
        constexpr std::integral_constant<int, 0> fast{};
        FSTAMP(18);
        dma(mk_rsrc(ka[10], ka[11], 16 * 1568), lane * 16, nt * 3136 + w * 1024, L_PHI + w * 1024);
        feed(0, fast, fast);
        if (LATE1 == 0) feed(1, fast, fast);
        asm volatile("s_barrier" ::: "memory");
        if (LATE1 == 1) { WAIT_VM(4); feed(1, fast, fast); }
        if (LATE1 == 2) { WAIT_VM(0); *(volatile LAS unsigned*)(uintptr_t)lw = 1; feed(1, fast, fast); }
        if (w == 0) WAIT_VM(10); else WAIT_VM(9);
        *(volatile LAS unsigned*)(uintptr_t)lw = 1;
        STAMP(1);
#pragma unroll 1
        for (int g = 1; g < 6; ++g) {
            if (g >= 3) wait4(C_DONE0, g - 2);
            if (g < FASTG) feed(g + 1, fast, fast); else feed(g + 1, std::integral_constant<int, PNX>{}, std::integral_constant<int, PNW>{});
            if (w == 0) WAIT_VM(10); else WAIT_VM(9);
            *(volatile LAS unsigned*)(uintptr_t)lw = g + 1;
        }
        {
            const rsrc_t rt_ = mk_rsrc(w < 2 ? ka[4] : ka[12], w < 2 ? ka[5] : ka[13], 448 * 896);
            asm volatile("s_mov_b32 m0, %0\n\ts_nop 3\n\tbuffer_load_dword %1, %2, 0 offen lds"
                         :: "s"(L_JUNK), "v"(lane < 49 ? (r * 98 + (w & 1) * 49 + lane) * 128 : 0x7FFFFF00), "s"(rt_) : "memory");
        }
        WAIT_VM(1);
        *(volatile LAS unsigned*)(uintptr_t)lw = 8;
        STAMP(2);
        tag = rd_tag();
        wovo = (lane >> 3) * 896 + (((lane & 7) ^ (((lane >> 4) & 1) << 2)) << 4);
        wt0 = tr_addr128(L_R0, 0, (w >> 1) * 224 + 4 * (lane >> 5), lane); wt1 = tr_addr128(L_R0, 64, (w >> 1) * 224 + 4 * (lane >> 5), lane);
        rvo = (lane >> 4) * 32768 + (16 * (qt & 3) + (lane & 15)) * 16; rfo = WS_F2 + ((head * 16 + (lane & 15)) * 2 + (qt >> 3)) * 128;
        PIN(wovo); PIN(wt0); PIN(wt1); PIN(rvo); PIN(rfo);
        if (isb) {
            bf16x8 ph0; u32x4 ph1;
            wait4(C_LAND0, 1);
            phi_w(ph0, ph1, w >> 1, lane, hi);
            unsigned ib = phik_base(w, lane), qa = L_QB + w * 8192 + lane * 16;
            float lo = hi ? 0.f : 1.f;
            {
                const int q = (lane & 15) >> 2, P = 8 * (w & 1) + 4 * ((lane >> 4) & 1) + (lane & 3);
#pragma unroll
                for (int i = 0; i < 4; ++i) {
                    const int t = 8 * i + 4 * (lane >> 5) + q, pt = (((t >> 1) & 1) << 3) | (t & 1) | (((t >> 2) & 3) << 1);
                    kva[i] = L_R1 + (w >> 1) * 16384 + t * 128 + ((P ^ pt) << 3); PIN(kva[i]);
                }
                kva[4] = L_R0 + (w >> 1) * 8192 + lane * 16; PIN(kva[4]);
            }
            PIN(ib); PIN(qa); PIN(lo); PIN(ph0); PIN(ph1);
            waitc<1>(C_QB + w, 1);
            f32x16 pp[4];
#pragma unroll
            for (int t = 0; t < 4; ++t) pp[t] = phi_pre(lds_rd128(qa + t * 2048), lds_rd128(qa + t * 2048 + 1024), ph0, ph1);
            phik_store(pp[0], ib, lo); phik_store(pp[1], ib + 4096, lo);
            WAIT_LGKM0(); sig(C_PV, lane);
#pragma unroll
            for (int t = 0; t < 2; ++t) {
                f32x16 e, g;
                feat(pp[2 + t], e, g);
                pe[t][0] = pk8(e, 0); pe[t][1] = pk8(e, 1); pe[t][2] = pk8(g, 0); pe[t][3] = pk8(g, 1);
            }
        }
        STAMP(3);
    } else {
        f32x16 acc[6];
        const int tp = w & 1, sg = w >> 1;
        const int rr = tp * 64 + (lane & 31);
        const unsigned xa0 = rr * 128 + ((((lane >> 5) ^ (rr >> 1)) & 7) << 4);
        const unsigned lb = 16384 + (4 * ((lane & 15) >> 2) + 2 * (lane >> 5)) * 336 + 32 * ((lane >> 4) & 1) + 8 * (lane & 3);
        const unsigned wst = isb ? 112 : 56, wa0 = lb + 56 * (isb ? sg : 3 * sg), wb0 = wa0 + wst, wc0 = wb0 + wst;
        const int G = 2 * nt + sg;
        if (w == 0) {
            const u32x2 ep = ldflag(rws, WS_EPOCH);
            const unsigned long long e = (((unsigned long long)ep[1] << 32) | ep[0]) + 1;
            lds_wr64(L_TAG, (u32x2){(unsigned)e, (unsigned)(e >> 32)});
            WAIT_VM(0);
            asm volatile("" :: "v"(pfd));
        }
        unsigned so[3];
#pragma unroll
        for (int i = 0; i < 3; ++i) { const int sl = 3 * G + i; so[i] = WS_QKV + (((sl & 15) * 64 + mt * 4 + 2 * tp) * 6 + (sl >> 4) * 2) * 1024; asm volatile("" : "+s"(so[i])); }
        const int nq = 16 - 3 * G;
        u32x2 oc; oc[0] = oc[1] = (lane & 31) == 28 ? 0x3F803F80u : 0u; PIN(oc);
        auto start = [&]() { asm volatile("s_waitcnt lgkmcnt(0)\n\ts_barrier" ::: "memory"); tag = rd_tag(); asm volatile("" : "+s"(tag)); wait4(C_LAND0, 1); STAMP(1); STAMPC(23); };
        if (isb || G == 10) { gemm6<1>(xa0, wa0, wb0, wc0, w, acc, start); STAMPC(24); STAMP(2); if (!isb) publish_a<1>(acc, rws, so, nq, lane, hi, oc); }
        else if (G < 10) { gemm6<0>(xa0, wa0, wb0, wc0, w, acc, start); STAMPC(24); STAMP(2); publish_a<0>(acc, rws, so, nq, lane, hi, oc); }
        else { gemm6<3>(xa0, wa0, wb0, wc0, w, acc, start); STAMPC(24); STAMP(2); publish_a<3>(acc, rws, so, nq, lane, hi, oc); }
        if (!isb) {
            FSTAMP(15);
            post(rws, WS_F1 + (G * 32 + mt * 2 + tp) * 128, tag, lane);
            wait4(C_DONE0, 7);
        } else {
            u32x4 fr[2][3][2];
#pragma unroll
            for (int t = 0; t < 2; ++t) {
#pragma unroll
                for (int i = 0; i < 3; ++i) { fr[t][i][0] = pk8(acc[3 * t + i], 0); fr[t][i][1] = pk8(acc[3 * t + i], 1); }
                ones_row(fr[t][2][0], fr[t][2][1], oc);
            }
            wait4(C_DONE0, 7);
#pragma unroll
            for (int t = 0; t < 2; ++t)
#pragma unroll
                for (int s = 0; s < 2; ++s) {
                    lds_wr128(L_QB + w * 8192 + t * 2048 + s * 1024 + lane * 16, fr[t][1][s]);
                    lds_wr128(L_QB + w * 8192 + 4096 + t * 2048 + s * 1024 + lane * 16, fr[t][0][s]);
                    lds_wr128(L_R0 + ((sg * 4 + 2 * tp + t) * 2 + s) * 1024 + lane * 16, fr[t][2][s]);
                }
            WAIT_LGKM0(); sig(C_QB + w, lane);
        }
        STAMP(3);
    }
    FSTAMP(4);

    if (role) {

        if (isb) {
            waitc<0>(C_PV, 4);
            const int hh = w >> 1, ft = w & 1;
            bf16x8 fa[8], fb[8];
#pragma unroll
            for (int st = 0; st < 8; ++st) {
                fa[st] = lds_tr2(kva[2 * (st & 1)] + (st >> 1) * 4096, kva[2 * (st & 1) + 1] + (st >> 1) * 4096);
                fb[st] = asbf(lds_rd128(kva[4] + st * 1024));
            }
            SBAR();
            f32x16 kv = {};
#pragma unroll
            for (int st = 0; st < 8; ++st) kv = MFMA(fa[st], fb[st], kv);
            WAIT_LGKM0(); sig(C_KVDONE, lane);
            const int hk = 2 * nt + hh;
#pragma unroll
            for (int u = 0; u < 4; ++u)
                st16(rws, __builtin_bit_cast(u32x4, (f32x4){kv[4 * u], kv[4 * u + 1], kv[4 * u + 2], kv[4 * u + 3]}), lane * 16 + u * 1024, WS_KVP + ((hk * 16 + mt) * 8 + ft * 4) * 1024);
            FSTAMP(19);
            post(rws, WS_F2 + ((hk * 16 + mt) * 2 + ft) * 128, tag, lane);
            waitc<0>(C_KVDONE, 4);
        }
        STAMP(5);
        if (r < 28) {
            waitc<HSLP>(C_QX, 4);
            if (w == 0) *(LAS u32x2*)(uintptr_t)(L_R0 + 57344 + 8 * lane) = (u32x2){0u, 0u};
            const rsrc_t rwob = mk_rsrc(ka[12], ka[13], 448 * 896);
#pragma unroll
            for (int i = 0; i < 14; ++i) { WAIT_VM(WOWIN); dma(rwob, wovo, ct * 128 + (w + 4 * i) * 8 * 896, L_R0 + (w + 4 * i) * 1024); }
            WAIT_VM(0); WAIT_LGKM0(); sig(C_WO0 + 1, lane);
        }
        if (w == 3) {
            const int hr = head, i = lane & 15;
            poll<HSLP>(rws, rfo, tag);
            u32x4 pl[8];
#pragma unroll
            for (int p = 0; p < 4; ++p) {
                const unsigned so = WS_KVP + hr * 131072 + (qt >> 2) * 2048 + p * 8192;
                pl[2 * p] = ld16(rws, rvo, so); pl[2 * p + 1] = ld16(rws, rvo + 1024, so);
            }
            SBAR();
            f32x4 s0 = {}, s1 = {};
#pragma unroll
            for (int p = 0; p < 4; ++p) { s0 += __builtin_bit_cast(f32x4, pl[2 * p]); s1 += __builtin_bit_cast(f32x4, pl[2 * p + 1]); }
            float v[8] = {s0[0], s0[1], s0[2], s0[3], s1[0], s1[1], s1[2], s1[3]};
#pragma unroll
            for (int k = 0; k < 8; ++k) {
                auto t = __builtin_amdgcn_permlane16_swap(__float_as_uint(v[k]), __float_as_uint(v[k]), false, false);
                v[k] = swap_sum(__uint_as_float(t[0]) + __uint_as_float(t[1]));
            }
            u32x4 o = {pk2(v[0], v[1]), pk2(v[2], v[3]), pk2(v[4], v[5]), pk2(v[6], v[7])};
            const int cl = 16 * (qt & 3) + i;
            if ((cl & 31) == 29) o = (u32x4){0x3F803F80u, 0x3F803F80u, 0x3F803F80u, 0x3F803F80u};
            if (((qt >> 2) & 1) && cl >= 32) { o[2] = 0u; o[3] = 0u; }
            st16(rws, o, lane < 16 ? i * 16 : 0x7FFFFF00u, WS_KVR + (hr * 256 + qt * 16) * 16);
            post(rws, WS_F3 + (hr * 16 + qt) * 128, tag, lane);
        }
        STAMP(6);
        if (r < 28) {
            waitc<4>(C_WO0 + 1, 4);
            read_wo(wf, wt0, wt1, 0);
            WAIT_LGKM0(); sig(C_WOFREE, lane);
        }
        if (isb) {
            const int hb_ = 2 * nt + (w >> 1), tb = mt * 4 + 2 * (w & 1);
            poll<HSLP>(rws, WS_F3 + (hb_ * 16 + (lane & 15)) * 128, tag);
            u32x4 kf[4];
#pragma unroll
            for (int f = 0; f < 4; ++f) kf[f] = ld16(rws, lane * 16 + f * 1024, WS_KVR + hb_ * 4096);
            SBAR();
#pragma unroll
            for (int t = 0; t < 2; ++t) {
                f32x16 np = MFMA(asbf(kf[0]), asbf(pe[t][0]), f32x16{}); np = MFMA(asbf(kf[1]), asbf(pe[t][1]), np);
                f32x16 nm = MFMA(asbf(kf[2]), asbf(pe[t][2]), f32x16{}); nm = MFMA(asbf(kf[3]), asbf(pe[t][3]), nm);
                const float rp = __builtin_amdgcn_rcpf(np[13]), rm = __builtin_amdgcn_rcpf(nm[13]);
                const float rd = __builtin_amdgcn_rcpf(__builtin_fmaf(rp, np[12], rm * nm[12]) + 1e-6f);
                const float cp = from_hi(rp * rd), cm = from_hi(rm * rd);
                f32x16 o;
#pragma unroll
                for (int i = 0; i < 16; ++i) o[i] = __builtin_fmaf(cp, np[i], cm * nm[i]);
                u32x4 o0 = pk8(o, 0), o1 = pk8(o, 1);
                if (hi) { o1[2] = 0; o1[3] = 0; }
                const unsigned so = WS_O + (2u << 20) + (((tb + t) * 16 + hb_) * 2) * 1024;
                st16(rws, o0, lane * 16, so); st16(rws, o1, lane * 16 + 1024, so);
            }
            WAIT_VM(0);
            if (lane < 2) __builtin_amdgcn_raw_buffer_store_b64(tag, rws, WS_FO + (1024 + (tb + lane) * 16 + hb_) * 128, 0, 16);
            CBAR();
        }
        STAMP(7);
        if (r < 28) {
            const rsrc_t rwoa = mk_rsrc(ka[4], ka[5], 448 * 896);
            waitc<HSLP>(C_WOFREE, 4);
#pragma unroll
            for (int i = 0; i < 14; ++i) { WAIT_VM(WOWIN); dma(rwoa, wovo, ct * 128 + (w + 4 * i) * 8 * 896, L_R0 + (w + 4 * i) * 1024); }
            WAIT_VM(0); sig(C_WO0, lane);
        }
        if (b == 255 && w == 0) {
#pragma unroll 1
            for (int i = 0; i < 4; ++i) poll<HSLP>(rws, WS_F3 + (i * 64 + lane) * 128, tag);
            if (lane == 0) __builtin_amdgcn_raw_buffer_store_b64(tag, rws, WS_EPOCH, 0, 16);
        }
    } else {
        const int tq = qt * 4 + w;
        const int gq = (head * 43) >> 7, gk = ((16 + head) * 43) >> 7, gv = ((32 + head) * 43) >> 7;
        const int rk0 = qt >> 2;
        const unsigned frk = WS_F1 + (((lane & 2) ? gv : gk) * 32 + 8 * w + (lane & 1)) * 128;
        __builtin_amdgcn_s_setprio(3);
        f32x16 O[4] = {};
        u32x4 pA0 = {}, pA1 = {}, pB0, pB1;
        u32x4 kA[4], kB[4], kC[4], kD[4];
        kD[2] = (u32x4){}; kD[3] = (u32x4){};
        PIN(O[0]); PIN(O[1]); PIN(O[2]); PIN(O[3]); PIN(pA0); PIN(pA1); PIN(kD[2]); PIN(kD[3]);
        const unsigned hb = WS_QKV + head * 64 * 6144, kvb = hb + 2048;
        unsigned kb0 = kvb + (16 * w + (qt & 3)) * 6144, kb1 = kvb + (16 * w + ((qt + 1) & 3)) * 6144,
                 kb2 = kvb + (16 * w + ((qt + 2) & 3)) * 6144, kb3 = kvb + (16 * w + ((qt + 3) & 3)) * 6144, qb = hb + tq * 6144;
        unsigned vc = lane * 16 + rk0 * 24576;
        asm volatile("" : "+s"(kb0), "+s"(kb1), "+s"(kb2), "+s"(kb3), "+s"(qb), "+v"(vc));
        u32x2 pf0, pf1, pf2;
        poll_fast(rws, WS_F1 + (lane >= 4 ? gq * 32 + (tq >> 1) : ((lane & 2) ? gv : gk) * 32 + 8 * w + 2 * rk0 + (lane & 1)) * 128, tag, pf0, pf1, pf2);
        STAMP(5);
#define LD(d, f, v, sb) d[f] = ld16(rws, (v) + (f) * 1024, sb)
        u32x4 q0 = ld16(rws, lane * 16, qb), q1 = ld16(rws, lane * 16 + 1024, qb);
        LD(kA, 0, vc, kb0); LD(kA, 1, vc, kb0);
        SBAR();
        bf16x8 Q[4][2];
        {
            f32x16 s = MFMA(asbf(kA[0]), asbf(q0), f32x16{}); s = MFMA(asbf(kA[1]), asbf(q1), s);
            SBAR(); FSTAMP(13);
            asm volatile("" :: "v"(pf0), "v"(pf1), "v"(pf2));
            LD(kA, 2, vc, kb0); LD(kA, 3, vc, kb0); LD(kB, 0, vc, kb1); LD(kB, 1, vc, kb1); LD(kB, 2, vc, kb1); LD(kB, 3, vc, kb1);
            LD(kC, 0, vc, kb2); LD(kC, 1, vc, kb2); LD(kC, 2, vc, kb2); LD(kC, 3, vc, kb2);
            SBAR();
            lds_wr128(L_QX + (w * 2) * 1024 + lane * 16, q0);
            const float m = swap_max(max16(s)) + REF_SHIFT;
            u32x4 q1m = q1;
            if (hi) q1m[2] = pk2(-m, 0.f);
            lds_wr128(L_QX + (w * 2 + 1) * 1024 + lane * 16, q1m);
            WAIT_LGKM0(); sig(C_QX, lane); FSTAMP(14); waitc<0>(C_QX, 4);
#pragma unroll
            for (int a = 0; a < 3; ++a) { const unsigned qa = L_QX + ((w + 1 + a) & 3) * 2048 + lane * 16; Q[a][0] = asbf(lds_rd128(qa)); Q[a][1] = asbf(lds_rd128(qa + 1024)); }
            Q[3][0] = asbf(q0); Q[3][1] = asbf(q1m);
        }
        u32x2 fl = ldflag(rws, frk + ((rk0 + 1) & 3) * 256);
        STAMP(6); STAMPC(20);
        __builtin_amdgcn_s_setprio(0);
        f32x16 sA, sB;
        sA = MFMA(asbf(kA[0]), Q[0][0], f32x16{}); sA = MFMA(asbf(kA[1]), Q[0][1], sA);
        PIN(Q[1][0]); PIN(Q[1][1]); PIN(Q[2][0]); PIN(Q[2][1]); PIN(Q[3][0]); PIN(Q[3][1]);
#define EX4(S, i) S[i] = __builtin_amdgcn_exp2f(S[i]); S[i + 1] = __builtin_amdgcn_exp2f(S[i + 1]); S[i + 2] = __builtin_amdgcn_exp2f(S[i + 2]); S[i + 3] = __builtin_amdgcn_exp2f(S[i + 3])
#define STAGE(SC, SN, PC0, PC1, PP0, PP1, KN, an, VP, ap, F0, F1, F2, F3) do { \
        SN = MFMA(asbf(KN[0]), Q[an][0], f32x16{}); SBAR(); F0; SBAR(); EX4(SC, 0); PIN(SC); SBAR(); \
        SN = MFMA(asbf(KN[1]), Q[an][1], SN); SBAR(); F1; SBAR(); EX4(SC, 4); PC0[0] = pk2(SC[0], SC[1]); PC0[1] = pk2(SC[2], SC[3]); PIN(SC); PIN(PC0); SBAR(); \
        O[ap] = MFMA(asbf(VP[2]), asbf(PP0), O[ap]); SBAR(); F2; SBAR(); EX4(SC, 8); PC0[2] = pk2(SC[4], SC[5]); PC0[3] = pk2(SC[6], SC[7]); PIN(SC); PIN(PC0); SBAR(); \
        O[ap] = MFMA(asbf(VP[3]), asbf(PP1), O[ap]); SBAR(); F3; SBAR(); EX4(SC, 12); PC1[0] = pk2(SC[8], SC[9]); PC1[1] = pk2(SC[10], SC[11]); \
        PC1[2] = pk2(SC[12], SC[13]); PC1[3] = pk2(SC[14], SC[15]); PIN(PC1); SBAR(); } while (0)
#define NOP (void)0
#define ARRIVE(K) asm volatile("" : "+v"(K[0]), "+v"(K[1]), "+v"(K[2]), "+v"(K[3]))
#define TILE(KP, KC, KN, V, SB, G0) do { \
        STAGE(sA, sB, pB0, pB1, pA0, pA1, KC, 1, KP, 3, G0, NOP, NOP, NOP); \
        STAGE(sB, sA, pA0, pA1, pB0, pB1, KC, 2, KC, 0, LD(KP, 0, V, SB), LD(KP, 1, V, SB), LD(KP, 2, V, SB), LD(KP, 3, V, SB)); \
        STAGE(sA, sB, pB0, pB1, pA0, pA1, KC, 3, KC, 1, NOP, NOP, NOP, ARRIVE(KN)); \
        STAGE(sB, sA, pA0, pA1, pB0, pB1, KN, 0, KC, 2, NOP, NOP, NOP, NOP); } while (0)
#define TILN(KP, KC, KN) do { \
        STAGE(sA, sB, pB0, pB1, pA0, pA1, KC, 1, KP, 3, NOP, NOP, NOP, NOP); \
        STAGE(sB, sA, pA0, pA1, pB0, pB1, KC, 2, KC, 0, NOP, NOP, NOP, NOP); \
        STAGE(sA, sB, pB0, pB1, pA0, pA1, KC, 3, KC, 1, NOP, NOP, NOP, ARRIVE(KN)); \
        STAGE(sB, sA, pA0, pA1, pB0, pB1, KN, 0, KC, 2, NOP, NOP, NOP, NOP); } while (0)
#pragma unroll 1
        for (int it = 0; it < 3; ++it) {
            const unsigned vn = lane * 16 + ((rk0 + it + 1) & 3) * 24576;
            const unsigned fn = frk + ((rk0 + it + 1) & 3) * 256, f2 = frk + ((rk0 + it + 2) & 3) * 256;
            TILE(kD, kA, kB, vc, kb3, NOP);
            if (!tagok(fl, tag)) poll<1>(rws, fn, tag);
            CBAR();
            TILE(kA, kB, kC, vn, kb0, NOP);
            TILE(kB, kC, kD, vn, kb1, NOP);
            TILE(kC, kD, kA, vn, kb2, fl = ldflag(rws, f2));
            vc = vn;
        }
        unsigned cw[3];
#pragma unroll
        for (int a = 0; a < 3; ++a) { cw[a] = L_R2 + (((w + 1 + a) & 3) * 3 + 2 - a) * 4096 + lane * 16; PIN(cw[a]); }
#define SND(a, u) lds_wrf4(cw[a] + (u) * 1024, (f32x4){O[a][4 * (u)], O[a][4 * (u) + 1], O[a][4 * (u) + 2], O[a][4 * (u) + 3]})
        TILE(kD, kA, kB, vc, kb3, NOP);
        TILN(kA, kB, kC);
        TILN(kB, kC, kD);
        STAGE(sA, sB, pB0, pB1, pA0, pA1, kD, 1, kC, 3, NOP, NOP, NOP, NOP);
        STAGE(sB, sA, pA0, pA1, pB0, pB1, kD, 2, kD, 0, NOP, NOP, NOP, NOP);
        STAGE(sA, sB, pB0, pB1, pA0, pA1, kD, 3, kD, 1, NOP, SND(0, 0), SND(0, 1), SND(0, 2));
        O[2] = MFMA(asbf(kD[2]), asbf(pB0), O[2]); SBAR(); SND(0, 3); SBAR(); EX4(sB, 0); EX4(sB, 4); PIN(sB); SBAR();
        O[2] = MFMA(asbf(kD[3]), asbf(pB1), O[2]); SBAR(); SND(1, 0); SBAR();
        pA0 = pk8(sB, 0); PIN(pA0); SBAR(); SND(1, 1); SBAR(); EX4(sB, 8); PIN(sB); SBAR();
        O[3] = MFMA(asbf(kD[2]), asbf(pA0), O[3]); SBAR(); SND(1, 2); SBAR(); EX4(sB, 12); PIN(sB); SBAR(); SND(1, 3); SBAR();
        pA1 = pk8(sB, 1); PIN(pA1); SBAR(); SND(2, 0); SBAR();
        O[3] = MFMA(asbf(kD[3]), asbf(pA1), O[3]); SBAR();
        SND(2, 1); SND(2, 2); SND(2, 3);
        STAMP(7); STAMPC(21);
        f32x16 o;
        {
            WAIT_LGKM0(); sig(C_CMB, lane); waitc<0>(C_CMB, 4);
            f32x4 pr[12];
#pragma unroll
            for (int k = 0; k < 12; ++k) pr[k] = lds_rdf4(L_R2 + (w * 12 + k) * 1024 + lane * 16);
            SBAR();
#pragma unroll
            for (int u = 0; u < 4; ++u) {
                const f32x4 t = (pr[u] + (f32x4){O[3][4 * u], O[3][4 * u + 1], O[3][4 * u + 2], O[3][4 * u + 3]}) + (pr[4 + u] + pr[8 + u]);
                o[4 * u] = t[0]; o[4 * u + 1] = t[1]; o[4 * u + 2] = t[2]; o[4 * u + 3] = t[3];
            }
        }
        float l = from_hi(o[12]);
        {
            f32x2 chk = {0.f, 0.f};
#pragma unroll
            for (int i = 0; i < 16; i += 2) chk = __builtin_elementwise_fma((f32x2){o[i], o[i + 1]}, (f32x2){0.f, 0.f}, chk);
            bool bad = !(chk[0] + chk[1] == 0.f) || !(l >= 1e-32f && l <= 1e30f);
            if (__any(bad)) {
                float mrun = -3.0e38f;
                o = f32x16{};
#pragma unroll 1
                for (int k2 = 0; k2 < 4; ++k2) {
                    poll(rws, WS_F1 + (((lane & 8) ? gv : gk) * 32 + 8 * k2 + (lane & 7)) * 128, tag);
#pragma unroll 1
                    for (int m2 = 0; m2 < 16; ++m2) {
                        const unsigned so = kvb + (k2 * 16 + m2) * 6144;
                        const u32x4 k0 = ld16(rws, lane * 16, so), k1 = ld16(rws, lane * 16 + 1024, so), v0 = ld16(rws, lane * 16 + 2048, so), v1 = ld16(rws, lane * 16 + 3072, so);
                        f32x16 s = MFMA(asbf(k0), asbf(q0), f32x16{}); s = MFMA(asbf(k1), asbf(q1), s);
                        const float mn = __builtin_fmaxf(mrun, swap_max(max16(s))), al = __builtin_amdgcn_exp2f(mrun - mn);
                        mrun = mn;
#pragma unroll
                        for (int i = 0; i < 16; ++i) { s[i] = __builtin_amdgcn_exp2f(s[i] - mn); o[i] *= al; }
                        o = MFMA(asbf(v0), asbf(pk8(s, 0)), o); o = MFMA(asbf(v1), asbf(pk8(s, 1)), o);
                    }
                }
                l = from_hi(o[12]);
            }
        }
        const float rl = __builtin_amdgcn_rcpf(l);
#pragma unroll
        for (int i = 0; i < 16; ++i) o[i] *= rl;
        u32x4 o0 = pk8(o, 0), o1 = pk8(o, 1);
        if (hi) { o1[2] = 0; o1[3] = 0; }
        const unsigned so = WS_O + ((tq * 16 + head) * 2) * 1024;
        st16(rws, o0, lane * 16, so); st16(rws, o1, lane * 16 + 1024, so);
        sig(C_ATT, lane);
        STAMP(8); FSTAMPC(27);
        const u32x2 fl0 = ldflag(rws, WS_FO + (tto * 16 + (w >> 1) * 8 + ((lane + ct) & 7)) * 128);
        SBAR();
        WAIT_VM(0);
        rdy0 = ready8(fl0, tag);
        asm volatile("" : "+s"(rdy0) :: "memory");
        if (lane == 0) __builtin_amdgcn_raw_buffer_store_b64(tag, rws, WS_FO + (tq * 16 + head) * 128, 0, 16);
        CBAR();
        if (r < 28) { waitc<0>(C_WO0, 4); read_wo(wf, tr_addr128(L_R0, 0, (w >> 1) * 224 + 4 * (lane >> 5), lane), tr_addr128(L_R0, 64, (w >> 1) * 224 + 4 * (lane >> 5), lane), ct); }
        FSTAMP(9);
    }

    if (r < 28) {
        const int br = role, tp = w & 1, kh = w >> 1;
        const rsrc_t rout = mk_rsrc(ka[14], ka[15], 2048 * 1792);
        const unsigned ffo = WS_FO + (br * 1024 + tto * 16 + kh * 8 + ((lane + ct) & 7)) * 128;
        const unsigned ob = WS_O + br * (2u << 20) + (tto * 16 + kh * 8) * 2048;
        u32x4 of[16];
        f32x16 c0 = {}, c1 = {};
        if (role) {
            poll<HSLP>(rws, ffo, tag);
#pragma unroll
            for (int k = 0; k < 16; ++k) {
                if (k == 8) WAIT_VM(GWIN);
                of[k] = ld16(rws, lane * 16 + (k & 3) * 1024, ob + (k >> 2) * 4096); SBAR();
            }
            waitc<4>(C_ATT, 4);
            __builtin_amdgcn_s_setprio(0);
#pragma unroll
            for (int k = 0; k < 16; ++k) { c0 = MFMA(wf[k][0], asbf(of[k]), c0); c1 = MFMA(wf[k][1], asbf(of[k]), c1); }
        } else {
            unsigned req = 0, fin = 0, rdy = rdy0;
#pragma unroll
            for (int k = 0; k < 16; ++k) asm volatile("" : "=v"(of[k]));
#pragma clang loop unroll(disable)
            for (unsigned it = 0; it < SPIN_CAP; ++it) {
                const unsigned nw = rdy & ~req;
#pragma unroll
                for (int k = 0; k < 8; ++k)
                    if (nw & (1u << k)) { const unsigned sb = ob + ((k + ct) & 7) * 2048; of[2 * k] = ld16(rws, lane * 16, sb); of[2 * k + 1] = ld16(rws, lane * 16 + 1024, sb); CBAR(); }
                req |= nw;
                const u32x2 f = ldflag(rws, ffo);
                CBAR();
                const unsigned todo = (req ^ (req + 1u)) >> 1 & ~fin;
#pragma unroll
                for (int k = 0; k < 8; ++k)
                    if (todo & (1u << k)) {
                        c0 = MFMA(wf[2 * k][0], asbf(of[2 * k]), c0); c1 = MFMA(wf[2 * k][1], asbf(of[2 * k]), c1);
                        c0 = MFMA(wf[2 * k + 1][0], asbf(of[2 * k + 1]), c0); c1 = MFMA(wf[2 * k + 1][1], asbf(of[2 * k + 1]), c1); CBAR();
                    }
                fin |= todo;
                if (fin == 255u) break;
                rdy = ready8(f, tag);
                CBAR();
            }
            sig(C_GATH, lane);
        }
        STAMP(10);
        const unsigned xb_ = L_R1 + br * 16384;
        const unsigned vo = (tto * 32 + (lane & 31)) * 1792 + (hi ? 16 : 0), so = br * 896 + ct * 128 + kh * 64;
#define FINISH(KEEP, GIVE) do { \
            _Pragma("unroll") for (int u = 0; u < 4; ++u) lds_wrf4(xb_ + (w * 4 + u) * 1024 + lane * 16, (f32x4){GIVE[4 * u], GIVE[4 * u + 1], GIVE[4 * u + 2], GIVE[4 * u + 3]}); \
            WAIT_LGKM0(); sig(C_EX0 + br * 2 + tp, lane); waitc<0>(C_EX0 + br * 2 + tp, 2); \
            _Pragma("unroll") for (int u = 0; u < 4; ++u) { const f32x4 s = lds_rdf4(xb_ + ((w ^ 2) * 4 + u) * 1024 + lane * 16); \
                _Pragma("unroll") for (int i = 0; i < 4; ++i) KEEP[4 * u + i] += s[i]; } \
            if (role) waitc<1>(C_GATH, 4); \
            _Pragma("unroll") for (int gp = 0; gp < 2; ++gp) { \
                auto s0 = __builtin_amdgcn_permlane32_swap(pk2(KEEP[8 * gp], KEEP[8 * gp + 1]), pk2(KEEP[8 * gp + 4], KEEP[8 * gp + 5]), false, false); \
                auto s1 = __builtin_amdgcn_permlane32_swap(pk2(KEEP[8 * gp + 2], KEEP[8 * gp + 3]), pk2(KEEP[8 * gp + 6], KEEP[8 * gp + 7]), false, false); \
                st16a<0>(rout, (u32x4){s0[0], s1[0], s0[1], s1[1]}, vo + gp * 32, so); } } while (0)
        if (kh) FINISH(c1, c0); else FINISH(c0, c1);
        STAMP(12);
    }
    STAMP_FLUSH();
    if (wid == 0 * 4) { const unsigned d_ = ((unsigned)__builtin_amdgcn_readlane(stv, 3) - (unsigned)__builtin_amdgcn_readlane(stv, 0)) / 1;
        unsigned long long r0_ = __builtin_amdgcn_s_memrealtime(), dur_ = (d_ > 20000u ? 20000u : d_) * 10ull;
        if (__builtin_amdgcn_readlane(stv, 3) != 0) while (__builtin_amdgcn_s_memrealtime() - r0_ < dur_) __builtin_amdgcn_s_sleep(4); }
    asm volatile(".Lpf_end:");
}

extern "C" void kernel_launch(void* const* d_in, const int* in_sizes, int n_in, void* d_out, int out_size, void* d_ws, size_t ws_size,
                              hipStream_t stream) {
    (void)in_sizes; (void)n_in; (void)out_size; (void)ws_size;
    (void)hipFuncSetAttribute((const void*)fused_fwd, hipFuncAttributeMaxDynamicSharedMemorySize, LDS_BYTES);
    fused_fwd<<<256, 512, LDS_BYTES, stream>>>(d_in[0], d_in[1], d_in[2], d_in[3], d_in[4], d_in[5], d_in[6], d_out, d_ws);
}
```

```cpp
#include <hip/hip_runtime.h>
#include <hip/hip_bf16.h>
#include <stdint.h>
#include <type_traits>

typedef __attribute__((ext_vector_type(8))) __bf16 bf16x8;
typedef __attribute__((ext_vector_type(2))) __bf16 bf16x2;
typedef __attribute__((ext_vector_type(2))) float f32x2;
typedef __attribute__((ext_vector_type(4))) float f32x4;
typedef __attribute__((ext_vector_type(16))) float f32x16;
typedef __attribute__((ext_vector_type(4))) unsigned u32x4;
typedef __attribute__((ext_vector_type(2))) unsigned u32x2;
typedef __attribute__((ext_vector_type(4))) short s16x4;
typedef __amdgpu_buffer_rsrc_t rsrc_t;
#define LAS __attribute__((address_space(3)))
#define DI __device__ __forceinline__

constexpr unsigned WS_EPOCH = 0;
constexpr unsigned WS_F1 = 4096;
constexpr unsigned WS_F2 = WS_F1 + 512 * 128;
constexpr unsigned WS_F3 = WS_F2 + 512 * 128;
constexpr unsigned WS_FO = WS_F3 + 256 * 128;
constexpr unsigned WS_FP = WS_FO + 2048 * 128;
constexpr unsigned WS_QKV = 1u << 20;
constexpr unsigned WS_KVP = WS_QKV + (6u << 20);
constexpr unsigned WS_KVR = WS_KVP + (2u << 20);
constexpr unsigned WS_O = WS_KVR + (1u << 20);
constexpr unsigned WS_DBG = WS_O + (4u << 20);
constexpr unsigned WS_BYTES = WS_DBG + (1u << 20);

constexpr unsigned SLOT = 37888;
constexpr unsigned L_R0 = 0;
constexpr unsigned L_R1 = 57856;
constexpr unsigned L_R2 = L_R1 + 32768;
constexpr unsigned L_QX = L_R2 + 49152;
constexpr unsigned L_KB = 16384;
constexpr unsigned L_QB = 3 * SLOT;
constexpr unsigned L_PHI = 4 * SLOT + 64;
constexpr unsigned L_JUNK = L_PHI + 4096;
constexpr unsigned L_CNT = L_JUNK + 1024;
constexpr unsigned L_TAG = L_CNT + 256;
constexpr unsigned LDS_BYTES = L_TAG + 16;
enum { C_LAND0 = 24, C_DONE0 = 32, C_QB = 4, C_QX = 10, C_PV = 11, C_KVDONE = 12, C_WO0 = 13, C_WOFREE = 15, C_EX0 = 16, C_CMB = 20, C_ATT = 21, C_GATH = 22, C_TAG = 23, C_PF = 40 };

#define PNX 3
#define PNW 3
#define OUTAUX 0
#define HSLP 8
#define WOWIN 3
#define GWIN 0
constexpr unsigned SPIN_CAP = 1u << 16;
constexpr float QSCALE = 0.18898223650461362f * 1.4426950408889634f;
constexpr float LOG2E = 1.4426950408889634f;
constexpr float REF_SHIFT = 100.f;

#define MFMA(a, b, c) __builtin_amdgcn_mfma_f32_32x32x16_bf16((a), (b), (c), 0, 0, 0)
#define CBAR() asm volatile("" ::: "memory")
#define WAIT_VM(n) asm volatile("s_waitcnt vmcnt(%0)" :: "i"(n) : "memory")
#define WAIT_LGKM0() asm volatile("s_waitcnt lgkmcnt(0)" ::: "memory")
#define SBAR() __builtin_amdgcn_sched_barrier(0)

#define STAMP_DECL
#define STAMP(i) do {} while (0)
#define STAMPC(i) do {} while (0)
#define STAMP_FLUSH() do {} while (0)
#define FSTAMP(i) do {} while (0)
#define FSTAMPC(i) do {} while (0)
#define PIN(x) asm volatile("" : "+v"(x))

DI int fresh_lane() { int l = __builtin_amdgcn_mbcnt_hi(~0u, __builtin_amdgcn_mbcnt_lo(~0u, 0u)); asm volatile("" : "+v"(l)); return l; }
DI unsigned pk2(float a, float b) { f32x2 v = {a, b}; return __builtin_bit_cast(unsigned, __builtin_convertvector(v, bf16x2)); }
DI u32x4 pk8(const f32x16& a, int s) {
    return (u32x4){pk2(a[8 * s], a[8 * s + 1]), pk2(a[8 * s + 2], a[8 * s + 3]), pk2(a[8 * s + 4], a[8 * s + 5]), pk2(a[8 * s + 6], a[8 * s + 7])}; }
DI bf16x8 asbf(u32x4 v) { return __builtin_bit_cast(bf16x8, v); }
DI float swap_max(float m) { auto r = __builtin_amdgcn_permlane32_swap(__float_as_uint(m), __float_as_uint(m), false, false);
    return __builtin_amdgcn_fmed3f(__uint_as_float(r[0]), __uint_as_float(r[1]), __builtin_inff()); }
DI float swap_min(float m) { auto r = __builtin_amdgcn_permlane32_swap(__float_as_uint(m), __float_as_uint(m), false, false);
    return __builtin_amdgcn_fmed3f(__uint_as_float(r[0]), __uint_as_float(r[1]), -__builtin_inff()); }
DI float swap_sum(float m) { auto r = __builtin_amdgcn_permlane32_swap(__float_as_uint(m), __float_as_uint(m), false, false);
    return __uint_as_float(r[0]) + __uint_as_float(r[1]); }
DI float from_hi(float m) { auto r = __builtin_amdgcn_permlane32_swap(__float_as_uint(m), __float_as_uint(m), false, false);
    return __uint_as_float(r[1]); }
DI float max16(const f32x16& s) {
    float a = __builtin_fmaxf(__builtin_fmaxf(__builtin_amdgcn_fmed3f(s[0], s[1], __builtin_inff()), s[2]), s[15]), b = __builtin_fmaxf(__builtin_fmaxf(s[3], s[4]), s[5]);
    a = __builtin_fmaxf(__builtin_fmaxf(a, s[6]), s[7]); b = __builtin_fmaxf(__builtin_fmaxf(b, s[8]), s[9]);
    a = __builtin_fmaxf(__builtin_fmaxf(a, s[10]), s[11]); b = __builtin_fmaxf(__builtin_fmaxf(b, s[12]), s[13]);
    return __builtin_fmaxf(__builtin_fmaxf(a, s[14]), b); }
DI float min16(const f32x16& s) {
    float a = __builtin_fminf(__builtin_fminf(__builtin_amdgcn_fmed3f(s[0], s[1], -__builtin_inff()), s[2]), s[15]), b = __builtin_fminf(__builtin_fminf(s[3], s[4]), s[5]);
    a = __builtin_fminf(__builtin_fminf(a, s[6]), s[7]); b = __builtin_fminf(__builtin_fminf(b, s[8]), s[9]);
    a = __builtin_fminf(__builtin_fminf(a, s[10]), s[11]); b = __builtin_fminf(__builtin_fminf(b, s[12]), s[13]);
    return __builtin_fminf(__builtin_fminf(a, s[14]), b); }

DI void dma(rsrc_t rs, unsigned voff, unsigned soff, unsigned lds) {
    asm volatile("s_mov_b32 m0, %0\n\ts_nop 0\n\tbuffer_load_dwordx4 %1, %2, %3 offen lds" :: "s"(lds), "v"(voff), "s"(rs), "s"(soff) : "memory"); }
DI u32x4 lds_rd128(unsigned a) { return *(LAS const u32x4*)(uintptr_t)a; }
DI f32x4 lds_rdf4(unsigned a) { return *(LAS const f32x4*)(uintptr_t)a; }
DI void lds_wr128(unsigned a, u32x4 v) { *(LAS u32x4*)(uintptr_t)a = v; }
DI void lds_wrf4(unsigned a, f32x4 v) { *(LAS f32x4*)(uintptr_t)a = v; }
DI void lds_wr64(unsigned a, u32x2 v) { *(LAS u32x2*)(uintptr_t)a = v; }
DI bf16x8 lds_tr2(unsigned a0, unsigned a1) {
    s16x4 lo = __builtin_amdgcn_ds_read_tr16_b64_v4i16((LAS s16x4*)(uintptr_t)a0), hi = __builtin_amdgcn_ds_read_tr16_b64_v4i16((LAS s16x4*)(uintptr_t)a1);
    return __builtin_bit_cast(bf16x8, __builtin_shufflevector(lo, hi, 0, 1, 2, 3, 4, 5, 6, 7)); }
DI void sig(int c, int) {
    asm volatile("s_mov_b64 exec, 1\n\tds_add_u32 %0, %1\n\ts_mov_b64 exec, -1" :: "v"(L_CNT + 4 * c), "v"(1u) : "memory"); }
template <int SLP = 1> DI void waitc(int c, unsigned n) {
    unsigned v, sv, it;
    asm volatile("s_mov_b32 %2, %5\n"
                 "1:\n\tds_read_b32 %0, %3\n\ts_waitcnt lgkmcnt(0)\n\tv_readfirstlane_b32 %1, %0\n\ts_cmp_ge_u32 %1, %4\n\ts_cbranch_scc1 2f\n\t"
                 "s_sleep %6\n\ts_sub_u32 %2, %2, 1\n\ts_cbranch_scc0 1b\n2:"
                 : "=&v"(v), "=&s"(sv), "=&s"(it) : "v"(L_CNT + 4 * c), "i"(n), "i"(SPIN_CAP), "i"(SLP) : "memory", "scc"); }
DI unsigned rd_cnt(int c) { return *(volatile LAS const unsigned*)(uintptr_t)(L_CNT + 4 * c); }
DI u32x2 rd_tag() { const u32x2 t = *(LAS const u32x2*)(uintptr_t)L_TAG;
    return (u32x2){(unsigned)__builtin_amdgcn_readfirstlane(t[0]), (unsigned)__builtin_amdgcn_readfirstlane(t[1])}; }
DI u32x4 ld16(rsrc_t rs, unsigned voff, unsigned soff) { return __builtin_amdgcn_raw_buffer_load_b128(rs, voff, soff, 16); }
template <int AUX> DI void st16a(rsrc_t rs, u32x4 v, unsigned voff, unsigned soff) {
    __builtin_amdgcn_raw_buffer_store_b128(v, rs, voff, soff, AUX); asm volatile("s_nop 1" :: "v"(v) : "memory"); }
DI void st16(rsrc_t rs, u32x4 v, unsigned voff, unsigned soff) { st16a<16>(rs, v, voff, soff); }
DI u32x2 ldflag(rsrc_t rs, unsigned voff) { return __builtin_amdgcn_raw_buffer_load_b64(rs, voff, 0, 16); }
DI bool tagok(u32x2 f, u32x2 tag) {
    return __builtin_amdgcn_ballot_w64(__builtin_bit_cast(unsigned long long, f) == __builtin_bit_cast(unsigned long long, tag)) == ~0ull; }
template <int SLP = 2> DI void poll(rsrc_t rs, unsigned voff, u32x2 tag) {
    CBAR();
#pragma clang loop unroll(disable)
    for (unsigned it = 0; it < SPIN_CAP; ++it) { u32x2 f = ldflag(rs, voff); if (tagok(f, tag)) break; __builtin_amdgcn_s_sleep(SLP); CBAR(); }
    CBAR(); }
DI void poll_fast(rsrc_t rs, unsigned voff, u32x2 tag, u32x2& a, u32x2& b, u32x2& c) {
    unsigned it;
    asm volatile("s_mov_b32 %3, %7\n\tbuffer_load_dwordx2 %0, %4, %5, 0 offen sc1\n\ts_nop 7\n\ts_nop 7\n\tbuffer_load_dwordx2 %1, %4, %5, 0 offen sc1\n\ts_nop 7\n\ts_nop 7\n"
                 "1:\n\tbuffer_load_dwordx2 %2, %4, %5, 0 offen sc1\n\ts_waitcnt vmcnt(2)\n\tv_cmp_eq_u64 vcc, %6, %0\n\ts_cmp_eq_u64 vcc, -1\n\ts_cbranch_scc1 2f\n\t"
                 "buffer_load_dwordx2 %0, %4, %5, 0 offen sc1\n\ts_waitcnt vmcnt(2)\n\tv_cmp_eq_u64 vcc, %6, %1\n\ts_cmp_eq_u64 vcc, -1\n\ts_cbranch_scc1 2f\n\t"
                 "buffer_load_dwordx2 %1, %4, %5, 0 offen sc1\n\ts_waitcnt vmcnt(2)\n\tv_cmp_eq_u64 vcc, %6, %2\n\ts_cmp_eq_u64 vcc, -1\n\ts_cbranch_scc1 2f\n\t"
                 "s_sub_u32 %3, %3, 1\n\ts_cbranch_scc0 1b\n2:"
                 : "=&v"(a), "=&v"(b), "=&v"(c), "=&s"(it) : "v"(voff), "s"(rs), "s"(tag), "i"(SPIN_CAP) : "memory", "vcc", "scc");
}
DI void post(rsrc_t rs, unsigned off, u32x2 tag, int lane) {
    WAIT_VM(0);
    if (lane == 0) __builtin_amdgcn_raw_buffer_store_b64(tag, rs, off, 0, 16);
    CBAR(); }

#define GB(n, k) "v[176+20*" #n "+4*" #k ":179+20*" #n "+4*" #k "]"
#define GT(A, n, t, i) "v_mfma_f32_32x32x16_bf16 %[" #A "], " GB(n, (2+i)) ", " GB(n, t) ", %[" #A "]\n\t"
#define GD(A, n, t, i) "v_mfma_f32_32x32x16_bf16 %[" #A "], " GB(n, t) ", " GB(n, (2+i)) ", %[" #A "]\n\t"
#define GX(n, t, X) "ds_read_b128 " GB(n, t) ", " X " offset:4096*" #t "\n\t"
#define GF(n, i, W, O) "ds_read_b64_tr_b16 v[176+20*" #n "+8+4*" #i ":177+20*" #n "+8+4*" #i "], " W " offset:" O "\n\t" \
                       "ds_read_b64_tr_b16 v[178+20*" #n "+8+4*" #i ":179+20*" #n "+8+4*" #i "], " W " offset:" O "+336\n\t"
#define GSTEP(M0, M1, M2, c, n, X, O, P0, P0B, P2, P4, P5) \
    M0(a0, c, 0, 0) P0 GX(n, 0, X) P0B M0(a3, c, 1, 0) GF(n, 0, "%[wa]", O) M1(a1, c, 0, 1) GX(n, 1, X) P2 M1(a4, c, 1, 1) GF(n, 1, "%[wb]", O) \
    M2(a2, c, 0, 2) GF(n, 2, "%[wc]", O) P4 M2(a5, c, 1, 2) P5
#define GLOOP(M0, M1, M2) \
    GX(0, 0, "%[xa]") GF(0, 0, "%[wa]", "0") GX(0, 1, "%[xa]") GF(0, 1, "%[wb]", "0") GF(0, 2, "%[wc]", "0") \
    GX(1, 0, "v170") GF(1, 0, "%[wa]", "5376") GX(1, 1, "v170") GF(1, 1, "%[wb]", "5376") GF(1, 2, "%[wc]", "5376") \
    "s_waitcnt lgkmcnt(8)\n" \
    "1:\n\t" \
    GSTEP(M0, M1, M2, 0, 2, "v171", "10752", "", "ds_read_b128 v[166:169], %[la]\n\t", "v_xor_b32 v172, 0x60, %[xa]\n\t", "s_and_b32 %[so], %[g], 3\n\t", \
          "s_mul_i32 %[so], %[so], %[slot]\n\ts_waitcnt lgkmcnt(9)\n\t") \
    GSTEP(M0, M1, M2, 1, 3, "v172", "16128", "", "v_add_u32 %[xa], %[so], %[xa0]\n\ts_waitcnt lgkmcnt(8)\n\t", "v_min3_u32 v173, v166, v167, v168\n\tv_xor_b32 v170, 32, %[xa]\n\t", \
          "v_min_u32 v173, v173, v169\n\tv_add_u32 %[wa], %[so], %[wa0]\n\t", "v_add_u32 %[wb], %[so], %[wb0]\n\tv_readfirstlane_b32 %[rf], v173\n\tv_add_u32 %[wc], %[so], %[wc0]\n\ts_waitcnt lgkmcnt(8)\n\t") \
    GSTEP(M0, M1, M2, 2, 0, "%[xa]", "0", "s_cmp_gt_u32 %[rf], %[g]\n\ts_cbranch_scc0 3f\n2:\n\t", "", "v_xor_b32 v171, 64, %[xa]\n\t", "", \
          "s_waitcnt lgkmcnt(8)\n\tv_mov_b32 v174, %[g]\n\t") \
    GSTEP(M0, M1, M2, 3, 1, "v170", "5376", "", "s_add_u32 %[g], %[g], 1\n\t", "s_cmp_lt_u32 %[g], 6\n\t", "", \
          "s_waitcnt lgkmcnt(8)\n\tds_write_b32 %[da], v174\n\ts_cbranch_scc1 1b\n\t") \
    "s_branch 4f\n" \
    "3:\n\ts_mov_b32 %[so], %[cap]\n" \
    "5:\n\tds_read_b128 v[166:169], %[la]\n\ts_waitcnt lgkmcnt(0)\n\tv_min3_u32 v173, v166, v167, v168\n\tv_min_u32 v173, v173, v169\n\ts_nop 0\n\tv_readfirstlane_b32 %[rf], v173\n\ts_cmp_gt_u32 %[rf], %[g]\n\ts_cbranch_scc1 2b\n\t" \
    "s_sub_u32 %[so], %[so], 1\n\ts_cbranch_scc0 5b\n\ts_branch 2b\n" \
    "4:\n\ts_waitcnt lgkmcnt(0)\n\ts_nop 7"
struct Pre { u32x4 x[2][2], f[2]; unsigned xa, wa, wb, wc; };
template <int ND, class Start>
DI void gemm6(unsigned xa0, unsigned wa0, unsigned wb0, unsigned wc0, int w, f32x16 (&acc)[6], Pre& p, Start start) {
    unsigned g = 1, so, rf;
    p.xa = xa0; p.wa = wa0; p.wb = wb0; p.wc = wc0;
    const unsigned la = L_CNT + 4 * C_LAND0, da = L_CNT + 4 * (C_DONE0 + w);
#pragma unroll
    for (int i = 0; i < 6; ++i) { acc[i] = f32x16{}; PIN(acc[i]); }
    PIN(p.xa); PIN(p.wa); PIN(p.wb); PIN(p.wc);
    start();
#define GASM(M0, M1, M2) asm volatile("v_xor_b32 v170, 32, %[xa]\n\tv_xor_b32 v171, 64, %[xa]\n\t" GLOOP(M0, M1, M2) \
        : [a0] "+v"(acc[0]), [a1] "+v"(acc[1]), [a2] "+v"(acc[2]), [a3] "+v"(acc[3]), [a4] "+v"(acc[4]), [a5] "+v"(acc[5]), \
          [xa] "+v"(p.xa), [wa] "+v"(p.wa), [wb] "+v"(p.wb), [wc] "+v"(p.wc), [g] "+s"(g), [so] "=&s"(so), [rf] "=&s"(rf), \
          "=&{v[176:179]}"(p.x[0][0]), "=&{v[180:183]}"(p.x[0][1]), "=&{v[184:187]}"(p.f[0]), \
          "=&{v[196:199]}"(p.x[1][0]), "=&{v[200:203]}"(p.x[1][1]), "=&{v[204:207]}"(p.f[1]) \
        : [xa0] "v"(xa0), [wa0] "v"(wa0), [wb0] "v"(wb0), [wc0] "v"(wc0), [la] "v"(la), [da] "v"(da), [slot] "i"(SLOT), [cap] "i"(SPIN_CAP) \
        : "memory", "scc", "v166", "v167", "v168", "v169", "v170", "v171", "v172", "v173", "v174", "v175", "v188", "v189", "v190", "v191", "v192", "v193", "v194", "v195", "v208", "v209", "v210", "v211", "v212", "v213", "v214", "v215", "v216", "v217", "v218", "v219", "v220", "v221", "v222", "v223", "v224", "v225", "v226", "v227", "v228", "v229", "v230", "v231", "v232", "v233", "v234", "v235", "v236", "v237", "v238", "v239", "v240", "v241", "v242", "v243", "v244", "v245", "v246", "v247", "v248", "v249", "v250", "v251", "v252", "v253", "v254", "v255")
    if constexpr (ND == 0) GASM(GT, GT, GT); else if constexpr (ND == 1) GASM(GT, GT, GD); else GASM(GD, GD, GD);
}
template <class Rd> DI void gatherv(unsigned c, unsigned n, Rd rd) {
#pragma clang loop unroll(disable)
    for (unsigned it = 0; it < SPIN_CAP; ++it) {
        CBAR();
        const unsigned v = *(volatile LAS const unsigned*)(uintptr_t)c;
        SBAR(); CBAR();
        rd();
        SBAR();
        if ((unsigned)__builtin_amdgcn_readfirstlane(v) >= n) break;
    }
    CBAR(); }
template <class Rd> DI void gather(int c, unsigned n, Rd rd) { gatherv(L_CNT + 4 * c, n, rd); }
DI void sigv(unsigned a) { asm volatile("s_mov_b64 exec, 1\n\tds_add_u32 %0, %1\n\ts_mov_b64 exec, -1" :: "v"(a), "v"(1u) : "memory"); }
DI unsigned min4(u32x4 d) { return __builtin_amdgcn_readfirstlane(min(min(d[0], d[1]), min(d[2], d[3]))); }
DI u32x4 rd4(int c) { return *(volatile LAS const u32x4*)(uintptr_t)(L_CNT + 4 * c); }
DI void wait4(int c, unsigned n) {
    CBAR();
#pragma clang loop unroll(disable)
    for (unsigned it = 0; it < SPIN_CAP; ++it) if (min4(rd4(c)) >= n) break;
    CBAR(); }
template <int P> DI void dma_x4(rsrc_t rs, unsigned voff, unsigned soff, unsigned lds) {
    asm volatile("s_mov_b32 m0, %1\n\ts_nop %4\n\tbuffer_load_dwordx4 %2, %3, %0 offen lds\n\t"
                 "s_add_u32 m0, m0, 0x1000\n\ts_add_u32 %0, %0, 28672\n\ts_nop %4\n\tbuffer_load_dwordx4 %2, %3, %0 offen lds\n\t"
                 "s_add_u32 m0, m0, 0x1000\n\ts_add_u32 %0, %0, 28672\n\ts_nop %4\n\tbuffer_load_dwordx4 %2, %3, %0 offen lds\n\t"
                 "s_add_u32 m0, m0, 0x1000\n\ts_add_u32 %0, %0, 28672\n\ts_nop %4\n\tbuffer_load_dwordx4 %2, %3, %0 offen lds"
                 : "+s"(soff) : "s"(lds), "v"(voff), "s"(rs), "i"(P) : "memory", "scc"); }
template <int P> DI void dma_w5(rsrc_t rs, const unsigned (&v)[6], unsigned soff, unsigned lds) {
    asm volatile("s_mov_b32 m0, %0\n\ts_nop %8\n\tbuffer_load_dwordx4 %3, %1, %2 offen lds\n\t"
                 "s_add_u32 m0, m0, 0x1000\n\ts_nop %8\n\tbuffer_load_dwordx4 %4, %1, %2 offen lds\n\t"
                 "s_add_u32 m0, m0, 0x1000\n\ts_nop %8\n\tbuffer_load_dwordx4 %5, %1, %2 offen lds\n\t"
                 "s_add_u32 m0, m0, 0x1000\n\ts_nop %8\n\tbuffer_load_dwordx4 %6, %1, %2 offen lds\n\t"
                 "s_add_u32 m0, m0, 0x1000\n\ts_nop %8\n\tbuffer_load_dwordx4 %7, %1, %2 offen lds"
                 :: "s"(lds), "s"(rs), "s"(soff), "v"(v[0]), "v"(v[1]), "v"(v[2]), "v"(v[3]), "v"(v[4]), "i"(P) : "memory", "scc"); }
DI unsigned tr_addr128(unsigned base, int cb, int rowgrp, int lane) {
    const int q = (lane & 15) >> 2, byte = cb + 32 * ((lane >> 4) & 1) + 8 * (lane & 3);
    return base + (rowgrp + q) * 128 + ((((byte >> 4) ^ ((q >> 1) << 2)) & 7) << 4) + (byte & 8);
}
DI void feat(const f32x16& p, f32x16& e, f32x16& g) {
    const float mx = swap_max(max16(p)) * LOG2E, mn = swap_min(min16(p)) * LOG2E;
#pragma unroll
    for (int i = 0; i < 16; i += 2) {
        const f32x2 pp = {p[i], p[i + 1]};
        const f32x2 a = __builtin_elementwise_fma(pp, (f32x2){LOG2E, LOG2E}, (f32x2){-mx, -mx}), c = __builtin_elementwise_fma(pp, (f32x2){-LOG2E, -LOG2E}, (f32x2){mn, mn});
        e[i] = __builtin_amdgcn_exp2f(a[0]); e[i + 1] = __builtin_amdgcn_exp2f(a[1]); g[i] = __builtin_amdgcn_exp2f(c[0]); g[i + 1] = __builtin_amdgcn_exp2f(c[1]);
    }
}

typedef __attribute__((ext_vector_type(16))) unsigned u32x16;
DI rsrc_t mk_rsrc(unsigned lo, unsigned hi, unsigned bytes) {
    return __builtin_amdgcn_make_buffer_rsrc((void*)(((unsigned long long)hi << 32) | lo), 0, bytes, 0x00020000); }
DI void read_wo(bf16x8 (&wf)[16][2], unsigned ta0, unsigned ta1, int rot) {
#pragma unroll
    for (int k = 0; k < 8; ++k) {
        const unsigned ro = ((k + rot) & 7) * 28 * 128, b0 = ta0 + ro, b1 = ta1 + ro;
        wf[2 * k][0] = lds_tr2(b0, b0 + 1024); wf[2 * k][1] = lds_tr2(b1, b1 + 1024);
        wf[2 * k + 1][0] = lds_tr2(b0 + 2048, b0 + 3072); wf[2 * k + 1][1] = lds_tr2(b1 + 2048, b1 + 3072);
    }
}
DI void ones_half(u32x4& a, u32x2 c) {
    u32x2 a0 = {a[0], a[1]}, a1 = {a[2], a[3]};
    asm volatile("s_mov_b64 exec, %2\n\tv_mov_b64 %0, %3\n\tv_mov_b64 %1, %3\n\ts_mov_b64 exec, -1" : "+v"(a0), "+v"(a1) : "s"(0xF0000000F0000000ull), "v"(c));
    a = (u32x4){a0[0], a0[1], a1[0], a1[1]}; }
DI u32x4 lds_tr2u(unsigned a) { return __builtin_bit_cast(u32x4, lds_tr2(a, a + 336)); }
template <int ND, bool ISB>
DI void tail1(f32x16 (&acc)[6], Pre& p, rsrc_t rws, const unsigned (&so)[3], const unsigned (&pad0)[3], int w) {
    u32x4 x[8][2], f[4], v0, v1;
    const int lane = fresh_lane();
    const bool hi = lane >= 32;
    unsigned pad[3] = {pad0[0], pad0[1], pad0[2]}, l16 = lane * 16;
    u32x2 oc; oc[0] = oc[1] = (lane & 31) == 28 ? 0x3F803F80u : 0u;
    const unsigned xb = p.xa + SLOT, wadr[3][2] = {{p.wa, p.wa + SLOT}, {p.wb, p.wb + SLOT}, {p.wc, p.wc + SLOT}};
    unsigned xad[8] = {0, 0, p.xa ^ 64, p.xa ^ 96, xb, xb ^ 32, xb ^ 64, xb ^ 96};
    x[0][0] = p.x[0][0]; x[0][1] = p.x[0][1]; x[1][0] = p.x[1][0]; x[1][1] = p.x[1][1]; f[0] = p.f[0]; f[1] = p.f[1];
    const u32x4 ld = rd4(C_LAND0);
    f[2] = lds_tr2u(p.wa + 2 * 5376);
    if (ND < 3 && !ISB) { PIN(pad[0]); PIN(pad[1]); PIN(pad[2]); }
    u32x4 dn;
    if (ND > 0) PIN(oc);
    PIN(l16);
    SBAR();
    auto emit = [&](auto I, auto K) {
        constexpr int i = decltype(I)::value, k = decltype(K)::value, t = k >> 3, j = k & 7;
        constexpr bool D = i >= 3 - ND;
        const f32x16& a = acc[3 * t + i];
        if constexpr (j == 1) { v0[0] = pk2(a[0], a[1]); v0[1] = pk2(a[2], a[3]); PIN(v0); }
        if constexpr (j == 2) { v0[2] = pk2(a[4], a[5]); v0[3] = pk2(a[6], a[7]); PIN(v0); }
        if constexpr (j == 3) { if (D) ones_half(v0, oc); v1[0] = pk2(a[8], a[9]); PIN(v1); }
        if constexpr (j == 4) { if (ISB) lds_wr128(l16 + so[i] + t * 2048, v0); else st16(rws, v0, l16, so[i] + t * 6144); }
        if constexpr (j == 5) { v1[1] = pk2(a[10], a[11]); v1[2] = pk2(a[12], a[13]); v1[3] = pk2(a[14], a[15]); PIN(v1); }
        if constexpr (j == 6) { if (D) ones_half(v1, oc); else if (!ISB) { v1[2] = hi ? pad[i] : v1[2]; v1[3] = hi ? 0u : v1[3]; } PIN(v1); }
        if constexpr (j == 7) { if (ISB) lds_wr128(l16 + so[i] + t * 2048 + 1024, v1); else st16(rws, v1, l16 + 1024, so[i] + t * 6144); }
        if constexpr (ISB && k == 15) sig(C_QB + w, 0); };
#define TMM(i, s, t) acc[3 * t + i] = (i >= 3 - ND) ? MFMA(asbf(x[s][t]), asbf(f[s & 3]), acc[3 * t + i]) : MFMA(asbf(f[s & 3]), asbf(x[s][t]), acc[3 * t + i]); SBAR()
#define IC(n) (std::integral_constant<int, (n)>{})
#define TSTEP(i, s) do { constexpr int n = 8 * i + s + 3, ni = n >> 3, ns = n & 7; \
        TMM(i, s, 0); \
        if constexpr (i == 0 && s == 1) { if (min4(ld) < 8u) wait4(C_LAND0, 8); CBAR(); } \
        if constexpr (i == 0 && s < 6) x[s + 2][0] = lds_rd128(xad[s + 2]); \
        if constexpr (n < 24) f[n & 3] = lds_tr2u(wadr[ni < 3 ? ni : 0][ns >> 2] + (ns & 3) * 5376); \
        if constexpr (i > 0) emit(IC(i > 0 ? i - 1 : 0), IC(2 * s)); \
        SBAR(); \
        TMM(i, s, 1); \
        if constexpr (i == 0 && s < 6) x[s + 2][1] = lds_rd128(xad[s + 2] + 4096); \
        if constexpr (ISB && i == 0 && s == 6) dn = rd4(C_DONE0); \
        if constexpr (ISB && i == 1 && s == 0) { if (min4(dn) < 5u) wait4(C_DONE0, 5); CBAR(); } \
        if constexpr (i > 0) emit(IC(i > 0 ? i - 1 : 0), IC(2 * s + 1)); \
        SBAR(); } while (0)
#define TSLICE(i) TSTEP(i, 0); TSTEP(i, 1); TSTEP(i, 2); TSTEP(i, 3); TSTEP(i, 4); TSTEP(i, 5); TSTEP(i, 6); TSTEP(i, 7)
    TSLICE(0); TSLICE(1); TSLICE(2);
    *(volatile LAS unsigned*)(uintptr_t)(L_CNT + 4 * (C_DONE0 + w)) = 7;
#define EM4(k) emit(IC(2), IC(k)); emit(IC(2), IC(k + 1)); emit(IC(2), IC(k + 2)); emit(IC(2), IC(k + 3))
    EM4(0); EM4(4); EM4(8); EM4(12);
}
DI void phi_w(bf16x8& f0, u32x4& f1, int hh, int lane, bool hi) {
    const unsigned pb = L_PHI + hh * 1568 + (4 * (lane >> 5) + ((lane & 15) >> 2)) * 56 + ((lane & 19) == 19 ? 0 : 32 * ((lane >> 4) & 1) + 8 * (lane & 3));
    f0 = lds_tr2(pb, pb + 8 * 56);
    f1 = __builtin_bit_cast(u32x4, lds_tr2(pb + 16 * 56, pb + 24 * 56));
    if (hi) { f1[2] = 0; f1[3] = 0; }
}
DI f32x16 phi_pre(u32x4 k0, u32x4 k1, bf16x8 f0, u32x4 f1) {
    f32x16 p = MFMA(f0, asbf(k0), f32x16{});
    return MFMA(asbf(f1), asbf(k1), p);
}

DI void phik_pack(const f32x16& p, u32x2 (&im)[8], float lo) {
    f32x16 e, g;
    feat(p, e, g);
    f32x2 s2 = {e[0], e[1]}, g2 = {g[0], g[1]};
#pragma unroll
    for (int i = 2; i < 12; i += 2) { s2 += (f32x2){e[i], e[i + 1]}; g2 += (f32x2){g[i], g[i + 1]}; }
    s2 = __builtin_elementwise_fma((f32x2){e[12], e[13]} + (f32x2){e[14], e[15]}, (f32x2){lo, lo}, s2);
    g2 = __builtin_elementwise_fma((f32x2){g[12], g[13]} + (f32x2){g[14], g[15]}, (f32x2){lo, lo}, g2);
    const float se = __builtin_amdgcn_rcpf(swap_sum(s2[0] + s2[1])), sg = __builtin_amdgcn_rcpf(swap_sum(g2[0] + g2[1]));
#pragma unroll
    for (int u = 0; u < 8; ++u) {
        const int k = 4 * (u & 3);
        const f32x16& z = (u >> 2) ? g : e; const float sc = (u >> 2) ? sg : se;
        const f32x2 a = (f32x2){z[k], z[k + 1]} * (f32x2){sc, sc}, b = (f32x2){z[k + 2], z[k + 3]} * (f32x2){sc, sc};
        im[u] = (u32x2){pk2(a[0], a[1]), pk2(b[0], b[1])}; PIN(im[u]);
    }
}
DI unsigned phik_base(int w, int lane) {
    const int tk = lane & 31, pi = (((tk >> 1) & 1) << 3) | (tk & 1) | (((tk >> 2) & 3) << 1);
    return L_R1 + (w >> 1) * 16384 + ((w & 1) * 64 + tk) * 128 + ((pi ^ (lane >> 5)) << 3); }

__global__ __launch_bounds__(512, 2) void fused_fwd(const void*, const void*, const void*, const void*, const void*, const void*, const void*, void*, void*) {
    STAMP_DECL; STAMP(0); STAMPC(30);
    u32x16 ka; u32x2 kb;
    asm volatile("s_load_dwordx16 %0, %2, 0x0\n\ts_load_dwordx2 %1, %2, 0x40" : "=&s"(ka), "=&s"(kb) : "s"(__builtin_amdgcn_kernarg_segment_ptr()));
    const int wid = __builtin_amdgcn_readfirstlane(threadIdx.x >> 6);
    int lane = threadIdx.x & 63;
    const int b = blockIdx.x, xcd = b & 7, r = b >> 3, w = wid & 3, role = wid >> 2;
    const int isb = r & 1, ml = (r >> 1) & 3, nl = r >> 3;
    const int mt = (xcd & 3) * 4 + ml, nt = (xcd >> 2) * 4 + nl;
    const bool hi = lane >= 32;
    const int g0 = 2 * ml;
    const int head = 2 * xcd + (r >> 4), qt = r & 15;
    const int ct = r >> 2, tto = (2 * xcd + (w & 1)) * 4 + (r & 3);
    const int sl2 = qt + (w & 1);
    u32x2 tag;
    bf16x8 wf[16][2];

    unsigned xvo, wvo[6];
    if (role) {
        __builtin_amdgcn_s_setprio(3);
        xvo = (w * 8 + (lane >> 3)) * 896 + ((((lane & 7) ^ (lane >> 4) ^ ((w & 1) << 2)) & 7) << 4);
        PIN(xvo);
#pragma unroll
        for (int i = 0; i < 6; ++i) {
            const int c = 64 * (w + 4 * i) + lane, rho = (c * 3121) >> 16, ch = c - 21 * rho, win = (ch * 37) >> 8;
            const int krow = (rho & ~15) | ((rho & 3) << 2) | ((rho >> 2) & 3);
            wvo[i] = krow * 2688 + ch * 16 + (isb ? win * (896 - 112) : 0);
            PIN(wvo[i]);
        }
    }
    if (wid == 0) {
        unsigned long long pc; unsigned len;
        asm volatile("s_getpc_b64 %0\n.Lpf_begin%=:\n\ts_mov_b32 %1, .Lpf_end-.Lpf_begin%=" : "=s"(pc), "=s"(len));
        const rsrc_t rc = __builtin_amdgcn_make_buffer_rsrc((void*)pc, 0, len, 0x00020000);
        asm volatile("s_mov_b32 m0, %0\n\ts_nop 3\n\tbuffer_load_dword %1, %2, 0 offen lds" :: "s"(L_JUNK), "v"(lane < 14 ? (r * 14 + lane) * 128 : 0x7FFFFFF0), "s"(rc) : "memory");
        *(LAS unsigned*)(uintptr_t)(L_CNT + 4 * lane) = 0u;
    }
    asm volatile("s_waitcnt lgkmcnt(0)" : "+s"(ka), "+s"(kb) :: "memory");
    const rsrc_t rws = mk_rsrc(kb[0], kb[1], WS_BYTES);
    if (role) {
        u32x4 pe[2][4];
        unsigned kva[5], wovo, wt0, wt1, rvo, rfo;
        const rsrc_t rx = mk_rsrc(isb ? ka[6] : ka[0], isb ? ka[7] : ka[1], 2048 * 896), rw = mk_rsrc(isb ? ka[8] : ka[2], isb ? ka[9] : ka[3], 448 * 2688);
        const unsigned xs0 = mt * 114688, ws0 = (isb ? 112 : 336) * nt;
        auto feed = [&](int g, auto PX, auto PW) {
            int ga = g + g0; ga -= (ga >= 7) ? 7 : 0;
            const unsigned ws = ws0 + ga * 64 * 2688, l = (g & 3) * SLOT + w * 1024;
            dma_x4<decltype(PX)::value>(rx, xvo, xs0 + ga * 128, l);
            dma_w5<decltype(PW)::value>(rw, wvo, ws, l + 16384);
            if (w == 0) dma(rw, wvo[5], ws, l + 16384 + 5 * 4096); };
        const unsigned lw = L_CNT + 4 * (C_LAND0 + w);
        constexpr std::integral_constant<int, 0> fast{};
        unsigned long long ep;
        asm volatile("s_load_dwordx2 %0, %1, 0x0" : "=s"(ep) : "s"(kb));
        FSTAMP(18);
        dma(mk_rsrc(ka[10], ka[11], 16 * 1568), lane * 16, nt * 3136 + w * 1024, L_PHI + w * 1024);
        feed(0, fast, fast);
        feed(1, fast, fast);
        asm volatile("s_barrier" ::: "memory");
        if (w == 0) WAIT_VM(10); else WAIT_VM(9);
        *(volatile LAS unsigned*)(uintptr_t)lw = 1;
        STAMP(1);
        feed(2, fast, fast);
        if (w == 0) WAIT_VM(10); else WAIT_VM(9);
        *(volatile LAS unsigned*)(uintptr_t)lw = 2;
        feed(3, fast, fast);
        if (w == 0) WAIT_VM(10); else WAIT_VM(9);
        *(volatile LAS unsigned*)(uintptr_t)lw = 3;
        asm volatile("s_waitcnt lgkmcnt(0)" : "+s"(ep) :: "memory");
        ++ep;
        tag = (u32x2){(unsigned)ep, (unsigned)(ep >> 32)};
        if (w == 0) { lds_wr64(L_TAG, tag); sig(C_TAG, lane); }
#pragma unroll 1
        for (int g = 3; g < 6; ++g) {
            wait4(C_DONE0, g - 2);
            feed(g + 1, fast, fast);
            if (w == 0) WAIT_VM(10); else WAIT_VM(9);
            *(volatile LAS unsigned*)(uintptr_t)lw = g + 1;
        }
        {
            const rsrc_t rt_ = mk_rsrc(w < 2 ? ka[4] : ka[12], w < 2 ? ka[5] : ka[13], 448 * 896);
            asm volatile("s_mov_b32 m0, %0\n\ts_nop 3\n\tbuffer_load_dword %1, %2, 0 offen lds"
                         :: "s"(L_JUNK), "v"(lane < 49 ? (r * 98 + (w & 1) * 49 + lane) * 128 : 0x7FFFFF00), "s"(rt_) : "memory");
        }
        WAIT_VM(1);
        *(volatile LAS unsigned*)(uintptr_t)lw = 8;
        __builtin_amdgcn_s_setprio(1);
        STAMP(2);
        wovo = (lane >> 3) * 896 + (((lane & 7) ^ (((lane >> 4) & 1) << 2)) << 4);
        wt0 = tr_addr128(L_R0, 0, (w >> 1) * 224 + 4 * (lane >> 5), lane); wt1 = tr_addr128(L_R0, 64, (w >> 1) * 224 + 4 * (lane >> 5), lane);
        rvo = (lane >> 4) * 32768 + (16 * (sl2 & 3) + (lane & 15)) * 16; rfo = WS_F2 + ((head * 16 + (lane & 15)) * 2 + (sl2 >> 3)) * 128;
        PIN(wovo); PIN(wt0); PIN(wt1); PIN(rvo); PIN(rfo);
        if (isb) {
            bf16x8 ph0; u32x4 ph1;
            wait4(C_LAND0, 1);
            phi_w(ph0, ph1, w >> 1, lane, hi);
            unsigned ib = phik_base(w, lane), qa = w * 4096 + lane * 16;
            float lo = hi ? 0.f : 1.f;
            {
                const int q = (lane & 15) >> 2, P = 8 * (w & 1) + 4 * ((lane >> 4) & 1) + (lane & 3);
#pragma unroll
                for (int i = 0; i < 4; ++i) {
                    const int t = 8 * i + 4 * (lane >> 5) + q, pt = (((t >> 1) & 1) << 3) | (t & 1) | (((t >> 2) & 3) << 1);
                    kva[i] = L_R1 + (w >> 1) * 16384 + t * 128 + ((P ^ pt) << 3); PIN(kva[i]);
                }
                kva[4] = L_R0 + (w >> 1) * 8192 + lane * 16; PIN(kva[4]);
            }
            PIN(ib); PIN(qa); PIN(lo); PIN(ph0); PIN(ph1);
            waitc<1>(C_QB + w, 1);
            f32x16 pp[2];
#pragma unroll
            for (int t = 0; t < 2; ++t) pp[t] = phi_pre(lds_rd128(L_KB + qa + t * 2048), lds_rd128(L_KB + qa + t * 2048 + 1024), ph0, ph1);
            u32x2 im[2][8];
            phik_pack(pp[0], im[0], lo); phik_pack(pp[1], im[1], lo);
            waitc<0>(C_QB + w, 3);
#pragma unroll
            for (int t = 0; t < 2; ++t) pp[t] = phi_pre(lds_rd128(L_QB + qa + t * 2048), lds_rd128(L_QB + qa + t * 2048 + 1024), ph0, ph1);
            wait4(C_DONE0, 7);
#pragma unroll
            for (int t = 0; t < 2; ++t)
#pragma unroll
                for (int u = 0; u < 8; ++u) lds_wr64((ib ^ ((8 * (u >> 2) + 2 * (u & 3)) << 3)) + t * 4096, im[t][u]);
            sig(C_PV, lane);
#pragma unroll
            for (int t = 0; t < 2; ++t) {
                f32x16 e, g;
                feat(pp[t], e, g);
                pe[t][0] = pk8(e, 0); pe[t][1] = pk8(e, 1); pe[t][2] = pk8(g, 0); pe[t][3] = pk8(g, 1);
            }
        }
        STAMP(3);

        if (isb) {
            waitc<0>(C_PV, 4);
            const int hh = w >> 1, ft = w & 1;
            bf16x8 fa[8], fb[8];
#pragma unroll
            for (int st = 0; st < 8; ++st) {
                fa[st] = lds_tr2(kva[2 * (st & 1)] + (st >> 1) * 4096, kva[2 * (st & 1) + 1] + (st >> 1) * 4096);
                fb[st] = asbf(lds_rd128(kva[4] + st * 1024));
            }
            SBAR();
            f32x16 kv = {};
#pragma unroll
            for (int st = 0; st < 8; ++st) kv = MFMA(fa[st], fb[st], kv);
            WAIT_LGKM0(); sig(C_KVDONE, lane);
            const int hk = 2 * nt + hh;
#pragma unroll
            for (int u = 0; u < 4; ++u)
                st16(rws, __builtin_bit_cast(u32x4, (f32x4){kv[4 * u], kv[4 * u + 1], kv[4 * u + 2], kv[4 * u + 3]}), lane * 16 + u * 1024, WS_KVP + ((hk * 16 + mt) * 8 + ft * 4) * 1024);
            FSTAMP(19);
            post(rws, WS_F2 + ((hk * 16 + mt) * 2 + ft) * 128, tag, lane);
            waitc<0>(C_KVDONE, 4);
        }
        STAMP(5);
        waitc<HSLP>(C_QX, 4);
        {
            const int tq = qt * 4 + w;
            const u32x4 sv = {~0u, ~0u, ~0u, ~0u};
            st16(rws, sv, lane * 16, WS_O + (tq * 16 + head) * 2048); st16(rws, sv, lane * 16 + 1024, WS_O + (tq * 16 + head) * 2048);
            post(rws, WS_FP + (tq * 16 + head) * 128, tag, lane);
        }
        if (r < 28) {
            if (w == 0) *(LAS u32x2*)(uintptr_t)(L_R0 + 57344 + 8 * lane) = (u32x2){0u, 0u};
            const rsrc_t rwob = mk_rsrc(ka[12], ka[13], 448 * 896);
#pragma unroll
            for (int i = 0; i < 14; ++i) { WAIT_VM(WOWIN); dma(rwob, wovo, ct * 128 + (w + 4 * i) * 8 * 896, L_R0 + (w + 4 * i) * 1024); }
            WAIT_VM(0); WAIT_LGKM0(); sig(C_WO0 + 1, lane);
        }
        if (!isb && w >= 2) {
            const int hr = head, i = lane & 15;
            poll<HSLP>(rws, rfo, tag);
            u32x4 pl[8];
#pragma unroll
            for (int p = 0; p < 4; ++p) {
                const unsigned so = WS_KVP + hr * 131072 + (sl2 >> 2) * 2048 + p * 8192;
                pl[2 * p] = ld16(rws, rvo, so); pl[2 * p + 1] = ld16(rws, rvo + 1024, so);
            }
            SBAR();
            f32x4 s0 = {}, s1 = {};
#pragma unroll
            for (int p = 0; p < 4; ++p) { s0 += __builtin_bit_cast(f32x4, pl[2 * p]); s1 += __builtin_bit_cast(f32x4, pl[2 * p + 1]); }
            float v[8] = {s0[0], s0[1], s0[2], s0[3], s1[0], s1[1], s1[2], s1[3]};
#pragma unroll
            for (int k = 0; k < 8; ++k) {
                auto t = __builtin_amdgcn_permlane16_swap(__float_as_uint(v[k]), __float_as_uint(v[k]), false, false);
                v[k] = swap_sum(__uint_as_float(t[0]) + __uint_as_float(t[1]));
            }
            u32x4 o = {pk2(v[0], v[1]), pk2(v[2], v[3]), pk2(v[4], v[5]), pk2(v[6], v[7])};
            const int cl = 16 * (sl2 & 3) + i;
            if ((cl & 31) == 29) o = (u32x4){0x3F803F80u, 0x3F803F80u, 0x3F803F80u, 0x3F803F80u};
            if (((sl2 >> 2) & 1) && cl >= 32) { o[2] = 0u; o[3] = 0u; }
            st16(rws, o, lane < 16 ? i * 16 : 0x7FFFFF00u, WS_KVR + (hr * 256 + sl2 * 16) * 16);
            post(rws, WS_F3 + (hr * 16 + sl2) * 128, tag, lane);
        }
        STAMP(6);
        if (r < 28) {
            waitc<4>(C_WO0 + 1, 4);
            read_wo(wf, wt0, wt1, 0);
            WAIT_LGKM0(); sig(C_WOFREE, lane);
        }
        if (r < 28) {
            const rsrc_t rwoa = mk_rsrc(ka[4], ka[5], 448 * 896);
            waitc<HSLP>(C_WOFREE, 4);
#pragma unroll
            for (int i = 0; i < 14; ++i) { WAIT_VM(WOWIN); dma(rwoa, wovo, ct * 128 + (w + 4 * i) * 8 * 896, L_R0 + (w + 4 * i) * 1024); }
            WAIT_VM(0); sig(C_WO0, lane);
        }
        if (isb) {
            const int hb_ = 2 * nt + (w >> 1), tb = mt * 4 + 2 * (w & 1);
            poll<HSLP>(rws, WS_F3 + (hb_ * 16 + (lane & 15)) * 128, tag);
            u32x4 kf[4];
#pragma unroll
            for (int f = 0; f < 4; ++f) kf[f] = ld16(rws, lane * 16 + f * 1024, WS_KVR + hb_ * 4096);
            SBAR();
#pragma unroll
            for (int t = 0; t < 2; ++t) {
                f32x16 np = MFMA(asbf(kf[0]), asbf(pe[t][0]), f32x16{}); np = MFMA(asbf(kf[1]), asbf(pe[t][1]), np);
                f32x16 nm = MFMA(asbf(kf[2]), asbf(pe[t][2]), f32x16{}); nm = MFMA(asbf(kf[3]), asbf(pe[t][3]), nm);
                const float rp = __builtin_amdgcn_rcpf(np[13]), rm = __builtin_amdgcn_rcpf(nm[13]);
                const float rd = __builtin_amdgcn_rcpf(__builtin_fmaf(rp, np[12], rm * nm[12]) + 1e-6f);
                const float cp = from_hi(rp * rd), cm = from_hi(rm * rd);
                f32x16 o;
#pragma unroll
                for (int i = 0; i < 16; ++i) o[i] = __builtin_fmaf(cp, np[i], cm * nm[i]);
                u32x4 o0 = pk8(o, 0), o1 = pk8(o, 1);
                if (hi) { o1[2] = 0; o1[3] = 0; }
                const unsigned so = WS_O + (2u << 20) + (((tb + t) * 16 + hb_) * 2) * 1024;
                st16(rws, o0, lane * 16, so); st16(rws, o1, lane * 16 + 1024, so);
            }
            WAIT_VM(0);
            if (lane < 2) __builtin_amdgcn_raw_buffer_store_b64(tag, rws, WS_FO + (1024 + (tb + lane) * 16 + hb_) * 128, 0, 16);
            CBAR();
        }
        STAMP(7);
        if (r < 28) poll<HSLP>(rws, WS_FP + (tto * 16 + (w >> 1) * 8 + (lane & 7)) * 128, tag);
        sig(C_PF + w, lane);
        if (b == 255 && w == 0) {
#pragma unroll 1
            for (int i = 0; i < 16; ++i) poll<HSLP>(rws, WS_F1 + (i * 64 + lane) * 128, tag);
            if (lane == 0) __builtin_amdgcn_raw_buffer_store_b64(tag, rws, WS_EPOCH, 0, 16);
        }
    } else {
        f32x16 acc[6];
        const int tp = w & 1, sg = w >> 1;
        const int rr = tp * 64 + (lane & 31);
        const unsigned xa0 = rr * 128 + ((((lane >> 5) ^ (rr >> 1)) & 7) << 4);
        const unsigned lb = 16384 + (4 * ((lane & 15) >> 2) + 2 * (lane >> 5)) * 336 + 32 * ((lane >> 4) & 1) + 8 * (lane & 3);
        const unsigned wa0 = lb + 56 * (isb ? 2 + sg : 3 * sg), wb0 = lb + 56 * (isb ? sg : 3 * sg + 1), wc0 = lb + 56 * (isb ? 4 + sg : 3 * sg + 2);
        const int G = 2 * nt + sg;
        unsigned so[3];
#pragma unroll
        for (int i = 0; i < 3; ++i) { const int sl = 3 * G + i; so[i] = WS_QKV + (((sl & 15) * 64 + mt * 4 + 2 * tp) * 6 + (sl >> 4) * 2) * 1024;
            if (isb) so[i] = i == 0 ? L_KB + w * 4096 : i == 1 ? L_QB + w * 4096 : L_R0 + (sg * 4 + 2 * tp) * 2048;
            asm volatile("" : "+s"(so[i])); }
        unsigned pad[3];
#pragma unroll
        for (int i = 0; i < 3; ++i) { pad[i] = 3 * G + i < 16 ? 0u : 0x00003F80u; asm volatile("" : "+s"(pad[i])); }
        auto start = [&]() { asm volatile("s_waitcnt lgkmcnt(0)\n\ts_barrier" ::: "memory"); wait4(C_LAND0, 1); __builtin_amdgcn_s_setprio(2); STAMP(1); STAMPC(23); };
        Pre pr;
        if (isb) { gemm6<1>(xa0, wa0, wb0, wc0, w, acc, pr, start); tail1<1, true>(acc, pr, rws, so, pad, w); }
        else if (G == 10) { gemm6<1>(xa0, wa0, wb0, wc0, w, acc, pr, start); tail1<1, false>(acc, pr, rws, so, pad, w); }
        else if (G < 10) { gemm6<0>(xa0, wa0, wb0, wc0, w, acc, pr, start); tail1<0, false>(acc, pr, rws, so, pad, w); }
        else { gemm6<3>(xa0, wa0, wb0, wc0, w, acc, pr, start); tail1<3, false>(acc, pr, rws, so, pad, w); }
        lane = fresh_lane();
        STAMPC(24); STAMP(2);
        waitc<0>(C_TAG, 1);
        tag = rd_tag();
        if (!isb) post(rws, WS_F1 + (G * 32 + mt * 2 + tp) * 128, tag, lane);
        wait4(C_DONE0, 7);
        STAMP(3);
        const int tq = qt * 4 + w;
        const int gq = (head * 43) >> 7, gk = ((16 + head) * 43) >> 7, gv = ((32 + head) * 43) >> 7;
        const int rk0 = qt >> 2;
        const unsigned frk = WS_F1 + (((lane & 2) ? gv : gk) * 32 + 8 * w + (lane & 1)) * 128;
        f32x16 O[4] = {};
        u32x4 pA0 = {}, pA1 = {}, pB0, pB1;
        u32x4 kA[4], kB[4], kC[4], kD[4];
        kD[2] = (u32x4){}; kD[3] = (u32x4){};
        PIN(O[0]); PIN(O[1]); PIN(O[2]); PIN(O[3]); PIN(pA0); PIN(pA1); PIN(kD[2]); PIN(kD[3]);
        const unsigned hb = WS_QKV + head * 64 * 6144, kvb = hb + 2048;
        unsigned kb0 = kvb + (16 * w + (qt & 3)) * 6144, kb1 = kvb + (16 * w + ((qt + 1) & 3)) * 6144,
                 kb2 = kvb + (16 * w + ((qt + 2) & 3)) * 6144, kb3 = kvb + (16 * w + ((qt + 3) & 3)) * 6144, qb = hb + tq * 6144;
        unsigned vc = lane * 16 + rk0 * 24576;
        asm volatile("" : "+s"(kb0), "+s"(kb1), "+s"(kb2), "+s"(kb3), "+s"(qb), "+v"(vc));
        unsigned qxa[4];
#pragma unroll
        for (int a = 0; a < 4; ++a) { qxa[a] = L_QX + ((w + 1 + a) & 3) * 2048 + lane * 16; PIN(qxa[a]); }
        u32x2 pf0, pf1, pf2;
        poll_fast(rws, WS_F1 + (lane >= 4 ? gq * 32 + (tq >> 1) : ((lane & 2) ? gv : gk) * 32 + 8 * w + 2 * rk0 + (lane & 1)) * 128, tag, pf0, pf1, pf2);
        STAMP(5);
#define LD(d, f, v, sb) d[f] = ld16(rws, (v) + (f) * 1024, sb)
        u32x4 q0 = ld16(rws, lane * 16, qb), q1 = ld16(rws, lane * 16 + 1024, qb);
        LD(kA, 0, vc, kb0); LD(kA, 1, vc, kb0);
        SBAR();
        bf16x8 Q[4][2];
        {
#pragma unroll
            for (int k = 0; k < 4; ++k) {
                q0[k] = pk2(__uint_as_float(q0[k] << 16) * QSCALE, __uint_as_float(q0[k] & 0xFFFF0000u) * QSCALE);
                q1[k] = pk2(__uint_as_float(q1[k] << 16) * QSCALE, __uint_as_float(q1[k] & 0xFFFF0000u) * QSCALE);
            }
            f32x16 s = MFMA(asbf(kA[0]), asbf(q0), f32x16{}); s = MFMA(asbf(kA[1]), asbf(q1), s);
            SBAR(); FSTAMP(13);
            asm volatile("" :: "v"(pf0), "v"(pf1), "v"(pf2));
            LD(kA, 2, vc, kb0); LD(kA, 3, vc, kb0); LD(kB, 0, vc, kb1); LD(kB, 1, vc, kb1); LD(kB, 2, vc, kb1); LD(kB, 3, vc, kb1);
            LD(kC, 0, vc, kb2); LD(kC, 1, vc, kb2); LD(kC, 2, vc, kb2); LD(kC, 3, vc, kb2);
            SBAR();
            lds_wr128(qxa[3], q0);
            const float m = swap_max(max16(s)) + REF_SHIFT;
            u32x4 q1m = q1;
            if (hi) q1m[2] = pk2(-m, 0.f);
            lds_wr128(qxa[3] + 1024, q1m);
            sig(C_QX, lane); FSTAMP(14);
            u32x4 qr[3][2];
            gather(C_QX, 4, [&]() {
#pragma unroll
                for (int a = 0; a < 3; ++a) { qr[a][0] = lds_rd128(qxa[a]); qr[a][1] = lds_rd128(qxa[a] + 1024); } });
#pragma unroll
            for (int a = 0; a < 3; ++a) { Q[a][0] = asbf(qr[a][0]); Q[a][1] = asbf(qr[a][1]); }
            Q[3][0] = asbf(q0); Q[3][1] = asbf(q1m);
        }
        u32x2 fl = ldflag(rws, frk + ((rk0 + 1) & 3) * 256);
        STAMP(6); STAMPC(20);
        __builtin_amdgcn_s_setprio(0);
        f32x16 sA, sB;
        sA = MFMA(asbf(kA[0]), Q[0][0], f32x16{}); sA = MFMA(asbf(kA[1]), Q[0][1], sA);
        PIN(Q[1][0]); PIN(Q[1][1]); PIN(Q[2][0]); PIN(Q[2][1]); PIN(Q[3][0]); PIN(Q[3][1]);
#define EX4(S, i) S[i] = __builtin_amdgcn_exp2f(S[i]); S[i + 1] = __builtin_amdgcn_exp2f(S[i + 1]); S[i + 2] = __builtin_amdgcn_exp2f(S[i + 2]); S[i + 3] = __builtin_amdgcn_exp2f(S[i + 3])
#define STAGE(SC, SN, PC0, PC1, PP0, PP1, KN, an, VP, ap, F0, F1, F2, F3) do { \
        SN = MFMA(asbf(KN[0]), Q[an][0], f32x16{}); SBAR(); F0; SBAR(); EX4(SC, 0); PIN(SC); SBAR(); \
        SN = MFMA(asbf(KN[1]), Q[an][1], SN); SBAR(); F1; SBAR(); EX4(SC, 4); PC0[0] = pk2(SC[0], SC[1]); PC0[1] = pk2(SC[2], SC[3]); PIN(SC); PIN(PC0); SBAR(); \
        O[ap] = MFMA(asbf(VP[2]), asbf(PP0), O[ap]); SBAR(); F2; SBAR(); EX4(SC, 8); PC0[2] = pk2(SC[4], SC[5]); PC0[3] = pk2(SC[6], SC[7]); PIN(SC); PIN(PC0); SBAR(); \
        O[ap] = MFMA(asbf(VP[3]), asbf(PP1), O[ap]); SBAR(); F3; SBAR(); EX4(SC, 12); PC1[0] = pk2(SC[8], SC[9]); PC1[1] = pk2(SC[10], SC[11]); \
        PC1[2] = pk2(SC[12], SC[13]); PC1[3] = pk2(SC[14], SC[15]); PIN(PC1); SBAR(); } while (0)
#define NOP (void)0
#define ARRIVE(K) asm volatile("" : "+v"(K[0]), "+v"(K[1]), "+v"(K[2]), "+v"(K[3]))
#define TILE(KP, KC, KN, V, SB, G0) do { \
        STAGE(sA, sB, pB0, pB1, pA0, pA1, KC, 1, KP, 3, G0, NOP, NOP, NOP); \
        STAGE(sB, sA, pA0, pA1, pB0, pB1, KC, 2, KC, 0, LD(KP, 0, V, SB), LD(KP, 1, V, SB), LD(KP, 2, V, SB), LD(KP, 3, V, SB)); \
        STAGE(sA, sB, pB0, pB1, pA0, pA1, KC, 3, KC, 1, PIN(O[3]), NOP, NOP, ARRIVE(KN)); \
        STAGE(sB, sA, pA0, pA1, pB0, pB1, KN, 0, KC, 2, NOP, NOP, NOP, NOP); } while (0)
#define TILN(KP, KC, KN) do { \
        STAGE(sA, sB, pB0, pB1, pA0, pA1, KC, 1, KP, 3, NOP, NOP, NOP, NOP); \
        STAGE(sB, sA, pA0, pA1, pB0, pB1, KC, 2, KC, 0, NOP, NOP, NOP, NOP); \
        STAGE(sA, sB, pB0, pB1, pA0, pA1, KC, 3, KC, 1, PIN(O[3]), NOP, NOP, ARRIVE(KN)); \
        STAGE(sB, sA, pA0, pA1, pB0, pB1, KN, 0, KC, 2, NOP, NOP, NOP, NOP); } while (0)
#pragma unroll 1
        for (int it = 0; it < 3; ++it) {
            const unsigned vn = lane * 16 + ((rk0 + it + 1) & 3) * 24576;
            const unsigned fn = frk + ((rk0 + it + 1) & 3) * 256, f2 = frk + ((rk0 + it + 2) & 3) * 256;
            TILE(kD, kA, kB, vc, kb3, NOP);
            if (!tagok(fl, tag)) poll<1>(rws, fn, tag);
            CBAR();
            TILE(kA, kB, kC, vn, kb0, NOP);
            TILE(kB, kC, kD, vn, kb1, NOP);
            TILE(kC, kD, kA, vn, kb2, fl = ldflag(rws, f2));
            vc = vn;
        }
        unsigned cw[3];
#pragma unroll
        for (int a = 0; a < 3; ++a) { cw[a] = L_R2 + (((w + 1 + a) & 3) * 3 + 2 - a) * 4096 + lane * 16; PIN(cw[a]); }
#define SND(a, u) lds_wrf4(cw[a] + (u) * 1024, (f32x4){O[a][4 * (u)], O[a][4 * (u) + 1], O[a][4 * (u) + 2], O[a][4 * (u) + 3]})
        unsigned pv;
        TILE(kD, kA, kB, vc, kb3, pv = rd_cnt(C_PV));
        if (isb && (unsigned)__builtin_amdgcn_readfirstlane(pv) < 4u) waitc<0>(C_PV, 4);
        CBAR();
        TILN(kA, kB, kC);
        TILN(kB, kC, kD);
        STAGE(sA, sB, pB0, pB1, pA0, pA1, kD, 1, kC, 3, NOP, NOP, NOP, NOP);
        STAGE(sB, sA, pA0, pA1, pB0, pB1, kD, 2, kD, 0, NOP, NOP, NOP, NOP);
        STAGE(sA, sB, pB0, pB1, pA0, pA1, kD, 3, kD, 1, PIN(O[3]), SND(0, 0), SND(0, 1), SND(0, 2));
        O[2] = MFMA(asbf(kD[2]), asbf(pB0), O[2]); SBAR(); SND(0, 3); SBAR(); EX4(sB, 0); EX4(sB, 4); PIN(sB); SBAR();
        O[2] = MFMA(asbf(kD[3]), asbf(pB1), O[2]); SBAR(); SND(1, 0); SBAR();
        pA0 = pk8(sB, 0); PIN(pA0); SBAR(); SND(1, 1); SBAR(); EX4(sB, 8); PIN(sB); SBAR();
        O[3] = MFMA(asbf(kD[2]), asbf(pA0), O[3]); SBAR(); SND(1, 2); SBAR(); EX4(sB, 12); PIN(sB); SBAR(); SND(1, 3); SBAR();
        pA1 = pk8(sB, 1); PIN(pA1); SBAR(); SND(2, 0); SBAR();
        O[3] = MFMA(asbf(kD[3]), asbf(pA1), O[3]); SBAR();
        SND(2, 1); SND(2, 2); SND(2, 3);
        sig(C_CMB, 0);
        PIN(O[3]);
        STAMP(7); STAMPC(21);
        lane = fresh_lane();
        const unsigned pfv = rd_cnt(C_PF + w);
        f32x16 o;
        {
            f32x4 pr[12];
            unsigned cr = L_R2 + w * 12288 + lane * 16; PIN(cr);
            gather(C_CMB, 4, [&]() {
#pragma unroll
                for (int k = 0; k < 12; ++k) pr[k] = lds_rdf4(cr + k * 1024); });
#pragma unroll
            for (int u = 0; u < 4; ++u) {
                const f32x4 t = (pr[u] + (f32x4){O[3][4 * u], O[3][4 * u + 1], O[3][4 * u + 2], O[3][4 * u + 3]}) + (pr[4 + u] + pr[8 + u]);
                o[4 * u] = t[0]; o[4 * u + 1] = t[1]; o[4 * u + 2] = t[2]; o[4 * u + 3] = t[3];
            }
        }
        float l = from_hi(o[12]);
        {
            f32x2 chk = {0.f, 0.f};
#pragma unroll
            for (int i = 0; i < 16; i += 2) chk = __builtin_elementwise_fma((f32x2){o[i], o[i + 1]}, (f32x2){0.f, 0.f}, chk);
            bool bad = !(chk[0] + chk[1] == 0.f) || !(l >= 1e-32f && l <= 1e30f);
            if (__any(bad)) {
                float mrun = -3.0e38f;
                o = f32x16{};
#pragma unroll 1
                for (int k2 = 0; k2 < 4; ++k2) {
                    poll(rws, WS_F1 + (((lane & 8) ? gv : gk) * 32 + 8 * k2 + (lane & 7)) * 128, tag);
#pragma unroll 1
                    for (int m2 = 0; m2 < 16; ++m2) {
                        const unsigned so = kvb + (k2 * 16 + m2) * 6144;
                        const u32x4 k0 = ld16(rws, lane * 16, so), k1 = ld16(rws, lane * 16 + 1024, so), v0 = ld16(rws, lane * 16 + 2048, so), v1 = ld16(rws, lane * 16 + 3072, so);
                        f32x16 s = MFMA(asbf(k0), asbf(q0), f32x16{}); s = MFMA(asbf(k1), asbf(q1), s);
                        const float mn = __builtin_fmaxf(mrun, swap_max(max16(s))), al = __builtin_amdgcn_exp2f(mrun - mn);
                        mrun = mn;
#pragma unroll
                        for (int i = 0; i < 16; ++i) { s[i] = __builtin_amdgcn_exp2f(s[i] - mn); o[i] *= al; }
                        o = MFMA(asbf(v0), asbf(pk8(s, 0)), o); o = MFMA(asbf(v1), asbf(pk8(s, 1)), o);
                    }
                }
                l = from_hi(o[12]);
            }
        }
        {
            const float rl = __builtin_amdgcn_rcpf(l);
#pragma unroll
            for (int i = 0; i < 16; ++i) o[i] *= rl;
            u32x4 o0 = pk8(o, 0), o1 = pk8(o, 1);
            if (hi) { o1[2] = 0; o1[3] = 0; }
#pragma unroll
            for (int i = 0; i < 4; ++i) { o0[i] = min(o0[i], 0xFFFFFFFEu); o1[i] = min(o1[i], 0xFFFFFFFEu); }
            if ((unsigned)__builtin_amdgcn_readfirstlane(pfv) < 1u) waitc<0>(C_PF + w, 1);
            CBAR();
            const unsigned oo = WS_O + (tq * 16 + head) * 2048;
            st16(rws, o0, lane * 16, oo); st16(rws, o1, lane * 16 + 1024, oo);
            if (lane == 0) __builtin_amdgcn_raw_buffer_store_b64(tag, rws, WS_FO + (tq * 16 + head) * 128, 0, 16);
            CBAR();
        }
        sig(C_ATT, lane);
        STAMP(8); FSTAMPC(27);
        if (r < 28) { waitc<0>(C_WO0, 4); read_wo(wf, tr_addr128(L_R0, 0, (w >> 1) * 224 + 4 * (lane >> 5), lane), tr_addr128(L_R0, 64, (w >> 1) * 224 + 4 * (lane >> 5), lane), ct); }
        FSTAMP(9);
    }

    lane = fresh_lane();
    if (r < 28) {
        const int br = role, tp = w & 1, kh = w >> 1;
        const rsrc_t rout = mk_rsrc(ka[14], ka[15], 2048 * 1792);
        const unsigned ffa = WS_FO + (tto * 16 + kh * 8 + ((lane + ct) & 7)) * 128, ffo = ffa + 1024 * 128;
        const unsigned ob = WS_O + br * (2u << 20) + (tto * 16 + kh * 8) * 2048;
        unsigned xw = L_R1 + br * 16384 + w * 4096 + lane * 16, xr = L_R1 + br * 16384 + (w ^ 2) * 4096 + lane * 16, xc = L_CNT + 4 * (C_EX0 + br * 2 + tp);
        unsigned vo = (tto * 32 + (lane & 31)) * 1792 + (hi ? 16 : 0);
        const unsigned so = br * 896 + ct * 128 + kh * 64;
        PIN(xw); PIN(xr); PIN(xc); PIN(vo);
        u32x4 of[16];
        f32x16 c0, c1;
        if (role) {
            c0 = f32x16{}; c1 = f32x16{};
            poll<HSLP>(rws, ffo, tag);
#pragma unroll
            for (int k = 0; k < 16; ++k) {
                if (k == 8) WAIT_VM(GWIN);
                of[k] = ld16(rws, lane * 16 + (k & 3) * 1024, ob + (k >> 2) * 4096); SBAR();
            }
            waitc<4>(C_ATT, 4);
            __builtin_amdgcn_s_setprio(0);
#pragma unroll
            for (int k = 0; k < 16; ++k) { c0 = MFMA(wf[k][0], asbf(of[k]), c0); c1 = MFMA(wf[k][1], asbf(of[k]), c1); }
        } else {
            unsigned sb[8], l16 = lane * 16;
#pragma unroll
            for (int k = 0; k < 8; ++k) { sb[k] = ob + ((k + ct) & 7) * 2048; asm volatile("" : "+s"(sb[k])); }
            PIN(l16);
            u32x2 pf0, pf1, pf2;
            poll_fast(rws, ffa, tag, pf0, pf1, pf2);
            FSTAMP(11);
#pragma clang loop unroll(disable)
            for (unsigned it = 0; it < SPIN_CAP; ++it) {
                CBAR();
#pragma unroll
                for (int k = 0; k < 16; ++k) { of[k] = ld16(rws, l16 + (k & 1) * 1024, sb[k >> 1]); SBAR(); }
                unsigned m = 0;
                c0 = MFMA(wf[0][0], asbf(of[0]), f32x16{}); c1 = MFMA(wf[0][1], asbf(of[0]), f32x16{});
                asm volatile("" :: "v"(pf0), "v"(pf1), "v"(pf2));
#pragma unroll
                for (int k = 0; k < 16; ++k) {
                    if (k) { c0 = MFMA(wf[k][0], asbf(of[k]), c0); SBAR(); }
                    m = max(max(m, of[k][0]), of[k][1]); PIN(m); SBAR();
                    if (k) { c1 = MFMA(wf[k][1], asbf(of[k]), c1); SBAR(); }
                    m = max(max(m, of[k][2]), of[k][3]); PIN(m); SBAR();
                }
                if (__builtin_amdgcn_ballot_w64(m == ~0u) == 0) break;
            }
            sig(C_GATH, lane);
        }
        STAMP(10);
#define FINISH(KEEP, GIVE) do { \
            _Pragma("unroll") for (int u = 0; u < 4; ++u) lds_wrf4(xw + u * 1024, (f32x4){GIVE[4 * u], GIVE[4 * u + 1], GIVE[4 * u + 2], GIVE[4 * u + 3]}); \
            sigv(xc); \
            f32x4 s[4]; \
            gatherv(xc, 2, [&]() { _Pragma("unroll") for (int u = 0; u < 4; ++u) s[u] = lds_rdf4(xr + u * 1024); }); \
            _Pragma("unroll") for (int u = 0; u < 4; ++u) _Pragma("unroll") for (int i = 0; i < 4; ++i) KEEP[4 * u + i] += s[u][i]; \
            if (role) waitc<1>(C_GATH, 4); \
            _Pragma("unroll") for (int gp = 0; gp < 2; ++gp) { \
                auto s0 = __builtin_amdgcn_permlane32_swap(pk2(KEEP[8 * gp], KEEP[8 * gp + 1]), pk2(KEEP[8 * gp + 4], KEEP[8 * gp + 5]), false, false); \
                auto s1 = __builtin_amdgcn_permlane32_swap(pk2(KEEP[8 * gp + 2], KEEP[8 * gp + 3]), pk2(KEEP[8 * gp + 6], KEEP[8 * gp + 7]), false, false); \
                st16a<OUTAUX>(rout, (u32x4){s0[0], s1[0], s0[1], s1[1]}, vo + gp * 32, so); } } while (0)
        if (kh) FINISH(c1, c0); else FINISH(c0, c1);
        STAMP(12);
    }
    STAMP_FLUSH();
    asm volatile(".Lpf_end:");
}

extern "C" void kernel_launch(void* const* d_in, const int* in_sizes, int n_in, void* d_out, int out_size, void* d_ws, size_t ws_size,
                              hipStream_t stream) {
    (void)in_sizes; (void)n_in; (void)out_size; (void)ws_size;
    (void)hipFuncSetAttribute((const void*)fused_fwd, hipFuncAttributeMaxDynamicSharedMemorySize, LDS_BYTES);
    fused_fwd<<<256, 512, LDS_BYTES, stream>>>(d_in[0], d_in[1], d_in[2], d_in[3], d_in[4], d_in[5], d_in[6], d_out, d_ws);
}
```
